```python
import torch

def _softmax(x, axis=-1):
    return torch.softmax(x, dim=axis)
def _gelu(x):
    return torch.nn.functional.gelu(x, approximate="tanh")
def _silu(x):
    return torch.nn.functional.silu(x)
def _sigmoid(x):
    return torch.sigmoid(x)
def _layernorm(x, axis=-1, eps=1e-5):
    m = x.mean(dim=axis, keepdim=True)
    v = x.var(dim=axis, keepdim=True, unbiased=False)
    return (x - m) / torch.sqrt(v + eps)
def _scatter_add(tgt, idx, src, axis=0):
    return tgt.clone().index_add_(axis, idx, src)
def _segment_sum(src, seg, n):
    out = torch.zeros((n,) + src.shape[1:], dtype=src.dtype)
    return out.index_add_(0, seg, src)
def _pad(x, pw):
    flat = tuple(v for pair in reversed(pw) for v in pair)
    return torch.nn.functional.pad(x, flat)

kernel_name = 'parallel_sum__sliding_local_dilated__dense_activation_silu__prompt'

def setup_inputs():

    _w = (lambda a: (lambda t: t.to(torch.bfloat16) if t.is_floating_point() else t)(torch.as_tensor(a)))
    def _A_setup():
        B, S, D = 1, 2048, 512
        return {
            "x": _w((lambda *s: torch.randn(*s, dtype=torch.bfloat16) * 0.05)(B, S, D)),
            "wqkv": _w((lambda *s: torch.randn(*s, dtype=torch.bfloat16) * 0.05)(D, 3 * D)),
            "wo": _w((lambda *s: torch.randn(*s, dtype=torch.bfloat16) * 0.05)(D, D)),
        }
    _A_in = _A_setup()

    def _B_setup():
        B, S, D, F = 1, 2048, 512, 1792
        return {
            "x": _w((lambda *s: torch.randn(*s, dtype=torch.bfloat16) * 0.05)(B, S, D)),
            "w1": _w((lambda *s: torch.randn(*s, dtype=torch.bfloat16) * 0.8)(D, F) * (D ** -0.5)),
            "w2": _w((lambda *s: torch.randn(*s, dtype=torch.bfloat16) * 0.8)(F, D) * (F ** -0.5)),
        }
    _B_in = _B_setup()

    return {
        "A_x": _A_in["x"],
        "A_wqkv": _A_in["wqkv"],
        "A_wo": _A_in["wo"],
        "B_x": _B_in["x"],
        "B_w1": _B_in["w1"],
        "B_w2": _B_in["w2"],
    }

def reference(A_x, A_wqkv, A_wo, B_x, B_w1, B_w2):
    def _A_ref(x, wqkv, wo):
        B, S, D = x.shape
        H = 32; Dh = D // H
        W, dil = 8, 2
        qkv = (lambda x, *d: x.permute(*d))((x @ wqkv).reshape(B, S, 3, H, Dh), 2, 0, 3, 1, 4)
        q, k, v = qkv[0], qkv[1], qkv[2]
        scale = Dh ** -0.5
        pos = torch.arange(S)
        off = torch.arange(W) * dil
        idx = pos[:, None] - off[None, :]
        mask = idx >= 0
        idx_c = torch.where(mask, idx, 0).reshape(-1)
        kg = k[:, :, idx_c, :].reshape(B, H, S, W, Dh)
        vg = v[:, :, idx_c, :].reshape(B, H, S, W, Dh)
        logits = torch.einsum("bhsd,bhswd->bhsw", q, kg) * scale
        logits = torch.where(mask[None, None, :, :], logits, -1e9)
        attn = _softmax(logits, axis=-1)
        o = torch.einsum("bhsw,bhswd->bhsd", attn, vg)
        return (lambda x, *d: x.permute(*d))(o, 0, 2, 1, 3).reshape(B, S, D) @ wo

    def _B_ref(x, w1, w2):
        h = torch.einsum("bsd,df->bsf", x, w1)
        h = _silu(h)
        out = torch.einsum("bsf,fd->bsd", h, w2)
        return out

    x = A_x
    xn = _layernorm(x, axis=-1)
    return x + _A_ref(xn, A_wqkv, A_wo) + _B_ref(xn, B_w1, B_w2)
```

```cpp
#include <hip/hip_runtime.h>
#include <hip/hip_bf16.h>
typedef __hip_bfloat16 bf16;
static constexpr int S=2048, D=512, F=1792, H=32, DH=16;
__device__ inline float b2f(bf16 v){return __bfloat162float(v);}
__device__ inline bf16 f2b(float v){return __float2bfloat16(v);}
__global__ void ln_k(const bf16* x, bf16* xn){
  int r=blockIdx.x; int t=threadIdx.x;
  float v[8]; float s=0;
  for(int j=0;j<8;j++){v[j]=b2f(x[r*D+t*8+j]); s+=v[j];}
  for(int o=32;o>0;o>>=1) s+=__shfl_xor(s,o);
  float m=s/D; float q=0;
  for(int j=0;j<8;j++){float d=v[j]-m; q+=d*d;}
  for(int o=32;o>0;o>>=1) q+=__shfl_xor(q,o);
  float rs=rsqrtf(q/D+1e-5f);
  for(int j=0;j<8;j++) xn[r*D+t*8+j]=f2b((v[j]-m)*rs);
}
__global__ void gemm_k(const bf16* A,int lda,const bf16* B,int N,int K, bf16* C,int ldc,float* Cf,int mode){
  __shared__ float As[16][17], Bs[16][17];
  int tx=threadIdx.x&15, ty=threadIdx.x>>4;
  int row=blockIdx.y*16+ty, col=blockIdx.x*16+tx;
  float acc=0;
  for(int k0=0;k0<K;k0+=16){
    As[ty][tx]=b2f(A[row*lda+k0+tx]);
    Bs[ty][tx]=b2f(B[(k0+ty)*N+col]);
    __syncthreads();
    for(int k=0;k<16;k++) acc+=As[ty][k]*Bs[k][tx];
    __syncthreads();
  }
  if(mode==0) C[row*ldc+col]=f2b(acc);
  else if(mode==1){ float h=b2f(f2b(acc)); C[row*ldc+col]=f2b(h/(1.f+__expf(-h))); }
  else Cf[row*ldc+col]=acc;
}
__global__ void attn_k(const bf16* qkv, bf16* o){
  int idx=blockIdx.x*blockDim.x+threadIdx.x;
  int s=idx/H, h=idx%H;
  float q[16]; for(int d=0;d<16;d++) q[d]=b2f(qkv[s*1536+h*16+d]);
  float l[8]; float mx=-1e30f;
  for(int w=0;w<8;w++){int j=s-2*w; if(j<0){l[w]=-1e9f;} else {float a=0; for(int d=0;d<16;d++) a+=q[d]*b2f(qkv[j*1536+512+h*16+d]); l[w]=a*0.25f;} mx=fmaxf(mx,l[w]);}
  float sum=0; for(int w=0;w<8;w++){l[w]=__expf(l[w]-mx); sum+=l[w];}
  float acc[16]; for(int d=0;d<16;d++) acc[d]=0;
  for(int w=0;w<8;w++){int j=s-2*w; if(j<0) j=0; float p=l[w]/sum; for(int d=0;d<16;d++) acc[d]+=p*b2f(qkv[j*1536+1024+h*16+d]);}
  for(int d=0;d<16;d++) o[s*512+h*16+d]=f2b(acc[d]);
}
__global__ void fin_k(const bf16* x,const float* a,const float* b, bf16* out){
  int i=blockIdx.x*blockDim.x+threadIdx.x;
  out[i]=f2b(b2f(x[i])+a[i]+b[i]);
}
extern "C" void kernel_launch(void* const* d_in, const int* in_sizes, int n_in, void* d_out, int out_size, void* d_ws, size_t ws_size, hipStream_t stream){
  const bf16* x=(const bf16*)d_in[0]; const bf16* wqkv=(const bf16*)d_in[1]; const bf16* wo=(const bf16*)d_in[2];
  const bf16* w1=(const bf16*)d_in[4]; const bf16* w2=(const bf16*)d_in[5];
  char* ws=(char*)d_ws;
  bf16* xn=(bf16*)ws; bf16* qkv=(bf16*)(ws+(4<<20)); bf16* h=(bf16*)(ws+(12<<20)); bf16* o=(bf16*)(ws+(20<<20));
  float* fa=(float*)(ws+(24<<20)); float* fb=(float*)(ws+(32<<20));
  ln_k<<<S,64,0,stream>>>(x,xn);
  gemm_k<<<dim3(1536/16,S/16),256,0,stream>>>(xn,D,wqkv,1536,D,qkv,1536,nullptr,0);
  gemm_k<<<dim3(F/16,S/16),256,0,stream>>>(xn,D,w1,F,D,h,F,nullptr,1);
  attn_k<<<S*H/256,256,0,stream>>>(qkv,o);
  gemm_k<<<dim3(D/16,S/16),256,0,stream>>>(o,D,wo,D,D,nullptr,D,fa,2);
  gemm_k<<<dim3(D/16,S/16),256,0,stream>>>(h,F,w2,D,F,nullptr,D,fb,2);
  fin_k<<<S*D/256,256,0,stream>>>(x,fa,fb,(bf16*)d_out);
}
```

```cpp
#include <hip/hip_runtime.h>
#include <hip/hip_bf16.h>

typedef unsigned long long u64;
typedef unsigned u32;
typedef __bf16 bf16x8 __attribute__((ext_vector_type(8)));
typedef __bf16 bf16x2 __attribute__((ext_vector_type(2)));
typedef short s16x4 __attribute__((ext_vector_type(4)));
typedef float f32x16 __attribute__((ext_vector_type(16)));
typedef float f32x4 __attribute__((ext_vector_type(4)));
typedef float f32x2 __attribute__((ext_vector_type(2)));
typedef u32 u32x4 __attribute__((ext_vector_type(4)));
typedef u32 u32x2 __attribute__((ext_vector_type(2)));
#define LDSP __attribute__((address_space(3)))
#define DI __device__ __forceinline__

#define STAMP 0

constexpr u32 WS_GEN = 0;
constexpr u32 WS_FLAG = 4096;
constexpr u32 WS_TOK = 16384;
constexpr u32 WS_CS = 65536;
constexpr u32 WS_H = 262144;
constexpr u32 WS_HBYTES = 64u * 4u * 36u * 1024u;
constexpr u32 WS_STAMP = 16u << 20;
constexpr u32 STAGE = 45056;
constexpr u32 L_K = 2 * STAGE;
constexpr u32 L_V = L_K + 18432;
constexpr u32 L_CS = 148480;
constexpr u32 L_CP = L_CS + 1024;
constexpr u32 LDS_BYTES = 160 * 1024;
constexpr u32 SPIN_CAP = 1u << 16;

DI void dma16(u32 voff, const void* sbase, u32 lds) {
  asm volatile("s_mov_b32 m0, %2\n\ts_nop 0\n\tglobal_load_lds_dwordx4 %0, %1" ::"v"(voff), "s"(sbase), "s"(lds) : "memory");
}
DI void st2_sc1(u32 voff, const void* sbase, u32x4 a, u32x4 b) {
  asm volatile("global_store_dwordx4 %0, %2, %1 sc1\n\tglobal_store_dwordx4 %0, %3, %1 offset:1024 sc1\n\ts_nop 1" ::"v"(voff), "s"(sbase), "v"(a), "v"(b) : "memory");
}
template <class T> DI T lds_ld(u32 a) { return *(LDSP const T*)(size_t)a; }
template <class T> DI void lds_st(u32 a, T v) { *(LDSP T*)(size_t)a = v; }
DI u32x4 tr_frag(u32 a0, u32 a1) {
  s16x4 lo = __builtin_amdgcn_ds_read_tr16_b64_v4i16((LDSP s16x4*)(size_t)a0);
  s16x4 hi = __builtin_amdgcn_ds_read_tr16_b64_v4i16((LDSP s16x4*)(size_t)a1);
  return __builtin_bit_cast(u32x4, __builtin_shufflevector(lo, hi, 0, 1, 2, 3, 4, 5, 6, 7));
}
DI f32x16 mfma(u32x4 a, u32x4 b, f32x16 c) {
  return __builtin_amdgcn_mfma_f32_32x32x16_bf16(__builtin_bit_cast(bf16x8, a), __builtin_bit_cast(bf16x8, b), c, 0, 0, 0);
}
DI u32 pk(float lo, float hi) { f32x2 f = {lo, hi}; return __builtin_bit_cast(u32, __builtin_convertvector(f, bf16x2)); }
DI float asf(u32 v) { return __builtin_bit_cast(float, v); }
DI u32 asu(float v) { return __builtin_bit_cast(u32, v); }
DI float blo(u32 v) { return __builtin_bit_cast(float, v << 16); }
DI float bhi(u32 v) { return __builtin_bit_cast(float, v & 0xffff0000u); }
DI float dot2(u32 a, u32 b, float c) {
  return __builtin_amdgcn_fdot2_f32_bf16(__builtin_bit_cast(bf16x2, a), __builtin_bit_cast(bf16x2, b), c, false);
}
template <int CTRL> DI float dpp(float v) {
  return __builtin_bit_cast(float, __builtin_amdgcn_update_dpp(0, __builtin_bit_cast(int, v), CTRL, 0xf, 0xf, true));
}
DI void swap32(u32& a, u32& b) { auto r = __builtin_amdgcn_permlane32_swap(a, b, false, false); a = r[0]; b = r[1]; }
DI void swap16(u32& a, u32& b) { auto r = __builtin_amdgcn_permlane16_swap(a, b, false, false); a = r[0]; b = r[1]; }
DI float addswap32(float a, float b) { u32 x = __builtin_bit_cast(u32, a), y = __builtin_bit_cast(u32, b); swap32(x, y); return __builtin_bit_cast(float, x) + __builtin_bit_cast(float, y); }
DI float addswap16(float a, float b) { u32 x = __builtin_bit_cast(u32, a), y = __builtin_bit_cast(u32, b); swap16(x, y); return __builtin_bit_cast(float, x) + __builtin_bit_cast(float, y); }
DI float wave_sum(float v) {
  v += dpp<0xB1>(v);
  v += dpp<0x4E>(v);
  v += dpp<0x141>(v);
  v += dpp<0x140>(v);
  v = addswap16(v, v);
  v = addswap32(v, v);
  return v;
}
#define WAITBAR(N) asm volatile("s_waitcnt vmcnt(" #N ") lgkmcnt(0)\n\ts_barrier" ::: "memory")
#define ST(i) do {} while (0)

struct Frags { u32x4 x0, x1, xe, w0, w1, w2, we; };

template <bool ATTN, int SEL, bool DMA>
DI void chunk_body(f32x16 (&acc)[7], Frags (&fr)[2], u32 slot, u32 xb, u32 xeb, u32 wb, u32 web,
                   const u32 (&voff)[11], const char* gx, const char* gw, u32 ldsA, u32 ldsB) {
  constexpr u32 SSTR = ATTN ? 2048u : 7168u;
  constexpr u32 TSTR = ATTN ? 512u : 1792u;
  constexpr u32 FSTR = ATTN ? 8192u : 64u;
  constexpr int NA = ATTN ? 5 : 4;
#pragma unroll
  for (int s = 0; s < 4; ++s) {
    Frags& n = fr[s & 1];
    Frags& c = fr[(s & 1) ^ 1];
    const u32 xa = slot + (xb ^ (u32)(s << 5));
    const u32 wa = slot + wb + s * SSTR;
    n.x0 = lds_ld<u32x4>(xa);
    n.w0 = tr_frag(wa, wa + TSTR);
    n.x1 = lds_ld<u32x4>(xa + 4096);
    n.w1 = tr_frag(wa + FSTR, wa + FSTR + TSTR);
    n.w2 = tr_frag(wa + 2 * FSTR, wa + 2 * FSTR + TSTR);
    if (ATTN) n.xe = lds_ld<u32x4>(slot + (xeb ^ (u32)(s << 5)));
    else { const u32 we = slot + web + s * SSTR; n.we = tr_frag(we, we + TSTR); }
    if (DMA) {
      constexpr int lo[5] = {0, 3, 6, 9, 11};
#pragma unroll
      for (int q = lo[s]; q < lo[s + 1]; ++q) {
        if (q < NA) dma16(voff[q], gx, ldsA + q * 1024);
        else dma16(voff[q], gw, ldsB + (q - NA) * 1024);
      }
    }
    acc[0] = mfma(c.w0, c.x0, acc[0]);
    acc[3] = mfma(c.w0, c.x1, acc[3]);
    acc[1] = mfma(c.w1, c.x0, acc[1]);
    acc[4] = mfma(c.w1, c.x1, acc[4]);
    acc[2] = mfma(c.w2, c.x0, acc[2]);
    acc[5] = mfma(c.w2, c.x1, acc[5]);
    if (ATTN) acc[6] = mfma(SEL ? c.w2 : c.w1, c.xe, acc[6]);
    else acc[6] = mfma(c.we, SEL ? c.x1 : c.x0, acc[6]);
  }
}

template <bool ATTN, int SEL>
DI void phase1(const char* __restrict__ x, const char* __restrict__ wbase, char* ws, const int p, const int g, const int rot,
               const int wv, const int lane, const u32 lds0, const u32 (&voff)[11], const u32 tag, const u64 tag64) {
  const int mh = wv >> 1, nh = wv & 1;
  const int t = lane & 31, hh = lane >> 5, q4 = (lane & 15) >> 2, p4 = lane & 3, blk = (lane >> 4) & 1;
  constexpr u32 BOFF = ATTN ? 20480u : 16384u;
  constexpr u32 KSTEP = ATTN ? 64u * 3072u : 64u * 3584u;
  constexpr int NA = ATTN ? 5 : 4, NB = ATTN ? 6 : 7;
  const u32 xsw = t * 128 + ((hh ^ ((t >> 1) & 7)) << 4);
  const u32 xb = xsw + (ATTN ? 4096u : 0u) + mh * 8192u;
  const u32 xeb = xsw;
  u32 wb, web = 0;
  if (ATTN) wb = BOFF + (8 * hh + q4) * 128 + (((nh * 4 + blk * 2 + (p4 >> 1)) ^ (((q4 >> 1) & 1) << 2)) << 4) + (p4 & 1) * 8;
  else { web = BOFF + (8 * hh + q4) * 448 + blk * 32 + p4 * 8; wb = web + nh * 192; web += 384; }

  f32x16 acc[7];
#pragma unroll
  for (int i = 0; i < 7; ++i)
#pragma unroll
    for (int j = 0; j < 16; ++j) acc[i][j] = 0.f;
  Frags fr[2];
  {
    const u32x4 z = {0u, 0u, 0u, 0u};
    fr[1].x0 = z; fr[1].x1 = z; fr[1].xe = z; fr[1].w0 = z; fr[1].w1 = z; fr[1].w2 = z; fr[1].we = z;
    fr[0] = fr[1];
  }
  const u32 wA = lds0 + wv * NA * 1024, wB = lds0 + BOFF + wv * NB * 1024;
  u32 slot = lds0, fill = 2 * STAGE;
  for (int c = 0; c < 6; ++c) {
    WAITBAR(11);
    if (c == 0) {
      ST(2);
      const u32 a = lds0 + L_CP + (wv * 16 + (lane & 15)) * 4;
      const float s = lds_ld<float>(a) + lds_ld<float>(a + 256) + lds_ld<float>(a + 512) + lds_ld<float>(a + 768);
      const u32 cso = lds_ld<u32>(lds0 + L_CP + 1024);
      if (lane < 16) {
        u32x2 gr = {__builtin_bit_cast(u32, s), tag};
        asm volatile("global_store_dwordx2 %0, %1, %2 sc1\n\ts_nop 1" ::"v"(cso + (wv * 16 + lane) * 8), "v"(gr), "s"(ws) : "memory");
      }
    }
    const int kc = (c + 2 + rot) & 7;
    chunk_body<ATTN, SEL, true>(acc, fr, slot, xb, xeb, wb, web, voff, x + kc * 128, wbase + kc * KSTEP, wA + fill, wB + fill);
    fill = slot - lds0;
    slot = (slot == lds0 + 2 * STAGE) ? lds0 : slot + STAGE;
  }
  WAITBAR(11);
  u32x4 ts0, ts1, ts2; u32x2 cg0, cg1, cg2, cg3;
  const int tid = wv * 64 + lane;
  const u32 tok0 = (u32)(p * 128 + mh * 64 + t) * 16 + WS_TOK;
  int hrow = p * 128 - 32 + t; hrow = hrow < 0 ? 0 : hrow;
  const u32 tok2 = ATTN ? (u32)hrow * 16 + WS_TOK : tok0 + SEL * 512;
  int ccol;
  if (ATTN) { const int tt = tid < 192 ? tid : 191; ccol = (tt >> 6) * 512 + g * 64 + (tt & 63); }
  else { const int tt = tid < 224 ? tid : 223; ccol = 1536 + g * 224 + tt; }
  const u32 cgo = WS_CS + ccol * 8;
#define STAT_LOADS "global_load_dwordx4 %0, %7, %10 sc1\n\tglobal_load_dwordx4 %1, %7, %10 offset:512 sc1\n\tglobal_load_dwordx4 %2, %8, %10 sc1\n\t" \
                   "global_load_dwordx2 %3, %9, %10 sc1\n\tglobal_load_dwordx2 %4, %9, %11 sc1\n\tglobal_load_dwordx2 %5, %9, %12 sc1\n\tglobal_load_dwordx2 %6, %9, %13 sc1"
#define STAT_OPS : "=&v"(ts0), "=&v"(ts1), "=&v"(ts2), "=&v"(cg0), "=&v"(cg1), "=&v"(cg2), "=&v"(cg3) \
                 : "v"(tok0), "v"(tok2), "v"(cgo), "s"(ws), "s"(ws + 3328 * 8), "s"(ws + 2 * 3328 * 8), "s"(ws + 3 * 3328 * 8) : "memory"
#define STAT_OK (ts0[0] == tag && ts0[2] == tag && ts1[0] == tag && ts1[2] == tag && ts2[0] == tag && ts2[2] == tag && \
                 cg0[1] == tag && cg1[1] == tag && cg2[1] == tag && cg3[1] == tag)
  asm volatile(STAT_LOADS STAT_OPS);
  chunk_body<ATTN, SEL, false>(acc, fr, slot, xb, xeb, wb, web, voff, x, wbase, 0, 0);
  slot = (slot == lds0 + 2 * STAGE) ? lds0 : slot + STAGE;
  asm volatile("s_waitcnt vmcnt(0)" : "+v"(ts0), "+v"(ts1), "+v"(ts2), "+v"(cg0), "+v"(cg1), "+v"(cg2), "+v"(cg3)::"memory");
  if (!__all(STAT_OK)) {
    for (u32 spins = 0; spins < SPIN_CAP; ++spins) {
      __builtin_amdgcn_s_sleep(1);
      asm volatile(STAT_LOADS "\n\ts_waitcnt vmcnt(0)" STAT_OPS);
      if (__all(STAT_OK)) break;
    }
  }
  lds_st<float>(lds0 + L_CS + tid * 4, (asf(cg0[0]) + asf(cg1[0])) +
                                           (asf(cg2[0]) + asf(cg3[0])));
  WAITBAR(0);
  chunk_body<ATTN, SEL, false>(acc, fr, slot, xb, xeb, wb, web, voff, x, wbase, 0, 0);
  {
    Frags& c = fr[1];
    acc[0] = mfma(c.w0, c.x0, acc[0]);
    acc[3] = mfma(c.w0, c.x1, acc[3]);
    acc[1] = mfma(c.w1, c.x0, acc[1]);
    acc[4] = mfma(c.w1, c.x1, acc[4]);
    acc[2] = mfma(c.w2, c.x0, acc[2]);
    acc[5] = mfma(c.w2, c.x1, acc[5]);
    if (ATTN) acc[6] = mfma(SEL ? c.w2 : c.w1, c.xe, acc[6]);
    else acc[6] = mfma(c.we, SEL ? c.x1 : c.x0, acc[6]);
  }
  ST(3);
  const float sa[3] = {asf(ts0[1]), asf(ts1[1]), asf(ts2[1])};
  const float sb[3] = {asf(ts0[3]), asf(ts1[3]), asf(ts2[3])};
  const int wq = g >> 1;
  auto lnfix = [&](const f32x16& a, int si, int fcol, float (&o)[16]) {
    const u32 ca = lds0 + L_CS + (fcol + 4 * hh) * 4;
#pragma unroll
    for (int qd = 0; qd < 4; ++qd) {
      const f32x4 cs = lds_ld<f32x4>(ca + qd * 32);
#pragma unroll
      for (int e = 0; e < 4; ++e) o[qd * 4 + e] = __builtin_fmaf(a[qd * 4 + e], sa[si], sb[si] * cs[e]);
    }
  };
  if (!ATTN) {
#pragma unroll
    for (int i = 0; i < 7; ++i) {
      const int tb = i < 6 ? i / 3 : SEL, fbs = i < 6 ? i % 3 : 0;
      const int fb = i < 6 ? 3 * nh + fbs : 6;
      float v[16];
      lnfix(acc[i], i < 6 ? i / 3 : 2, fb * 32, v);
      u32 P[8];
#pragma unroll
      for (int e = 0; e < 16; ++e) {
        const float ex = __builtin_amdgcn_exp2f(v[e] * -1.4426950408889634f);
        v[e] = v[e] * __builtin_amdgcn_rcpf(1.0f + ex);
      }
#pragma unroll
      for (int e = 0; e < 8; ++e) P[e] = pk(v[2 * e], v[2 * e + 1]);
      const u32 so = (u32)(((4 * p + 2 * mh + tb) * 4 + wq) * 36 + 8 + (g & 1) * 14 + 2 * fb) * 1024;
      st2_sc1(lane * 16, ws + WS_H + so, (u32x4){P[0], P[1], P[2], P[3]}, (u32x4){P[4], P[5], P[6], P[7]});
    }
  } else {
    u32 Q[2][8];
    auto putK = [&](const float (&v)[16], int r) {
      const u32 a = lds0 + L_K + ((2 * nh * 2 + hh) * 144 + r) * 16;
      lds_st<u32x4>(a, (u32x4){pk(v[0], v[1]), pk(v[2], v[3]), pk(v[4], v[5]), pk(v[6], v[7])});
      lds_st<u32x4>(a + 2 * 2304, (u32x4){pk(v[8], v[9]), pk(v[10], v[11]), pk(v[12], v[13]), pk(v[14], v[15])});
    };
    auto putV = [&](const float (&v)[16], int r) {
      const u32 a = lds0 + L_V + ((2 * nh * 4 + 2 * hh) * 144 + r) * 16;
      lds_st<f32x4>(a, (f32x4){v[0], v[1], v[2], v[3]});
      lds_st<f32x4>(a + 2304, (f32x4){v[4], v[5], v[6], v[7]});
      lds_st<f32x4>(a + 4 * 2304, (f32x4){v[8], v[9], v[10], v[11]});
      lds_st<f32x4>(a + 5 * 2304, (f32x4){v[12], v[13], v[14], v[15]});
    };
    float v[16];
#pragma unroll
    for (int tb = 0; tb < 2; ++tb) {
      const int r = 16 + 64 * mh + 32 * tb + t;
      lnfix(acc[3 * tb + 1], tb, 64 + nh * 32, v); putK(v, r);
      lnfix(acc[3 * tb + 2], tb, 128 + nh * 32, v); putV(v, r);
    }
    lnfix(acc[6], 2, (SEL ? 128 : 64) + nh * 32, v);
    if (t >= 16) { if (SEL) putV(v, t - 16); else putK(v, t - 16); }
#pragma unroll
    for (int tb = 0; tb < 2; ++tb) {
      lnfix(acc[3 * tb], tb, nh * 32, v);
#pragma unroll
      for (int e = 0; e < 8; ++e) Q[tb][e] = pk(v[2 * e], v[2 * e + 1]);
#pragma unroll
      for (int e = 0; e < 4; ++e) swap32(Q[tb][e], Q[tb][4 + e]);
    }
    asm volatile("s_waitcnt lgkmcnt(0)\n\ts_barrier" ::: "memory");
    const int hd = 2 * nh + hh;
#pragma unroll
    for (int tb = 0; tb < 2; ++tb) {
      const int r = 16 + 64 * mh + 32 * tb + t - 14;
      const u32 ka = lds0 + L_K + (hd * 2 * 144 + r) * 16, va = lds0 + L_V + (hd * 4 * 144 + r) * 16;
      float sc[8];
#pragma unroll
      for (int w = 0; w < 8; ++w) {
        const u32x4 k0 = lds_ld<u32x4>(ka + (14 - 2 * w) * 16), k1 = lds_ld<u32x4>(ka + 2304 + (14 - 2 * w) * 16);
        float s0 = dot2(Q[tb][0], k0[0], 0.f), s1 = dot2(Q[tb][4], k1[0], 0.f);
#pragma unroll
        for (int e = 1; e < 4; ++e) { s0 = dot2(Q[tb][e], k0[e], s0); s1 = dot2(Q[tb][4 + e], k1[e], s1); }
        sc[w] = s0 + s1;
      }
      if (tb == 0 && p == 0 && mh == 0) {
#pragma unroll
        for (int w = 1; w < 8; ++w) sc[w] = (t - 2 * w >= 0) ? sc[w] : -4.0e9f;
      }
      float m = fmaxf(fmaxf(fmaxf(sc[0], sc[1]), fmaxf(sc[2], sc[3])), fmaxf(fmaxf(sc[4], sc[5]), fmaxf(sc[6], sc[7])));
      constexpr float CS = 0.25f * 1.4426950408889634f;
      const float mc = -m * CS;
      float sum = 0.f;
#pragma unroll
      for (int w = 0; w < 8; ++w) { sc[w] = __builtin_amdgcn_exp2f(__builtin_fmaf(sc[w], CS, mc)); sum += sc[w]; }
      const float rs = __builtin_amdgcn_rcpf(sum);
      f32x4 o[4];
#pragma unroll
      for (int cch = 0; cch < 4; ++cch) {
        o[cch] = lds_ld<f32x4>(va + cch * 2304 + 14 * 16) * sc[0];
#pragma unroll
        for (int w = 1; w < 8; ++w) o[cch] += lds_ld<f32x4>(va + cch * 2304 + (14 - 2 * w) * 16) * sc[w];
      }
      u32 O[8];
#pragma unroll
      for (int cch = 0; cch < 4; ++cch) {
        O[2 * cch] = pk(o[cch][0] * rs, o[cch][1] * rs);
        O[2 * cch + 1] = pk(o[cch][2] * rs, o[cch][3] * rs);
      }
#pragma unroll
      for (int e = 0; e < 4; ++e) swap32(O[e], O[4 + e]);
      const u32 so = (u32)(((4 * p + 2 * mh + tb) * 4 + wq) * 36 + (g & 1) * 4 + 2 * nh) * 1024;
      st2_sc1(lane * 16, ws + WS_H + so, (u32x4){O[0], O[1], O[2], O[3]}, (u32x4){O[4], O[5], O[6], O[7]});
    }
  }
  ST(4);
  asm volatile("s_waitcnt vmcnt(0)" ::: "memory");
  if (lane == 0) {
    const u32 fo = WS_FLAG + (u32)((((p * 2 + mh) * 4 + wq) * 8) + ((ATTN ? 0 : 2) + (g & 1)) * 2 + nh) * 8;
    asm volatile("global_store_dwordx2 %0, %1, %2 sc1\n\ts_nop 1" ::"v"(fo), "v"(tag64), "s"(ws) : "memory");
  }
  ST(5);
}

__global__ __launch_bounds__(256, 1) void fused_kernel(const char* __restrict__ x, const char* __restrict__ wqkv, const char* __restrict__ wo,
                                                        const char* __restrict__ w1, const char* __restrict__ w2, char* __restrict__ out, char* ws) {
  extern __shared__ __attribute__((aligned(16))) char smem[];
  const u32 lds0 = (u32)(size_t)(LDSP char*)smem;
  const int b = blockIdx.x, xcd = b & 7, ii = b >> 3;
  const int lane = threadIdx.x & 63, wv = __builtin_amdgcn_readfirstlane(threadIdx.x >> 6);
  ST(0);
  const int ph = xcd & 1, cq = xcd >> 1, jj = ii >> 3;
  const int p = ph * 8 + (ii & 7), g = cq * 2 + (jj >> 1);
  const bool attn = (jj & 1) == 0;
  const int rot = (p + 2 * jj) & 7;

  u32x2 gen; u32x4 xr0, xr1, wr0, wr1, wr2, wr3;
  const int trow = ph * 1024 + cq * 256 + ii * 8 + wv * 2;
  const int task = ii < 26 ? ii : ii - 26, L = task % 13, kk = task / 13;
  const int crow = 256 * ph + 128 * kk + 32 * wv;
  const int ccol0 = L < 6 ? (L >> 1) * 512 + cq * 128 + (L & 1) * 64 : 448 * cq + (L - 6) * 64;
  {
    const u32 pitch = L < 6 ? 3072u : 3584u;
    const char* cb = (L < 6 ? wqkv : w1) + (size_t)crow * pitch + ccol0 * 2;
    const u32 co = (lane >> 3) * pitch + (lane & 7) * 16;
    asm volatile("global_load_dwordx2 %0, %7, %8 sc1\n\tglobal_load_dwordx4 %1, %9, %10\n\tglobal_load_dwordx4 %2, %9, %10 offset:1024\n\t"
                 "global_load_dwordx4 %3, %11, %12\n\tglobal_load_dwordx4 %4, %11, %13\n\tglobal_load_dwordx4 %5, %11, %14\n\tglobal_load_dwordx4 %6, %11, %15"
                 : "=&v"(gen), "=&v"(xr0), "=&v"(xr1), "=&v"(wr0), "=&v"(wr1), "=&v"(wr2), "=&v"(wr3)
                 : "v"(0u), "s"(ws), "v"(lane * 16), "s"(x + (size_t)trow * 1024), "v"(co), "s"(cb), "s"(cb + 8 * pitch), "s"(cb + 16 * pitch), "s"(cb + 24 * pitch)
                 : "memory");
  }
  u32 voff[11];
  {
    const int nA = attn ? 5 : 4, row0 = attn ? p * 128 - 32 : p * 128;
#pragma unroll
    for (int q = 0; q < 5; ++q) {
      const int r = (wv * nA + q) * 8 + (lane >> 3);
      int gr = row0 + r; gr = gr < 0 ? 0 : gr;
      voff[q] = gr * 1024 + (((lane & 7) ^ ((r >> 1) & 7)) << 4);
    }
    if (attn) {
#pragma unroll
      for (int q = 0; q < 6; ++q) {
        const int inst = wv * 6 + q, r = (inst & 7) * 8 + (lane >> 3);
        voff[5 + q] = r * 3072 + (inst >> 3) * 1024 + g * 128 + (((lane & 7) ^ (((r >> 1) & 1) << 2)) << 4);
      }
    } else {
#pragma unroll
      for (int q = 0; q < 7; ++q) {
        const int o = (wv * 7 + q) * 64 + lane, r = o / 28, ch = o - r * 28;
        voff[4 + q] = r * 3584 + g * 448 + ch * 16;
      }
    }
  }
  const char* wbase = attn ? wqkv : w1;
  auto stage = [&](int c) {
    const int kc = (c + rot) & 7;
    const char* gx = x + kc * 128;
    const u32 l = lds0 + c * STAGE;
    if (attn) {
      const char* gw = wbase + kc * (64 * 3072);
#pragma unroll
      for (int q = 0; q < 5; ++q) dma16(voff[q], gx, l + (wv * 5 + q) * 1024);
#pragma unroll
      for (int q = 0; q < 6; ++q) dma16(voff[5 + q], gw, l + 20480 + (wv * 6 + q) * 1024);
    } else {
      const char* gw = wbase + kc * (64 * 3584);
#pragma unroll
      for (int q = 0; q < 4; ++q) dma16(voff[q], gx, l + (wv * 4 + q) * 1024);
#pragma unroll
      for (int q = 0; q < 7; ++q) dma16(voff[4 + q], gw, l + 16384 + (wv * 7 + q) * 1024);
    }
  };
  stage(0);
  asm volatile("s_waitcnt vmcnt(11)" : "+v"(gen), "+v"(xr0), "+v"(xr1), "+v"(wr0), "+v"(wr1), "+v"(wr2), "+v"(wr3)::"memory");
  ST(1);
  const u64 tag64 = (((u64)(u32)__builtin_amdgcn_readfirstlane(gen[1]) << 32) | (u64)(u32)__builtin_amdgcn_readfirstlane(gen[0])) + 1ull;
  const u32 tag = (u32)tag64;
  {
    float a0[8], a1[8], s0 = 0.f, s1 = 0.f;
#pragma unroll
    for (int e = 0; e < 4; ++e) { a0[2 * e] = blo(xr0[e]); a0[2 * e + 1] = bhi(xr0[e]); a1[2 * e] = blo(xr1[e]); a1[2 * e + 1] = bhi(xr1[e]); }
#pragma unroll
    for (int e = 0; e < 8; ++e) { s0 += a0[e]; s1 += a1[e]; }
    const float m0 = wave_sum(s0) * (1.0f / 512.0f), m1 = wave_sum(s1) * (1.0f / 512.0f);
    float q0 = 0.f, q1 = 0.f;
#pragma unroll
    for (int e = 0; e < 8; ++e) { const float d0 = a0[e] - m0, d1 = a1[e] - m1; q0 = __builtin_fmaf(d0, d0, q0); q1 = __builtin_fmaf(d1, d1, q1); }
    const float r0 = __builtin_amdgcn_rsqf(wave_sum(q0) * (1.0f / 512.0f) + 1e-5f), r1 = __builtin_amdgcn_rsqf(wave_sum(q1) * (1.0f / 512.0f) + 1e-5f);
    if (lane < 4) {
      const float rr = (lane & 2) ? r1 : r0, mm = (lane & 2) ? m1 : m0;
      const float val = (lane & 1) ? -rr * mm : rr;
      u32x2 gr = {tag, __builtin_bit_cast(u32, val)};
      asm volatile("global_store_dwordx2 %0, %1, %2 sc1\n\ts_nop 1" ::"v"(WS_TOK + trow * 16 + lane * 8), "v"(gr), "s"(ws) : "memory");
    }
  }
  {
    float c[8];
#pragma unroll
    for (int e = 0; e < 4; ++e) {
      c[2 * e] = (blo(wr0[e]) + blo(wr1[e])) + (blo(wr2[e]) + blo(wr3[e]));
      c[2 * e + 1] = (bhi(wr0[e]) + bhi(wr1[e])) + (bhi(wr2[e]) + bhi(wr3[e]));
    }
    float d[4], f[2];
#pragma unroll
    for (int e = 0; e < 4; ++e) d[e] = addswap32(c[e], c[4 + e]);
    f[0] = addswap16(d[0], d[2]); f[1] = addswap16(d[1], d[3]);
    f[0] += dpp<0x128>(f[0]); f[1] += dpp<0x128>(f[1]);
    const int col = (lane & 7) * 8 + (lane >> 5) * 4 + ((lane >> 4) & 1) * 2;
    if ((lane & 8) == 0) lds_st<f32x2>(lds0 + L_CP + wv * 256 + col * 4, (f32x2){f[0], f[1]});
    if (threadIdx.x == 0) lds_st<u32>(lds0 + L_CP + 1024, WS_CS + (u32)((2 * ph + kk) * 3328 + (L < 6 ? 0 : 1536) + ccol0) * 8);
  }
  stage(1);

  if (attn) {
    if (wv >> 1) phase1<true, 1>(x, wbase, ws, p, g, rot, wv, lane, lds0, voff, tag, tag64);
    else phase1<true, 0>(x, wbase, ws, p, g, rot, wv, lane, lds0, voff, tag, tag64);
  } else {
    if (wv & 1) phase1<false, 1>(x, wbase, ws, p, g, rot, wv, lane, lds0, voff, tag, tag64);
    else phase1<false, 0>(x, wbase, ws, p, g, rot, wv, lane, lds0, voff, tag, tag64);
  }

  const int rt = xcd * 4 + (ii >> 3), ct = ii & 7;
  const int rot2 = (ct * 4 + (ii >> 3) * 9) % 36;
  const int t = lane & 31, hh = lane >> 5, q4 = (lane & 15) >> 2, p4 = lane & 3, blk = (lane >> 4) & 1;
  asm volatile("s_waitcnt lgkmcnt(0)\n\ts_barrier" ::: "memory");
  u32x4 wf[36][2];
  {
    const u32 wvo = (lane >> 3) * 1024 + ct * 128 + (((lane & 7) ^ (((lane >> 4) & 1) << 2)) << 4);
    const u32 reg = lds0 + wv * 36864;
    const u32 tb0 = reg + (4 * hh + q4) * 128 + (((blk * 2 + (p4 >> 1)) ^ (((q4 >> 1) & 1) << 2)) << 4) + (p4 & 1) * 8;
    const u32 tb1 = tb0 ^ 64u;
#pragma unroll
    for (int rd = 0; rd < 2; ++rd) {
#pragma unroll
      for (int u = 0; u < 18; ++u) {
        int ks = rd * 18 + u + rot2; ks = ks >= 36 ? ks - 36 : ks;
        const char* src = ks < 8 ? wo + (size_t)(128 * wv + 16 * ks) * 1024 : w2 + (size_t)(448 * wv + 16 * (ks - 8)) * 1024;
        dma16(wvo, src, reg + u * 2048);
        dma16(wvo, src + 8192, reg + u * 2048 + 1024);
      }
      asm volatile("s_waitcnt vmcnt(0)" ::: "memory");
#pragma unroll
      for (int u = 0; u < 18; ++u) {
        wf[rd * 18 + u][0] = tr_frag(tb0 + u * 2048, tb0 + u * 2048 + 1024);
        wf[rd * 18 + u][1] = tr_frag(tb1 + u * 2048, tb1 + u * 2048 + 1024);
      }
      asm volatile("s_waitcnt lgkmcnt(0)" ::: "memory");
    }
  }
  ST(6);
  const int tid = wv * 64 + lane;
  u32x4 xres[2];
  const u32 oo = (u32)(rt * 64 + (tid >> 3)) * 1024 + ct * 128 + (tid & 7) * 16;
  xres[0] = *(const u32x4*)(x + oo);
  xres[1] = *(const u32x4*)(x + oo + 32 * 1024);
  {
    const u32 fo = WS_FLAG + (u32)((rt * 4 + wv) * 8 + (lane & 7)) * 8;
    for (u32 spins = 0;; ++spins) {
      u64 f;
      asm volatile("global_load_dwordx2 %0, %1, %2 sc1\n\ts_waitcnt vmcnt(0)" : "=&v"(f) : "v"(fo), "s"(ws) : "memory");
      if (__all(f == tag64) || spins > SPIN_CAP) break;
      __builtin_amdgcn_s_sleep(1);
    }
  }
  ST(7);
  f32x16 acc[4];
#pragma unroll
  for (int i = 0; i < 4; ++i)
#pragma unroll
    for (int j = 0; j < 16; ++j) acc[i][j] = 0.f;
  {
    const __amdgpu_buffer_rsrc_t hr = __builtin_amdgcn_make_buffer_rsrc(ws + WS_H, 0, WS_HBYTES, 0x00020000);
    const u32 hb = (u32)((2 * rt * 4 + wv) * 36) * 1024;
    constexpr int DEPTH = 10;
    u32x4 af[DEPTH][2];
    auto ldA = [&](int u) {
      int ks = u + rot2; ks = ks >= 36 ? ks - 36 : ks;
      const u32 so = hb + ks * 1024;
      af[u % DEPTH][0] = __builtin_amdgcn_raw_buffer_load_b128(hr, lane * 16, so, 16);
      af[u % DEPTH][1] = __builtin_amdgcn_raw_buffer_load_b128(hr, lane * 16, so + 4 * 36 * 1024, 16);
    };
#pragma unroll
    for (int u = 0; u < DEPTH; ++u) ldA(u);
    __builtin_amdgcn_sched_barrier(0);
#pragma unroll
    for (int u = 0; u < 36; ++u) {
      const u32x4 a0 = af[u % DEPTH][0], a1 = af[u % DEPTH][1];
      acc[0] = mfma(wf[u][0], a0, acc[0]);
      acc[1] = mfma(wf[u][1], a0, acc[1]);
      acc[2] = mfma(wf[u][0], a1, acc[2]);
      acc[3] = mfma(wf[u][1], a1, acc[3]);
      if (u + DEPTH < 36) ldA(u + DEPTH);
      __builtin_amdgcn_sched_barrier(0);
    }
  }
  ST(8);
  asm volatile("s_barrier" ::: "memory");
#pragma unroll
  for (int i = 0; i < 4; ++i) {
    const int tok = (i >> 1) * 32 + t;
#pragma unroll
    for (int qd = 0; qd < 4; ++qd) {
      const u32 sl = (u32)((i & 1) * 8 + 2 * qd + hh) ^ (u32)(t & 15);
      lds_st<f32x4>(lds0 + wv * 16384 + tok * 256 + sl * 16, (f32x4){acc[i][4 * qd], acc[i][4 * qd + 1], acc[i][4 * qd + 2], acc[i][4 * qd + 3]});
    }
  }
  asm volatile("s_waitcnt lgkmcnt(0)\n\ts_barrier" ::: "memory");
#pragma unroll
  for (int it = 0; it < 2; ++it) {
    const int row = it * 32 + (tid >> 3);
    const u32 a0 = lds0 + row * 256 + (((u32)(2 * (tid & 7)) ^ (u32)(row & 15)) << 4), a1 = a0 ^ 16u;
    f32x4 s0 = lds_ld<f32x4>(a0), s1 = lds_ld<f32x4>(a1);
#pragma unroll
    for (int w = 1; w < 4; ++w) { s0 += lds_ld<f32x4>(a0 + w * 16384); s1 += lds_ld<f32x4>(a1 + w * 16384); }
    const u32x4 xr = xres[it];
    u32x4 r;
    r[0] = pk(s0[0] + blo(xr[0]), s0[1] + bhi(xr[0]));
    r[1] = pk(s0[2] + blo(xr[1]), s0[3] + bhi(xr[1]));
    r[2] = pk(s1[0] + blo(xr[2]), s1[1] + bhi(xr[2]));
    r[3] = pk(s1[2] + blo(xr[3]), s1[3] + bhi(xr[3]));
    *(u32x4*)(out + oo + it * 32 * 1024) = r;
  }
  if (b == 0) {
    for (u32 spins = 0;; ++spins) {
      u64 f0, f1, f2, f3;
      const u32 fo = WS_FLAG + tid * 8;
      asm volatile("global_load_dwordx2 %0, %4, %5 sc1\n\tglobal_load_dwordx2 %1, %4, %5 offset:2048 sc1\n\t"
                   "global_load_dwordx2 %2, %4, %6 sc1\n\tglobal_load_dwordx2 %3, %4, %6 offset:2048 sc1\n\ts_waitcnt vmcnt(0)"
                   : "=&v"(f0), "=&v"(f1), "=&v"(f2), "=&v"(f3) : "v"(fo), "s"(ws), "s"(ws + 4096) : "memory");
      if (__all(f0 == tag64 && f1 == tag64 && f2 == tag64 && f3 == tag64) || spins > SPIN_CAP) break;
      __builtin_amdgcn_s_sleep(1);
    }
    asm volatile("s_barrier" ::: "memory");
    if (tid == 0) asm volatile("global_store_dwordx2 %0, %1, %2 sc1\n\ts_nop 1" ::"v"(0u), "v"(tag64), "s"(ws) : "memory");
  }
  ST(9);
}

extern "C" void kernel_launch(void* const* d_in, const int* in_sizes, int n_in, void* d_out, int out_size, void* d_ws, size_t ws_size,
                              hipStream_t stream) {
  (void)in_sizes; (void)n_in; (void)out_size; (void)ws_size;
  (void)hipFuncSetAttribute((const void*)fused_kernel, hipFuncAttributeMaxDynamicSharedMemorySize, LDS_BYTES);
  fused_kernel<<<256, 256, LDS_BYTES, stream>>>((const char*)d_in[0], (const char*)d_in[1], (const char*)d_in[2], (const char*)d_in[4],
                                                (const char*)d_in[5], (char*)d_out, (char*)d_ws);
}
```

```cpp
#include <hip/hip_runtime.h>
#include <hip/hip_bf16.h>

typedef unsigned long long u64;
typedef unsigned u32;
typedef __bf16 bf16x8 __attribute__((ext_vector_type(8)));
typedef __bf16 bf16x2 __attribute__((ext_vector_type(2)));
typedef short s16x4 __attribute__((ext_vector_type(4)));
typedef float f32x16 __attribute__((ext_vector_type(16)));
typedef float f32x4 __attribute__((ext_vector_type(4)));
typedef float f32x2 __attribute__((ext_vector_type(2)));
typedef u32 u32x4 __attribute__((ext_vector_type(4)));
typedef u32 u32x2 __attribute__((ext_vector_type(2)));
typedef u32 u32x8 __attribute__((ext_vector_type(8)));
typedef u32 u32x16 __attribute__((ext_vector_type(16)));
#define LDSP __attribute__((address_space(3)))
#define DI __device__ __forceinline__

#define WSOFF 0
#define STAMP 0

constexpr u32 WS_GEN = 0;
constexpr u32 WS_FLAG = 196608;
constexpr u32 WS_TOK = 8192;
constexpr u32 WS_CS = 131072;
constexpr u32 WS_H = 262144;
constexpr u32 WS_HBYTES = 64u * 4u * 36u * 1024u;
constexpr u32 WS_STAMP = 24u << 20;
constexpr u32 STAGE = 45056;
constexpr u32 L_RING = 2 * STAGE;
constexpr u32 L_K = 139264;
constexpr u32 L_V = 0;
constexpr u32 L_CS = 157696;
constexpr u32 L_CP = L_CS + 1024;
constexpr u32 LDS_BYTES = 160 * 1024;
constexpr u32 SPIN_CAP = 1u << 16;

#define BIGASM \
  "s_load_dwordx4 s[40:43], %0, 0x0\n\t" \
  "s_load_dwordx4 s[64:67], %0, 0x18\n\t" \
  "s_load_dwordx2 s[44:45], %0, 0x30\n\t" \
  "s_load_dwordx2 s[62:63], %0, 0x10\n\t" \
  "s_load_dwordx2 s[80:81], %0, 0x28\n\t" \
  "s_getpc_b64 s[10:11]\n\t" \
  "BEG%=:\n\t" \
  "v_lshrrev_b32 v1, 6, v0\n\t" \
  "v_and_b32 v80, 63, v0\n\t" \
  "v_readfirstlane_b32 s86, v1\n\t" \
  "s_lshr_b32 s89, s84, 3\n\t" \
  "s_and_b32 s50, s84, 1\n\t" \
  "s_and_b32 s51, s89, 7\n\t" \
  "s_lshl3_add_u32 s87, s50, s51\n\t" \
  "s_and_b32 s52, s84, 6\n\t" \
  "s_lshr_b32 s53, s84, 7\n\t" \
  "s_add_u32 s88, s52, s53\n\t" \
  "v_lshrrev_b32 v82, 3, v80\n\t" \
  "v_and_b32 v83, 7, v80\n\t" \
  "v_lshlrev_b32 v0, 4, v80\n\t" \
  "v_lshrrev_b32 v90, 1, v82\n\t" \
  "v_xor_b32 v90, v90, v83\n\t" \
  "s_bitcmp1_b32 s84, 6\n\t" \
  "s_cbranch_scc0 ATT%=\n\t" \
  "s_lshl2_add_u32 s54, s87, s86\n\t" \
  "s_lshl_b32 s54, s54, 15\n\t" \
  "s_add_u32 s54, s54, 4096\n\t" \
  "v_lshl_add_u32 v91, v82, 10, s54\n\t" \
  "s_mul_i32 s55, s86, 57344\n\t" \
  "v_lshl_add_u32 v64, v90, 4, v91\n\t" \
  "s_mul_i32 s56, s88, 448\n\t" \
  "v_xor_b32 v65, 64, v64\n\t" \
  "s_add_u32 s55, s55, s56\n\t" \
  "v_add_u32 v66, 14336, v64\n\t" \
  "s_add_u32 s55, s55, 4096\n\t" \
  "v_add_u32 v65, 7168, v65\n\t" \
  "v_mul_u32_u24 v92, 2341, v80\n\t" \
  "v_add_u32 v67, 14336, v65\n\t" \
  "v_add_u32 v93, s55, v0\n\t" \
  "s_movk_i32 s57, 3136\n\t" \
  "s_lshl_b32 s47, s86, 12\n\t" \
  "s_mul_i32 s48, s86, 7168\n\t" \
  "v_add_u32 v94, 4096, v93\n\t" \
  "s_add_u32 s48, s48, 16384\n\t" \
  "v_lshrrev_b32 v95, 16, v92\n\t" \
  "v_mad_u32_u24 v68, v95, s57, v93\n\t" \
  "v_add_u32 v95, 149824, v92\n\t" \
  "v_lshrrev_b32 v95, 16, v95\n\t" \
  "v_mad_u32_u24 v69, v95, s57, v93\n\t" \
  "v_add_u32 v95, 299648, v92\n\t" \
  "v_lshrrev_b32 v95, 16, v95\n\t" \
  "v_mad_u32_u24 v70, v95, s57, v93\n\t" \
  "v_add_u32 v95, 449472, v92\n\t" \
  "v_lshrrev_b32 v95, 16, v95\n\t" \
  "v_mad_u32_u24 v71, v95, s57, v93\n\t" \
  "v_add_u32 v95, 599296, v92\n\t" \
  "v_lshrrev_b32 v95, 16, v95\n\t" \
  "v_mad_u32_u24 v72, v95, s57, v94\n\t" \
  "v_add_u32 v95, 749120, v92\n\t" \
  "v_lshrrev_b32 v95, 16, v95\n\t" \
  "v_mad_u32_u24 v73, v95, s57, v94\n\t" \
  "v_add_u32 v95, 898944, v92\n\t" \
  "v_lshrrev_b32 v95, 16, v95\n\t" \
  "v_mad_u32_u24 v74, v95, s57, v94\n\t" \
  "s_waitcnt lgkmcnt(0)\n\t" \
  "s_mov_b32 s92, s42\n\t" \
  "s_mov_b32 s93, s43\n\t" \
  "s_sub_u32 s40, s40, 4096\n\t" \
  "s_subb_u32 s41, s41, 0\n\t" \
  "s_sub_u32 s42, s64, 4096\n\t" \
  "s_subb_u32 s43, s65, 0\n\t" \
  "s_lshr_b32 s85, s84, 6\n\t" \
  "s_add_u32 s85, s85, s87\n\t" \
  "s_and_b32 s85, s85, 1\n\t" \
  "s_lshl_b32 s85, s85, 2\n\t" \
  "s_xor_b32 s57, s85, 0\n\t" \
  "s_lshl_b32 s58, s57, 7\n\t" \
  "s_add_u32 s4, s40, s58\n\t" \
  "s_addc_u32 s5, s41, 0\n\t" \
  "s_mul_i32 s58, s57, 229376\n\t" \
  "s_add_u32 s6, s42, s58\n\t" \
  "s_addc_u32 s7, s43, 0\n\t" \
  "s_add_u32 m0, s47, 0\n\t" \
  "s_nop 0\n\t" \
  "global_load_lds_dwordx4 v64, s[4:5] offset:0\n\t" \
  "global_load_lds_dwordx4 v65, s[4:5] offset:1024\n\t" \
  "global_load_lds_dwordx4 v66, s[4:5] offset:2048\n\t" \
  "global_load_lds_dwordx4 v67, s[4:5] offset:3072\n\t" \
  "s_add_u32 m0, s48, 0\n\t" \
  "s_nop 0\n\t" \
  "global_load_lds_dwordx4 v68, s[6:7] offset:0\n\t" \
  "global_load_lds_dwordx4 v69, s[6:7] offset:1024\n\t" \
  "global_load_lds_dwordx4 v70, s[6:7] offset:2048\n\t" \
  "global_load_lds_dwordx4 v71, s[6:7] offset:3072\n\t" \
  "s_add_u32 m0, s48, 4096\n\t" \
  "s_nop 0\n\t" \
  "global_load_lds_dwordx4 v72, s[6:7] offset:0\n\t" \
  "global_load_lds_dwordx4 v73, s[6:7] offset:1024\n\t" \
  "global_load_lds_dwordx4 v74, s[6:7] offset:2048\n\t" \
  "s_lshl2_add_u32 s54, s89, s86\n\t" \
  "s_lshl_b32 s54, s54, 9\n\t" \
  "v_and_b32 v96, 3, v80\n\t" \
  "v_lshl_add_u32 v96, v96, 7, s54\n\t" \
  "v_min_u32 v96, END%=-BEG%=, v96\n\t" \
  "global_load_dword v141, v96, s[10:11]\n\t" \
  "s_lshl2_add_u32 s54, s89, s86\n\t" \
  "v_min_u32 v96, 0, v80\n\t" \
  "s_mul_i32 s55, s54, 1\n\t" \
  "v_add_u32 v96, s55, v96\n\t" \
  "v_lshlrev_b32 v96, 16, v96\n\t" \
  "v_min_u32 v96, 1048448, v96\n\t" \
  "s_lshl_b32 s55, s50, 20\n\t" \
  "s_add_u32 s55, s55, 4096\n\t" \
  "v_add_u32 v96, s55, v96\n\t" \
  "global_load_dword v141, v96, s[40:41]\n\t" \
  "v_min_u32 v96, 0, v80\n\t" \
  "s_mul_i32 s55, s54, 1\n\t" \
  "v_add_u32 v96, s55, v96\n\t" \
  "v_lshlrev_b32 v96, 16, v96\n\t" \
  "v_min_u32 v96, 1572736, v96\n\t" \
  "global_load_dword v141, v96, s[92:93]\n\t" \
  "v_min_u32 v96, 0, v80\n\t" \
  "s_mul_i32 s55, s54, 1\n\t" \
  "v_add_u32 v96, s55, v96\n\t" \
  "v_lshlrev_b32 v96, 16, v96\n\t" \
  "v_min_u32 v96, 1834880, v96\n\t" \
  "global_load_dword v141, v96, s[64:65]\n\t" \
  "v_min_u32 v96, 0, v80\n\t" \
  "s_mul_i32 s55, s54, 1\n\t" \
  "v_add_u32 v96, s55, v96\n\t" \
  "v_lshlrev_b32 v96, 16, v96\n\t" \
  "v_min_u32 v96, 1834880, v96\n\t" \
  "global_load_dword v141, v96, s[66:67]\n\t" \
  "v_min_u32 v96, 0, v80\n\t" \
  "s_mul_i32 s55, s54, 1\n\t" \
  "v_add_u32 v96, s55, v96\n\t" \
  "v_lshlrev_b32 v96, 16, v96\n\t" \
  "v_min_u32 v96, 524160, v96\n\t" \
  "global_load_dword v141, v96, s[62:63]\n\t" \
  "v_min_u32 v96, 0, v80\n\t" \
  "s_mul_i32 s55, s54, 1\n\t" \
  "v_add_u32 v96, s55, v96\n\t" \
  "v_lshlrev_b32 v96, 16, v96\n\t" \
  "v_min_u32 v96, 2097024, v96\n\t" \
  "global_load_dword v141, v96, s[80:81]\n\t" \
  "v_min_u32 v96, 2, v80\n\t" \
  "s_mul_i32 s55, s54, 3\n\t" \
  "v_add_u32 v96, s55, v96\n\t" \
  "v_lshlrev_b32 v96, 16, v96\n\t" \
  "v_min_u32 v96, 17825664, v96\n\t" \
  "global_load_dword v141, v96, s[44:45]\n\t" \
  "s_xor_b32 s57, s85, 1\n\t" \
  "s_lshl_b32 s58, s57, 7\n\t" \
  "s_add_u32 s4, s40, s58\n\t" \
  "s_addc_u32 s5, s41, 0\n\t" \
  "s_mul_i32 s58, s57, 229376\n\t" \
  "s_add_u32 s6, s42, s58\n\t" \
  "s_addc_u32 s7, s43, 0\n\t" \
  "s_lshr_b32 s90, s86, 1\n\t" \
  "s_and_b32 s91, s86, 1\n\t" \
  "v_and_b32 v84, 31, v80\n\t" \
  "s_add_u32 m0, s47, 45056\n\t" \
  "s_nop 0\n\t" \
  "global_load_lds_dwordx4 v64, s[4:5] offset:0\n\t" \
  "v_lshrrev_b32 v85, 5, v80\n\t" \
  "v_bfe_u32 v86, v80, 2, 2\n\t" \
  "global_load_lds_dwordx4 v65, s[4:5] offset:1024\n\t" \
  "v_bfe_u32 v111, v80, 1, 3\n\t" \
  "v_xor_b32 v111, v111, v85\n\t" \
  "v_lshlrev_b32 v111, 4, v111\n\t" \
  "global_load_lds_dwordx4 v66, s[4:5] offset:2048\n\t" \
  "v_lshl_add_u32 v111, v84, 7, v111\n\t" \
  "v_and_b32 v87, 3, v80\n\t" \
  "v_lshlrev_b32 v87, 3, v87\n\t" \
  "global_load_lds_dwordx4 v67, s[4:5] offset:3072\n\t" \
  "v_and_b32 v112, 16, v80\n\t" \
  "v_lshl_add_u32 v87, v112, 1, v87\n\t" \
  "s_add_u32 m0, s48, 45056\n\t" \
  "s_nop 0\n\t" \
  "global_load_lds_dwordx4 v68, s[6:7] offset:0\n\t" \
  "v_lshl_add_u32 v112, v85, 3, v86\n\t" \
  "s_lshl_b32 s54, s86, 12\n\t" \
  "v_add_u32 v48, s54, v111\n\t" \
  "global_load_lds_dwordx4 v69, s[6:7] offset:1024\n\t" \
  "v_xor_b32 v52, 0x1000, v48\n\t" \
  "v_mul_u32_u24 v112, 448, v112\n\t" \
  "global_load_lds_dwordx4 v70, s[6:7] offset:2048\n\t" \
  "s_mul_i32 s55, s91, 192\n\t" \
  "s_add_u32 s55, s55, 16384\n\t" \
  "v_add3_u32 v56, v112, v87, s55\n\t" \
  "global_load_lds_dwordx4 v71, s[6:7] offset:3072\n\t" \
  "v_add_u32 v57, 16768, v112\n\t" \
  "v_add_u32 v57, v57, v87\n\t" \
  "v_xor_b32 v49, 32, v48\n\t" \
  "s_add_u32 m0, s48, 49152\n\t" \
  "s_nop 0\n\t" \
  "global_load_lds_dwordx4 v72, s[6:7] offset:0\n\t" \
  "v_xor_b32 v50, 64, v48\n\t" \
  "v_xor_b32 v51, 96, v48\n\t" \
  "global_load_lds_dwordx4 v73, s[6:7] offset:1024\n\t" \
  "v_xor_b32 v53, 32, v52\n\t" \
  "v_xor_b32 v54, 64, v52\n\t" \
  "v_xor_b32 v55, 96, v52\n\t" \
  "global_load_lds_dwordx4 v74, s[6:7] offset:2048\n\t" \
  "s_lshl2_add_u32 s94, s88, s86\n\t" \
  "s_lshl_b32 s94, s94, 2\n\t" \
  "s_lshl_b32 s33, s94, 7\n\t" \
  "s_add_u32 s33, s33, 0\n\t" \
  "v_lshrrev_b32 v24, 1, v0\n\t" \
  "v_add_u32 v24, s33, v24\n\t" \
  "v_add_u32 v25, 90112, v24\n\t" \
  "s_lshr_b32 s95, s87, 1\n\t" \
  "s_min_u32 s95, s95, 6\n\t" \
  "s_mul_i32 s33, s86, 7168\n\t" \
  "s_lshl_b32 s34, s95, 6\n\t" \
  "s_add_u32 s33, s33, s34\n\t" \
  "s_sub_u32 s33, s33, 384\n\t" \
  "v_add_u32 v30, s33, v57\n\t" \
  "v_add_u32 v31, 90112, v30\n\t" \
  "v_bfe_u32 v40, v0, 4, 4\n\t" \
  "v_bfe_u32 v41, v0, 8, 1\n\t" \
  "v_cmp_eq_u32 vcc, v40, v41\n\t" \
  "v_mov_b32 v41, 0x3f803f80\n\t" \
  "s_nop 0\n\t" \
  "v_cndmask_b32 v40, 0, v41, vcc\n\t" \
  "v_mov_b32 v41, v40\n\t" \
  "v_mov_b32 v42, v40\n\t" \
  "v_mov_b32 v43, v40\n\t" \
  "s_mov_b32 s61, 0x3f803f80\n\t" \
  "s_waitcnt vmcnt(19)\n\t" \
  "s_barrier\n\t" \
  "ds_read_b128 v[80:83], v48\n\t" \
  "ds_read_b64_tr_b16 v[92:93], v56 offset:0\n\t" \
  "ds_read_b64_tr_b16 v[94:95], v56 offset:1792\n\t" \
  "ds_read_b128 v[84:87], v52\n\t" \
  "ds_read_b64_tr_b16 v[96:97], v56 offset:64\n\t" \
  "ds_read_b64_tr_b16 v[98:99], v56 offset:1856\n\t" \
  "ds_read_b64_tr_b16 v[100:101], v56 offset:128\n\t" \
  "ds_read_b64_tr_b16 v[102:103], v56 offset:1920\n\t" \
  "ds_read_b64_tr_b16 v[88:89], v57 offset:0\n\t" \
  "ds_read_b64_tr_b16 v[90:91], v57 offset:1792\n\t" \
  "s_waitcnt lgkmcnt(7)\n\t" \
  "v_mfma_f32_32x32x16_bf16 v[144:159], v[92:95], v[80:83], 0\n\t" \
  "ds_read_b128 v[108:111], v49\n\t" \
  "ds_read_b64_tr_b16 v[120:121], v56 offset:7168\n\t" \
  "ds_read_b64_tr_b16 v[122:123], v56 offset:8960\n\t" \
  "s_xor_b32 s57, s85, 2\n\t" \
  "s_lshl_b32 s58, s57, 7\n\t" \
  "s_add_u32 s4, s40, s58\n\t" \
  "s_addc_u32 s5, s41, 0\n\t" \
  "s_waitcnt lgkmcnt(9)\n\t" \
  "v_mfma_f32_32x32x16_bf16 v[192:207], v[92:95], v[84:87], 0\n\t" \
  "ds_read_b128 v[112:115], v53\n\t" \
  "ds_read_b64_tr_b16 v[124:125], v56 offset:7232\n\t" \
  "ds_read_b64_tr_b16 v[126:127], v56 offset:9024\n\t" \
  "s_mul_i32 s58, s57, 229376\n\t" \
  "s_add_u32 s6, s42, s58\n\t" \
  "s_addc_u32 s7, s43, 0\n\t" \
  "s_waitcnt lgkmcnt(10)\n\t" \
  "v_mfma_f32_32x32x16_bf16 v[160:175], v[96:99], v[80:83], 0\n\t" \
  "ds_read_b64_tr_b16 v[128:129], v56 offset:7296\n\t" \
  "ds_read_b64_tr_b16 v[130:131], v56 offset:9088\n\t" \
  "v_mfma_f32_32x32x16_bf16 v[208:223], v[96:99], v[84:87], 0\n\t" \
  "ds_read_b64_tr_b16 v[116:117], v57 offset:7168\n\t" \
  "ds_read_b64_tr_b16 v[118:119], v57 offset:8960\n\t" \
  "v_mov_b32 v58, 0\n\t" \
  "global_load_dwordx2 v[138:139], v58, s[44:45] sc1\n\t" \
  "s_add_u32 m0, s47, 90112\n\t" \
  "s_waitcnt lgkmcnt(12)\n\t" \
  "v_mfma_f32_32x32x16_bf16 v[176:191], v[100:103], v[80:83], 0\n\t" \
  "global_load_lds_dwordx4 v64, s[4:5] offset:0\n\t" \
  "s_lshl2_add_u32 s33, s84, s86\n\t" \
  "s_mul_i32 s33, s33, 2304\n\t" \
  "v_min_u32 v75, 272, v0\n\t" \
  "v_mfma_f32_32x32x16_bf16 v[224:239], v[100:103], v[84:87], 0\n\t" \
  "global_load_lds_dwordx4 v65, s[4:5] offset:1024\n\t" \
  "v_lshl_add_u32 v75, v75, 3, s33\n\t" \
  "v_cmp_gt_u32 vcc, 1835008, v75\n\t" \
  "v_mov_b32 v136, s62\n\t" \
  "s_waitcnt lgkmcnt(10)\n\t" \
  "v_mfma_f32_32x32x16_bf16 v[240:255], v[88:91], v[80:83], 0\n\t" \
  "global_load_lds_dwordx4 v66, s[4:5] offset:2048\n\t" \
  "v_mov_b32 v137, s63\n\t" \
  "v_mov_b32 v78, s66\n\t" \
  "v_mov_b32 v79, s67\n\t" \
  "s_waitcnt lgkmcnt(0)\n\t" \
  "v_mfma_f32_32x32x16_bf16 v[144:159], v[120:123], v[108:111], v[144:159]\n\t" \
  "ds_read_b128 v[80:83], v50\n\t" \
  "ds_read_b64_tr_b16 v[92:93], v56 offset:14336\n\t" \
  "ds_read_b64_tr_b16 v[94:95], v56 offset:16128\n\t" \
  "s_mov_b32 s55, 0xb000\n\t" \
  "v_mfma_f32_32x32x16_bf16 v[192:207], v[120:123], v[112:115], v[192:207]\n\t" \
  "ds_read_b128 v[84:87], v54\n\t" \
  "ds_read_b64_tr_b16 v[96:97], v56 offset:14400\n\t" \
  "ds_read_b64_tr_b16 v[98:99], v56 offset:16192\n\t" \
  "v_mfma_f32_32x32x16_bf16 v[160:175], v[124:127], v[108:111], v[160:175]\n\t" \
  "ds_read_b64_tr_b16 v[100:101], v56 offset:14464\n\t" \
  "ds_read_b64_tr_b16 v[102:103], v56 offset:16256\n\t" \
  "v_mfma_f32_32x32x16_bf16 v[208:223], v[124:127], v[112:115], v[208:223]\n\t" \
  "ds_read_b64_tr_b16 v[88:89], v57 offset:14336\n\t" \
  "ds_read_b64_tr_b16 v[90:91], v57 offset:16128\n\t" \
  "v_mfma_f32_32x32x16_bf16 v[176:191], v[128:131], v[108:111], v[176:191]\n\t" \
  "global_load_lds_dwordx4 v67, s[4:5] offset:3072\n\t" \
  "ds_read_b64 v[26:27], v24 offset:0\n\t" \
  "ds_read_b64_tr_b16 v[32:33], v30 offset:0\n\t" \
  "ds_read_b64_tr_b16 v[34:35], v30 offset:1792\n\t" \
  "v_subrev_u32 v58, 1835008, v75\n\t" \
  "v_cndmask_b32 v136, v136, v78, vcc\n\t" \
  "v_cndmask_b32 v137, v137, v79, vcc\n\t" \
  "s_add_u32 m0, s48, 90112\n\t" \
  "v_mfma_f32_32x32x16_bf16 v[224:239], v[128:131], v[112:115], v[224:239]\n\t" \
  "global_load_lds_dwordx4 v68, s[6:7] offset:0\n\t" \
  "v_cndmask_b32 v75, v58, v75, vcc\n\t" \
  "v_add_co_u32 v136, vcc, v136, v75\n\t" \
  "s_nop 1\n\t" \
  "v_addc_co_u32 v137, vcc, 0, v137, vcc\n\t" \
  "v_mfma_f32_32x32x16_bf16 v[240:255], v[116:119], v[108:111], v[240:255]\n\t" \
  "global_load_lds_dwordx4 v69, s[6:7] offset:1024\n\t" \
  "s_lshl_b32 s33, s87, 7\n\t" \
  "s_add_u32 s33, s33, s94\n\t" \
  "s_lshl_b32 s33, s33, 5\n\t" \
  "s_waitcnt lgkmcnt(3)\n\t" \
  "v_mfma_f32_32x32x16_bf16 v[144:159], v[92:95], v[80:83], v[144:159]\n\t" \
  "ds_read_b128 v[108:111], v51\n\t" \
  "ds_read_b64_tr_b16 v[120:121], v56 offset:21504\n\t" \
  "ds_read_b64_tr_b16 v[122:123], v56 offset:23296\n\t" \
  "v_mfma_f32_32x32x16_bf16 v[192:207], v[92:95], v[84:87], v[192:207]\n\t" \
  "ds_read_b128 v[112:115], v55\n\t" \
  "ds_read_b64_tr_b16 v[124:125], v56 offset:21568\n\t" \
  "ds_read_b64_tr_b16 v[126:127], v56 offset:23360\n\t" \
  "v_mfma_f32_32x32x16_bf16 v[160:175], v[96:99], v[80:83], v[160:175]\n\t" \
  "ds_read_b64_tr_b16 v[128:129], v56 offset:21632\n\t" \
  "ds_read_b64_tr_b16 v[130:131], v56 offset:23424\n\t" \
  "v_mfma_f32_32x32x16_bf16 v[208:223], v[96:99], v[84:87], v[208:223]\n\t" \
  "ds_read_b64_tr_b16 v[116:117], v57 offset:21504\n\t" \
  "ds_read_b64_tr_b16 v[118:119], v57 offset:23296\n\t" \
  "s_waitcnt lgkmcnt(10)\n\t" \
  "v_mfma_f32_16x16x32_bf16 v[36:39], v[40:43], v[32:35], 0\n\t" \
  "v_mfma_f32_32x32x16_bf16 v[176:191], v[100:103], v[80:83], v[176:191]\n\t" \
  "global_load_lds_dwordx4 v70, s[6:7] offset:2048\n\t" \
  "v_dot2_f32_bf16 v28, v26, s61, 0\n\t" \
  "v_dot2_f32_bf16 v29, v26, v26, 0\n\t" \
  "s_and_b32 s34, s85, 4\n\t" \
  "s_lshl2_add_u32 s33, s34, s33\n\t" \
  "s_add_u32 s33, s33, 8192\n\t" \
  "v_mfma_f32_32x32x16_bf16 v[224:239], v[100:103], v[84:87], v[224:239]\n\t" \
  "global_load_lds_dwordx4 v71, s[6:7] offset:3072\n\t" \
  "v_lshrrev_b32 v75, 8, v0\n\t" \
  "v_lshl_add_u32 v142, v75, 5, s33\n\t" \
  "v_bfe_u32 v75, v0, 4, 1\n\t" \
  "s_add_u32 m0, s48, 94208\n\t" \
  "v_mfma_f32_32x32x16_bf16 v[240:255], v[88:91], v[80:83], v[240:255]\n\t" \
  "global_load_lds_dwordx4 v72, s[6:7] offset:0\n\t" \
  "v_dot2c_f32_bf16 v28, s61, v27\n\t" \
  "v_dot2c_f32_bf16 v29, v27, v27\n\t" \
  "v_lshl_add_u32 v142, v75, 3, v142\n\t" \
  "s_lshl_b32 s33, s86, 7\n\t" \
  "s_add_u32 s33, s33, 158720\n\t" \
  "s_waitcnt lgkmcnt(0)\n\t" \
  "v_mfma_f32_32x32x16_bf16 v[144:159], v[120:123], v[108:111], v[144:159]\n\t" \
  "global_load_lds_dwordx4 v73, s[6:7] offset:1024\n\t" \
  "v_add_u32 v48, s55, v48\n\t" \
  "v_add_u32 v49, s55, v49\n\t" \
  "v_add_u32 v50, s55, v50\n\t" \
  "v_add_u32 v51, s55, v51\n\t" \
  "v_add_u32 v52, s55, v52\n\t" \
  "v_mfma_f32_32x32x16_bf16 v[192:207], v[120:123], v[112:115], v[192:207]\n\t" \
  "global_load_lds_dwordx4 v74, s[6:7] offset:2048\n\t" \
  "v_add_u32 v53, s55, v53\n\t" \
  "v_add_u32 v54, s55, v54\n\t" \
  "v_add_u32 v55, s55, v55\n\t" \
  "v_add_u32 v56, s55, v56\n\t" \
  "v_add_u32 v57, s55, v57\n\t" \
  "v_mfma_f32_32x32x16_bf16 v[160:175], v[124:127], v[108:111], v[160:175]\n\t" \
  "s_waitcnt vmcnt(12)\n\t" \
  "s_waitcnt lgkmcnt(0)\n\t" \
  "s_barrier\n\t" \
  "ds_read_b128 v[80:83], v48\n\t" \
  "ds_read_b64_tr_b16 v[92:93], v56 offset:0\n\t" \
  "ds_read_b64_tr_b16 v[94:95], v56 offset:1792\n\t" \
  "v_mfma_f32_32x32x16_bf16 v[208:223], v[124:127], v[112:115], v[208:223]\n\t" \
  "ds_read_b128 v[84:87], v52\n\t" \
  "ds_read_b64_tr_b16 v[96:97], v56 offset:64\n\t" \
  "ds_read_b64_tr_b16 v[98:99], v56 offset:1856\n\t" \
  "v_mfma_f32_32x32x16_bf16 v[176:191], v[128:131], v[108:111], v[176:191]\n\t" \
  "ds_read_b64_tr_b16 v[100:101], v56 offset:128\n\t" \
  "ds_read_b64_tr_b16 v[102:103], v56 offset:1920\n\t" \
  "v_mfma_f32_32x32x16_bf16 v[224:239], v[128:131], v[112:115], v[224:239]\n\t" \
  "ds_read_b64_tr_b16 v[88:89], v57 offset:0\n\t" \
  "ds_read_b64_tr_b16 v[90:91], v57 offset:1792\n\t" \
  "v_mfma_f32_32x32x16_bf16 v[240:255], v[116:119], v[108:111], v[240:255]\n\t" \
  "v_min_u32 v143, 2048, v0\n\t" \
  "v_cmp_gt_u32 vcc, 256, v0\n\t" \
  "v_lshrrev_b32 v75, 2, v0\n\t" \
  "s_waitcnt lgkmcnt(7)\n\t" \
  "v_mfma_f32_32x32x16_bf16 v[144:159], v[92:95], v[80:83], v[144:159]\n\t" \
  "ds_read_b128 v[108:111], v49\n\t" \
  "ds_read_b64_tr_b16 v[120:121], v56 offset:7168\n\t" \
  "ds_read_b64_tr_b16 v[122:123], v56 offset:8960\n\t" \
  "s_xor_b32 s57, s85, 3\n\t" \
  "s_lshl_b32 s58, s57, 7\n\t" \
  "s_add_u32 s4, s40, s58\n\t" \
  "s_addc_u32 s5, s41, 0\n\t" \
  "s_waitcnt lgkmcnt(9)\n\t" \
  "v_mfma_f32_32x32x16_bf16 v[192:207], v[92:95], v[84:87], v[192:207]\n\t" \
  "ds_read_b128 v[112:115], v53\n\t" \
  "ds_read_b64_tr_b16 v[124:125], v56 offset:7232\n\t" \
  "ds_read_b64_tr_b16 v[126:127], v56 offset:9024\n\t" \
  "s_mul_i32 s58, s57, 229376\n\t" \
  "s_add_u32 s6, s42, s58\n\t" \
  "s_addc_u32 s7, s43, 0\n\t" \
  "s_waitcnt lgkmcnt(10)\n\t" \
  "v_mfma_f32_32x32x16_bf16 v[160:175], v[96:99], v[80:83], v[160:175]\n\t" \
  "ds_read_b64_tr_b16 v[128:129], v56 offset:7296\n\t" \
  "ds_read_b64_tr_b16 v[130:131], v56 offset:9088\n\t" \
  "v_mfma_f32_32x32x16_bf16 v[208:223], v[96:99], v[84:87], v[208:223]\n\t" \
  "ds_read_b64_tr_b16 v[116:117], v57 offset:7168\n\t" \
  "ds_read_b64_tr_b16 v[118:119], v57 offset:8960\n\t" \
  "s_add_u32 m0, s47, 0\n\t" \
  "s_waitcnt lgkmcnt(12)\n\t" \
  "v_mfma_f32_32x32x16_bf16 v[176:191], v[100:103], v[80:83], v[176:191]\n\t" \
  "global_load_lds_dwordx4 v64, s[4:5] offset:0\n\t" \
  "v_mov_b32 v143, 1024\n\t" \
  "v_cndmask_b32 v143, v143, v75, vcc\n\t" \
  "v_add_u32 v143, s33, v143\n\t" \
  "v_mfma_f32_32x32x16_bf16 v[224:239], v[100:103], v[84:87], v[224:239]\n\t" \
  "global_load_lds_dwordx4 v65, s[4:5] offset:1024\n\t" \
  "s_lshl_b32 s33, s86, 5\n\t" \
  "s_add_u32 s33, s33, 158720\n\t" \
  "v_and_b32 v75, 28, v75\n\t" \
  "s_waitcnt lgkmcnt(10)\n\t" \
  "v_mfma_f32_32x32x16_bf16 v[240:255], v[88:91], v[80:83], v[240:255]\n\t" \
  "global_load_lds_dwordx4 v66, s[4:5] offset:2048\n\t" \
  "v_add_u32 v23, s33, v75\n\t" \
  "s_mul_i32 s33, s88, 224\n\t" \
  "s_lshl_b32 s35, s95, 5\n\t" \
  "s_waitcnt lgkmcnt(0)\n\t" \
  "v_mfma_f32_32x32x16_bf16 v[144:159], v[120:123], v[108:111], v[144:159]\n\t" \
  "ds_read_b128 v[80:83], v50\n\t" \
  "ds_read_b64_tr_b16 v[92:93], v56 offset:14336\n\t" \
  "ds_read_b64_tr_b16 v[94:95], v56 offset:16128\n\t" \
  "s_mov_b32 s55, 0xb000\n\t" \
  "v_mfma_f32_32x32x16_bf16 v[192:207], v[120:123], v[112:115], v[192:207]\n\t" \
  "ds_read_b128 v[84:87], v54\n\t" \
  "ds_read_b64_tr_b16 v[96:97], v56 offset:14400\n\t" \
  "ds_read_b64_tr_b16 v[98:99], v56 offset:16192\n\t" \
  "v_mfma_f32_32x32x16_bf16 v[160:175], v[124:127], v[108:111], v[160:175]\n\t" \
  "ds_read_b64_tr_b16 v[100:101], v56 offset:14464\n\t" \
  "ds_read_b64_tr_b16 v[102:103], v56 offset:16256\n\t" \
  "v_mfma_f32_32x32x16_bf16 v[208:223], v[124:127], v[112:115], v[208:223]\n\t" \
  "ds_read_b64_tr_b16 v[88:89], v57 offset:14336\n\t" \
  "ds_read_b64_tr_b16 v[90:91], v57 offset:16128\n\t" \
  "v_mfma_f32_32x32x16_bf16 v[176:191], v[128:131], v[108:111], v[176:191]\n\t" \
  "global_load_lds_dwordx4 v67, s[4:5] offset:3072\n\t" \
  "ds_read_b64 v[26:27], v24 offset:45056\n\t" \
  "ds_read_b64_tr_b16 v[32:33], v30 offset:45056\n\t" \
  "ds_read_b64_tr_b16 v[34:35], v30 offset:46848\n\t" \
  "s_add_u32 s33, s33, s35\n\t" \
  "s_add_u32 s33, s33, 1536\n\t" \
  "s_lshl3_add_u32 s33, s86, s33\n\t" \
  "s_add_u32 m0, s48, 0\n\t" \
  "v_mfma_f32_32x32x16_bf16 v[224:239], v[128:131], v[112:115], v[224:239]\n\t" \
  "global_load_lds_dwordx4 v68, s[6:7] offset:0\n\t" \
  "s_lshl_b32 s33, s33, 4\n\t" \
  "s_lshl1_add_u32 s33, s34, s33\n\t" \
  "s_add_u32 s33, s33, 131072\n\t" \
  "v_mfma_f32_32x32x16_bf16 v[240:255], v[116:119], v[108:111], v[240:255]\n\t" \
  "global_load_lds_dwordx4 v69, s[6:7] offset:1024\n\t" \
  "v_add_u32 v22, s33, v0\n\t" \
  "s_lshr_b32 s97, s52, 1\n\t" \
  "s_lshr_b32 s98, s89, 3\n\t" \
  "s_waitcnt lgkmcnt(3)\n\t" \
  "v_mfma_f32_32x32x16_bf16 v[144:159], v[92:95], v[80:83], v[144:159]\n\t" \
  "ds_read_b128 v[108:111], v51\n\t" \
  "ds_read_b64_tr_b16 v[120:121], v56 offset:21504\n\t" \
  "ds_read_b64_tr_b16 v[122:123], v56 offset:23296\n\t" \
  "v_mfma_f32_32x32x16_bf16 v[192:207], v[92:95], v[84:87], v[192:207]\n\t" \
  "ds_read_b128 v[112:115], v55\n\t" \
  "ds_read_b64_tr_b16 v[124:125], v56 offset:21568\n\t" \
  "ds_read_b64_tr_b16 v[126:127], v56 offset:23360\n\t" \
  "v_mfma_f32_32x32x16_bf16 v[160:175], v[96:99], v[80:83], v[160:175]\n\t" \
  "ds_read_b64_tr_b16 v[128:129], v56 offset:21632\n\t" \
  "ds_read_b64_tr_b16 v[130:131], v56 offset:23424\n\t" \
  "v_mfma_f32_32x32x16_bf16 v[208:223], v[96:99], v[84:87], v[208:223]\n\t" \
  "ds_read_b64_tr_b16 v[116:117], v57 offset:21504\n\t" \
  "ds_read_b64_tr_b16 v[118:119], v57 offset:23296\n\t" \
  "s_waitcnt lgkmcnt(10)\n\t" \
  "v_mfma_f32_16x16x32_bf16 v[36:39], v[40:43], v[32:35], v[36:39]\n\t" \
  "v_mfma_f32_32x32x16_bf16 v[176:191], v[100:103], v[80:83], v[176:191]\n\t" \
  "global_load_lds_dwordx4 v70, s[6:7] offset:2048\n\t" \
  "v_dot2c_f32_bf16 v28, s61, v26\n\t" \
  "v_dot2c_f32_bf16 v29, v26, v26\n\t" \
  "s_lshl1_add_u32 s96, s87, s90\n\t" \
  "s_lshl2_add_u32 s96, s96, s97\n\t" \
  "s_and_b32 s33, s88, 1\n\t" \
  "v_mfma_f32_32x32x16_bf16 v[224:239], v[100:103], v[84:87], v[224:239]\n\t" \
  "global_load_lds_dwordx4 v71, s[6:7] offset:3072\n\t" \
  "s_lshl1_add_u32 s99, s33, s91\n\t" \
  "s_lshl_b32 s33, s96, 7\n\t" \
  "s_add_u32 s33, s33, 196608\n\t" \
  "s_add_u32 m0, s48, 4096\n\t" \
  "v_mfma_f32_32x32x16_bf16 v[240:255], v[88:91], v[80:83], v[240:255]\n\t" \
  "global_load_lds_dwordx4 v72, s[6:7] offset:0\n\t" \
  "v_dot2c_f32_bf16 v28, s61, v27\n\t" \
  "v_dot2c_f32_bf16 v29, v27, v27\n\t" \
  "v_bfe_u32 v75, v0, 4, 3\n\t" \
  "v_lshl_add_u32 v13, v75, 3, s33\n\t" \
  "s_lshl3_add_u32 s33, s99, s33\n\t" \
  "s_waitcnt lgkmcnt(0)\n\t" \
  "v_mfma_f32_32x32x16_bf16 v[144:159], v[120:123], v[108:111], v[144:159]\n\t" \
  "global_load_lds_dwordx4 v73, s[6:7] offset:1024\n\t" \
  "v_add_u32 v48, s55, v48\n\t" \
  "v_add_u32 v49, s55, v49\n\t" \
  "v_add_u32 v50, s55, v50\n\t" \
  "v_add_u32 v51, s55, v51\n\t" \
  "v_add_u32 v52, s55, v52\n\t" \
  "v_mfma_f32_32x32x16_bf16 v[192:207], v[120:123], v[112:115], v[192:207]\n\t" \
  "global_load_lds_dwordx4 v74, s[6:7] offset:2048\n\t" \
  "v_add_u32 v53, s55, v53\n\t" \
  "v_add_u32 v54, s55, v54\n\t" \
  "v_add_u32 v55, s55, v55\n\t" \
  "v_add_u32 v56, s55, v56\n\t" \
  "v_add_u32 v57, s55, v57\n\t" \
  "v_mfma_f32_32x32x16_bf16 v[160:175], v[124:127], v[108:111], v[160:175]\n\t" \
  "s_waitcnt vmcnt(11)\n\t" \
  "s_waitcnt lgkmcnt(0)\n\t" \
  "s_barrier\n\t" \
  "ds_read_b128 v[80:83], v48\n\t" \
  "ds_read_b64_tr_b16 v[92:93], v56 offset:0\n\t" \
  "ds_read_b64_tr_b16 v[94:95], v56 offset:1792\n\t" \
  "v_mfma_f32_32x32x16_bf16 v[208:223], v[124:127], v[112:115], v[208:223]\n\t" \
  "ds_read_b128 v[84:87], v52\n\t" \
  "ds_read_b64_tr_b16 v[96:97], v56 offset:64\n\t" \
  "ds_read_b64_tr_b16 v[98:99], v56 offset:1856\n\t" \
  "v_mfma_f32_32x32x16_bf16 v[176:191], v[128:131], v[108:111], v[176:191]\n\t" \
  "ds_read_b64_tr_b16 v[100:101], v56 offset:128\n\t" \
  "ds_read_b64_tr_b16 v[102:103], v56 offset:1920\n\t" \
  "v_mfma_f32_32x32x16_bf16 v[224:239], v[128:131], v[112:115], v[224:239]\n\t" \
  "ds_read_b64_tr_b16 v[88:89], v57 offset:0\n\t" \
  "ds_read_b64_tr_b16 v[90:91], v57 offset:1792\n\t" \
  "v_mfma_f32_32x32x16_bf16 v[240:255], v[116:119], v[108:111], v[240:255]\n\t" \
  "s_add_u32 s33, s33, 32\n\t" \
  "v_mov_b32 v12, s33\n\t" \
  "s_lshl2_add_u32 s26, s87, s98\n\t" \
  "s_waitcnt lgkmcnt(7)\n\t" \
  "v_mfma_f32_32x32x16_bf16 v[144:159], v[92:95], v[80:83], v[144:159]\n\t" \
  "ds_read_b128 v[108:111], v49\n\t" \
  "ds_read_b64_tr_b16 v[120:121], v56 offset:7168\n\t" \
  "ds_read_b64_tr_b16 v[122:123], v56 offset:8960\n\t" \
  "s_xor_b32 s57, s85, 4\n\t" \
  "s_lshl_b32 s58, s57, 7\n\t" \
  "s_add_u32 s4, s40, s58\n\t" \
  "s_addc_u32 s5, s41, 0\n\t" \
  "s_waitcnt lgkmcnt(9)\n\t" \
  "v_mfma_f32_32x32x16_bf16 v[192:207], v[92:95], v[84:87], v[192:207]\n\t" \
  "ds_read_b128 v[112:115], v53\n\t" \
  "ds_read_b64_tr_b16 v[124:125], v56 offset:7232\n\t" \
  "ds_read_b64_tr_b16 v[126:127], v56 offset:9024\n\t" \
  "s_mul_i32 s58, s57, 229376\n\t" \
  "s_add_u32 s6, s42, s58\n\t" \
  "s_addc_u32 s7, s43, 0\n\t" \
  "s_waitcnt lgkmcnt(10)\n\t" \
  "v_mfma_f32_32x32x16_bf16 v[160:175], v[96:99], v[80:83], v[160:175]\n\t" \
  "ds_read_b64_tr_b16 v[128:129], v56 offset:7296\n\t" \
  "ds_read_b64_tr_b16 v[130:131], v56 offset:9088\n\t" \
  "v_mfma_f32_32x32x16_bf16 v[208:223], v[96:99], v[84:87], v[208:223]\n\t" \
  "ds_read_b64_tr_b16 v[116:117], v57 offset:7168\n\t" \
  "ds_read_b64_tr_b16 v[118:119], v57 offset:8960\n\t" \
  "s_add_u32 m0, s47, 45056\n\t" \
  "s_waitcnt lgkmcnt(12)\n\t" \
  "v_mfma_f32_32x32x16_bf16 v[176:191], v[100:103], v[80:83], v[176:191]\n\t" \
  "global_load_lds_dwordx4 v64, s[4:5] offset:0\n\t" \
  "s_lshl4_add_u32 s26, s26, s86\n\t" \
  "s_lshl2_add_u32 s27, s97, s26\n\t" \
  "s_lshl_b32 s27, s27, 6\n\t" \
  "v_mfma_f32_32x32x16_bf16 v[224:239], v[100:103], v[84:87], v[224:239]\n\t" \
  "global_load_lds_dwordx4 v65, s[4:5] offset:1024\n\t" \
  "s_add_u32 s27, s27, 10485760\n\t" \
  "s_lshl2_add_u32 s33, s84, s86\n\t" \
  "s_lshl_b32 s34, s33, 7\n\t" \
  "s_waitcnt lgkmcnt(10)\n\t" \
  "v_mfma_f32_32x32x16_bf16 v[240:255], v[88:91], v[80:83], v[240:255]\n\t" \
  "global_load_lds_dwordx4 v66, s[4:5] offset:2048\n\t" \
  "s_add_u32 s35, s34, 131072\n\t" \
  "s_min_u32 s35, s35, 184192\n\t" \
  "s_and_b32 s34, s34, 0xffff\n\t" \
  "s_waitcnt lgkmcnt(0)\n\t" \
  "v_mfma_f32_32x32x16_bf16 v[144:159], v[120:123], v[108:111], v[144:159]\n\t" \
  "ds_read_b128 v[80:83], v50\n\t" \
  "ds_read_b64_tr_b16 v[92:93], v56 offset:14336\n\t" \
  "ds_read_b64_tr_b16 v[94:95], v56 offset:16128\n\t" \
  "s_mov_b32 s55, 0xfffea000\n\t" \
  "v_mfma_f32_32x32x16_bf16 v[192:207], v[120:123], v[112:115], v[192:207]\n\t" \
  "ds_read_b128 v[84:87], v54\n\t" \
  "ds_read_b64_tr_b16 v[96:97], v56 offset:14400\n\t" \
  "ds_read_b64_tr_b16 v[98:99], v56 offset:16192\n\t" \
  "v_mfma_f32_32x32x16_bf16 v[160:175], v[124:127], v[108:111], v[160:175]\n\t" \
  "ds_read_b64_tr_b16 v[100:101], v56 offset:14464\n\t" \
  "ds_read_b64_tr_b16 v[102:103], v56 offset:16256\n\t" \
  "v_mfma_f32_32x32x16_bf16 v[208:223], v[124:127], v[112:115], v[208:223]\n\t" \
  "ds_read_b64_tr_b16 v[88:89], v57 offset:14336\n\t" \
  "ds_read_b64_tr_b16 v[90:91], v57 offset:16128\n\t" \
  "v_mfma_f32_32x32x16_bf16 v[176:191], v[128:131], v[108:111], v[176:191]\n\t" \
  "global_load_lds_dwordx4 v67, s[4:5] offset:3072\n\t" \
  "ds_read_b64 v[26:27], v25 offset:0\n\t" \
  "ds_read_b64_tr_b16 v[32:33], v31 offset:0\n\t" \
  "ds_read_b64_tr_b16 v[34:35], v31 offset:1792\n\t" \
  "s_add_u32 s34, s34, 8192\n\t" \
  "v_mov_b32 v58, s27\n\t" \
  "v_writelane_b32 v58, s34, 1\n\t" \
  "s_add_u32 m0, s48, 45056\n\t" \
  "v_mfma_f32_32x32x16_bf16 v[224:239], v[128:131], v[112:115], v[224:239]\n\t" \
  "global_load_lds_dwordx4 v68, s[6:7] offset:0\n\t" \
  "v_writelane_b32 v58, s35, 2\n\t" \
  "v_cmp_eq_u32 vcc, 48, v0\n\t" \
  "s_nop 1\n\t" \
  "v_cndmask_b32 v58, v58, v13, vcc\n\t" \
  "v_mfma_f32_32x32x16_bf16 v[240:255], v[116:119], v[108:111], v[240:255]\n\t" \
  "global_load_lds_dwordx4 v69, s[6:7] offset:1024\n\t" \
  "global_load_dword v141, v58, s[44:45] sc1\n\t" \
  "s_mul_i32 s33, s97, 458752\n\t" \
  "s_add_u32 s66, s66, s33\n\t" \
  "s_addc_u32 s67, s67, 0\n\t" \
  "s_waitcnt lgkmcnt(3)\n\t" \
  "v_mfma_f32_32x32x16_bf16 v[144:159], v[92:95], v[80:83], v[144:159]\n\t" \
  "ds_read_b128 v[108:111], v51\n\t" \
  "ds_read_b64_tr_b16 v[120:121], v56 offset:21504\n\t" \
  "ds_read_b64_tr_b16 v[122:123], v56 offset:23296\n\t" \
  "v_mfma_f32_32x32x16_bf16 v[192:207], v[92:95], v[84:87], v[192:207]\n\t" \
  "ds_read_b128 v[112:115], v55\n\t" \
  "ds_read_b64_tr_b16 v[124:125], v56 offset:21568\n\t" \
  "ds_read_b64_tr_b16 v[126:127], v56 offset:23360\n\t" \
  "v_mfma_f32_32x32x16_bf16 v[160:175], v[96:99], v[80:83], v[160:175]\n\t" \
  "ds_read_b64_tr_b16 v[128:129], v56 offset:21632\n\t" \
  "ds_read_b64_tr_b16 v[130:131], v56 offset:23424\n\t" \
  "v_mfma_f32_32x32x16_bf16 v[208:223], v[96:99], v[84:87], v[208:223]\n\t" \
  "ds_read_b64_tr_b16 v[116:117], v57 offset:21504\n\t" \
  "ds_read_b64_tr_b16 v[118:119], v57 offset:23296\n\t" \
  "s_waitcnt lgkmcnt(10)\n\t" \
  "v_mfma_f32_16x16x32_bf16 v[36:39], v[40:43], v[32:35], v[36:39]\n\t" \
  "v_mfma_f32_32x32x16_bf16 v[176:191], v[100:103], v[80:83], v[176:191]\n\t" \
  "global_load_lds_dwordx4 v70, s[6:7] offset:2048\n\t" \
  "v_dot2c_f32_bf16 v28, s61, v26\n\t" \
  "v_dot2c_f32_bf16 v29, v26, v26\n\t" \
  "s_lshr_b32 s19, s99, 1\n\t" \
  "s_xor_b32 s33, s19, 1\n\t" \
  "s_mul_i32 s33, s33, 229376\n\t" \
  "v_mfma_f32_32x32x16_bf16 v[224:239], v[100:103], v[84:87], v[224:239]\n\t" \
  "global_load_lds_dwordx4 v71, s[6:7] offset:3072\n\t" \
  "s_add_u32 s20, s66, s33\n\t" \
  "s_addc_u32 s21, s67, 0\n\t" \
  "s_mul_i32 s33, s19, 229376\n\t" \
  "s_add_u32 s66, s66, s33\n\t" \
  "s_addc_u32 s67, s67, 0\n\t" \
  "s_add_u32 m0, s48, 49152\n\t" \
  "v_mfma_f32_32x32x16_bf16 v[240:255], v[88:91], v[80:83], v[240:255]\n\t" \
  "global_load_lds_dwordx4 v72, s[6:7] offset:0\n\t" \
  "v_dot2c_f32_bf16 v28, s61, v27\n\t" \
  "v_dot2c_f32_bf16 v29, v27, v27\n\t" \
  "s_lshl_b32 s33, s97, 17\n\t" \
  "s_add_u32 s62, s62, s33\n\t" \
  "s_addc_u32 s63, s63, 0\n\t" \
  "v_lshrrev_b32 v75, 8, v0\n\t" \
  "s_waitcnt lgkmcnt(0)\n\t" \
  "v_mfma_f32_32x32x16_bf16 v[144:159], v[120:123], v[108:111], v[144:159]\n\t" \
  "global_load_lds_dwordx4 v73, s[6:7] offset:1024\n\t" \
  "v_add_u32 v48, s55, v48\n\t" \
  "v_add_u32 v49, s55, v49\n\t" \
  "v_add_u32 v50, s55, v50\n\t" \
  "v_add_u32 v51, s55, v51\n\t" \
  "v_add_u32 v52, s55, v52\n\t" \
  "v_mfma_f32_32x32x16_bf16 v[192:207], v[120:123], v[112:115], v[192:207]\n\t" \
  "global_load_lds_dwordx4 v74, s[6:7] offset:2048\n\t" \
  "v_add_u32 v53, s55, v53\n\t" \
  "v_add_u32 v54, s55, v54\n\t" \
  "v_add_u32 v55, s55, v55\n\t" \
  "v_add_u32 v56, s55, v56\n\t" \
  "v_add_u32 v57, s55, v57\n\t" \
  "v_mfma_f32_32x32x16_bf16 v[160:175], v[124:127], v[108:111], v[160:175]\n\t" \
  "s_waitcnt vmcnt(12)\n\t" \
  "s_waitcnt lgkmcnt(0)\n\t" \
  "s_barrier\n\t" \
  "ds_read_b128 v[80:83], v48\n\t" \
  "ds_read_b64_tr_b16 v[92:93], v56 offset:0\n\t" \
  "ds_read_b64_tr_b16 v[94:95], v56 offset:1792\n\t" \
  "v_mfma_f32_32x32x16_bf16 v[208:223], v[124:127], v[112:115], v[208:223]\n\t" \
  "ds_read_b128 v[84:87], v52\n\t" \
  "ds_read_b64_tr_b16 v[96:97], v56 offset:64\n\t" \
  "ds_read_b64_tr_b16 v[98:99], v56 offset:1856\n\t" \
  "v_mfma_f32_32x32x16_bf16 v[176:191], v[128:131], v[108:111], v[176:191]\n\t" \
  "ds_read_b64_tr_b16 v[100:101], v56 offset:128\n\t" \
  "ds_read_b64_tr_b16 v[102:103], v56 offset:1920\n\t" \
  "v_mfma_f32_32x32x16_bf16 v[224:239], v[128:131], v[112:115], v[224:239]\n\t" \
  "ds_read_b64_tr_b16 v[88:89], v57 offset:0\n\t" \
  "ds_read_b64_tr_b16 v[90:91], v57 offset:1792\n\t" \
  "v_mfma_f32_32x32x16_bf16 v[240:255], v[116:119], v[108:111], v[240:255]\n\t" \
  "v_and_b32 v78, 0xf0, v0\n\t" \
  "v_lshlrev_b32 v79, 6, v75\n\t" \
  "v_xor_b32 v78, v78, v79\n\t" \
  "s_waitcnt lgkmcnt(7)\n\t" \
  "v_mfma_f32_32x32x16_bf16 v[144:159], v[92:95], v[80:83], v[144:159]\n\t" \
  "ds_read_b128 v[108:111], v49\n\t" \
  "ds_read_b64_tr_b16 v[120:121], v56 offset:7168\n\t" \
  "ds_read_b64_tr_b16 v[122:123], v56 offset:8960\n\t" \
  "s_xor_b32 s57, s85, 5\n\t" \
  "s_lshl_b32 s58, s57, 7\n\t" \
  "s_add_u32 s4, s40, s58\n\t" \
  "s_addc_u32 s5, s41, 0\n\t" \
  "s_waitcnt lgkmcnt(9)\n\t" \
  "v_mfma_f32_32x32x16_bf16 v[192:207], v[92:95], v[84:87], v[192:207]\n\t" \
  "ds_read_b128 v[112:115], v53\n\t" \
  "ds_read_b64_tr_b16 v[124:125], v56 offset:7232\n\t" \
  "ds_read_b64_tr_b16 v[126:127], v56 offset:9024\n\t" \
  "s_mul_i32 s58, s57, 229376\n\t" \
  "s_add_u32 s6, s42, s58\n\t" \
  "s_addc_u32 s7, s43, 0\n\t" \
  "s_waitcnt lgkmcnt(10)\n\t" \
  "v_mfma_f32_32x32x16_bf16 v[160:175], v[96:99], v[80:83], v[160:175]\n\t" \
  "ds_read_b64_tr_b16 v[128:129], v56 offset:7296\n\t" \
  "ds_read_b64_tr_b16 v[130:131], v56 offset:9088\n\t" \
  "v_mfma_f32_32x32x16_bf16 v[208:223], v[96:99], v[84:87], v[208:223]\n\t" \
  "ds_read_b64_tr_b16 v[116:117], v57 offset:7168\n\t" \
  "ds_read_b64_tr_b16 v[118:119], v57 offset:8960\n\t" \
  "s_add_u32 m0, s47, 90112\n\t" \
  "s_waitcnt lgkmcnt(12)\n\t" \
  "v_mfma_f32_32x32x16_bf16 v[176:191], v[100:103], v[80:83], v[176:191]\n\t" \
  "global_load_lds_dwordx4 v64, s[4:5] offset:0\n\t" \
  "v_lshl_add_u32 v78, v75, 10, v78\n\t" \
  "s_lshl_b32 s33, s86, 13\n\t" \
  "s_lshl_b32 s34, s98, 8\n\t" \
  "v_mfma_f32_32x32x16_bf16 v[224:239], v[100:103], v[84:87], v[224:239]\n\t" \
  "global_load_lds_dwordx4 v65, s[4:5] offset:1024\n\t" \
  "s_add_u32 s33, s33, s34\n\t" \
  "v_add_u32 v76, s33, v78\n\t" \
  "v_add_u32 v77, 3072, v76\n\t" \
  "s_waitcnt lgkmcnt(10)\n\t" \
  "v_mfma_f32_32x32x16_bf16 v[240:255], v[88:91], v[80:83], v[240:255]\n\t" \
  "global_load_lds_dwordx4 v66, s[4:5] offset:2048\n\t" \
  "s_lshl_b32 s83, s86, 11\n\t" \
  "s_lshl_b32 s33, s86, 8\n\t" \
  "s_add_u32 s33, s33, 157696\n\t" \
  "s_waitcnt lgkmcnt(0)\n\t" \
  "v_mfma_f32_32x32x16_bf16 v[144:159], v[120:123], v[108:111], v[144:159]\n\t" \
  "ds_read_b128 v[80:83], v50\n\t" \
  "ds_read_b64_tr_b16 v[92:93], v56 offset:14336\n\t" \
  "ds_read_b64_tr_b16 v[94:95], v56 offset:16128\n\t" \
  "s_mov_b32 s55, 0xb000\n\t" \
  "v_mfma_f32_32x32x16_bf16 v[192:207], v[120:123], v[112:115], v[192:207]\n\t" \
  "ds_read_b128 v[84:87], v54\n\t" \
  "ds_read_b64_tr_b16 v[96:97], v56 offset:14400\n\t" \
  "ds_read_b64_tr_b16 v[98:99], v56 offset:16192\n\t" \
  "v_mfma_f32_32x32x16_bf16 v[160:175], v[124:127], v[108:111], v[160:175]\n\t" \
  "ds_read_b64_tr_b16 v[100:101], v56 offset:14464\n\t" \
  "ds_read_b64_tr_b16 v[102:103], v56 offset:16256\n\t" \
  "v_mfma_f32_32x32x16_bf16 v[208:223], v[124:127], v[112:115], v[208:223]\n\t" \
  "ds_read_b64_tr_b16 v[88:89], v57 offset:14336\n\t" \
  "ds_read_b64_tr_b16 v[90:91], v57 offset:16128\n\t" \
  "v_mfma_f32_32x32x16_bf16 v[176:191], v[128:131], v[108:111], v[176:191]\n\t" \
  "global_load_lds_dwordx4 v67, s[4:5] offset:3072\n\t" \
  "ds_read_b64 v[26:27], v24 offset:0\n\t" \
  "ds_read_b64_tr_b16 v[32:33], v30 offset:0\n\t" \
  "ds_read_b64_tr_b16 v[34:35], v30 offset:1792\n\t" \
  "v_lshrrev_b32 v75, 2, v0\n\t" \
  "v_add_u32 v63, s33, v75\n\t" \
  "s_lshl1_add_u32 s33, s87, s90\n\t" \
  "s_add_u32 m0, s48, 90112\n\t" \
  "v_mfma_f32_32x32x16_bf16 v[224:239], v[128:131], v[112:115], v[224:239]\n\t" \
  "global_load_lds_dwordx4 v68, s[6:7] offset:0\n\t" \
  "s_lshl_b32 s33, s33, 11\n\t" \
  "s_add_u32 s33, s33, 8192\n\t" \
  "v_and_b32 v75, 0x1f0, v0\n\t" \
  "v_mfma_f32_32x32x16_bf16 v[240:255], v[116:119], v[108:111], v[240:255]\n\t" \
  "global_load_lds_dwordx4 v69, s[6:7] offset:1024\n\t" \
  "v_lshl_add_u32 v60, v75, 1, s33\n\t" \
  "s_lshl_b32 s33, s91, 10\n\t" \
  "v_add_u32 v60, s33, v60\n\t" \
  "s_waitcnt lgkmcnt(3)\n\t" \
  "v_mfma_f32_32x32x16_bf16 v[144:159], v[92:95], v[80:83], v[144:159]\n\t" \
  "ds_read_b128 v[108:111], v51\n\t" \
  "ds_read_b64_tr_b16 v[120:121], v56 offset:21504\n\t" \
  "ds_read_b64_tr_b16 v[122:123], v56 offset:23296\n\t" \
  "v_mfma_f32_32x32x16_bf16 v[192:207], v[92:95], v[84:87], v[192:207]\n\t" \
  "ds_read_b128 v[112:115], v55\n\t" \
  "ds_read_b64_tr_b16 v[124:125], v56 offset:21568\n\t" \
  "ds_read_b64_tr_b16 v[126:127], v56 offset:23360\n\t" \
  "v_mfma_f32_32x32x16_bf16 v[160:175], v[96:99], v[80:83], v[160:175]\n\t" \
  "ds_read_b64_tr_b16 v[128:129], v56 offset:21632\n\t" \
  "ds_read_b64_tr_b16 v[130:131], v56 offset:23424\n\t" \
  "v_mfma_f32_32x32x16_bf16 v[208:223], v[96:99], v[84:87], v[208:223]\n\t" \
  "ds_read_b64_tr_b16 v[116:117], v57 offset:21504\n\t" \
  "ds_read_b64_tr_b16 v[118:119], v57 offset:23296\n\t" \
  "s_waitcnt lgkmcnt(10)\n\t" \
  "v_mfma_f32_16x16x32_bf16 v[36:39], v[40:43], v[32:35], v[36:39]\n\t" \
  "v_mfma_f32_32x32x16_bf16 v[176:191], v[100:103], v[80:83], v[176:191]\n\t" \
  "global_load_lds_dwordx4 v70, s[6:7] offset:2048\n\t" \
  "v_dot2c_f32_bf16 v28, s61, v26\n\t" \
  "v_dot2c_f32_bf16 v29, v26, v26\n\t" \
  "v_xor_b32 v61, 1024, v60\n\t" \
  "s_lshl_b32 s33, s86, 10\n\t" \
  "v_add_u32 v62, s33, v0\n\t" \
  "v_mfma_f32_32x32x16_bf16 v[224:239], v[100:103], v[84:87], v[224:239]\n\t" \
  "global_load_lds_dwordx4 v71, s[6:7] offset:3072\n\t" \
  "v_min_u32 v62, 3568, v62\n\t" \
  "s_mul_i32 s33, s88, 3584\n\t" \
  "s_add_u32 s33, s33, 155648\n\t" \
  "s_add_u32 m0, s48, 94208\n\t" \
  "v_mfma_f32_32x32x16_bf16 v[240:255], v[88:91], v[80:83], v[240:255]\n\t" \
  "global_load_lds_dwordx4 v72, s[6:7] offset:0\n\t" \
  "v_dot2c_f32_bf16 v28, s61, v27\n\t" \
  "v_dot2c_f32_bf16 v29, v27, v27\n\t" \
  "v_add_u32 v62, s33, v62\n\t" \
  "v_readfirstlane_b32 s68, v138\n\t" \
  "v_readfirstlane_b32 s69, v139\n\t" \
  "s_nop 0\n\t" \
  "s_add_u32 s14, s68, 1\n\t" \
  "s_addc_u32 s15, s69, 0\n\t" \
  "s_mov_b32 s49, s14\n\t" \
  "s_waitcnt lgkmcnt(0)\n\t" \
  "v_mfma_f32_32x32x16_bf16 v[144:159], v[120:123], v[108:111], v[144:159]\n\t" \
  "global_load_lds_dwordx4 v73, s[6:7] offset:1024\n\t" \
  "v_add_u32 v48, s55, v48\n\t" \
  "v_add_u32 v49, s55, v49\n\t" \
  "v_add_u32 v50, s55, v50\n\t" \
  "v_add_u32 v51, s55, v51\n\t" \
  "v_add_u32 v52, s55, v52\n\t" \
  "v_mfma_f32_32x32x16_bf16 v[192:207], v[120:123], v[112:115], v[192:207]\n\t" \
  "global_load_lds_dwordx4 v74, s[6:7] offset:2048\n\t" \
  "v_add_u32 v53, s55, v53\n\t" \
  "v_add_u32 v54, s55, v54\n\t" \
  "v_add_u32 v55, s55, v55\n\t" \
  "v_add_u32 v56, s55, v56\n\t" \
  "v_add_u32 v57, s55, v57\n\t" \
  "ds_write2_b32 v143, v36, v37 offset1:16\n\t" \
  "v_mfma_f32_32x32x16_bf16 v[160:175], v[124:127], v[108:111], v[160:175]\n\t" \
  "s_waitcnt vmcnt(11)\n\t" \
  "s_waitcnt lgkmcnt(0)\n\t" \
  "s_barrier\n\t" \
  "ds_read_b128 v[80:83], v48\n\t" \
  "ds_read_b64_tr_b16 v[92:93], v56 offset:0\n\t" \
  "ds_read_b64_tr_b16 v[94:95], v56 offset:1792\n\t" \
  "v_mfma_f32_32x32x16_bf16 v[208:223], v[124:127], v[112:115], v[208:223]\n\t" \
  "ds_read_b128 v[84:87], v52\n\t" \
  "ds_read_b64_tr_b16 v[96:97], v56 offset:64\n\t" \
  "ds_read_b64_tr_b16 v[98:99], v56 offset:1856\n\t" \
  "v_mfma_f32_32x32x16_bf16 v[176:191], v[128:131], v[108:111], v[176:191]\n\t" \
  "ds_read_b64_tr_b16 v[100:101], v56 offset:128\n\t" \
  "ds_read_b64_tr_b16 v[102:103], v56 offset:1920\n\t" \
  "v_mfma_f32_32x32x16_bf16 v[224:239], v[128:131], v[112:115], v[224:239]\n\t" \
  "ds_read_b64_tr_b16 v[88:89], v57 offset:0\n\t" \
  "ds_read_b64_tr_b16 v[90:91], v57 offset:1792\n\t" \
  "v_mfma_f32_32x32x16_bf16 v[240:255], v[116:119], v[108:111], v[240:255]\n\t" \
  "s_lshl_b64 s[12:13], s[14:15], 3\n\t" \
  "s_or_b32 s68, s12, 4\n\t" \
  "s_mov_b32 s69, s13\n\t" \
  "v_mov_b32 v138, s12\n\t" \
  "v_mov_b32 v139, s13\n\t" \
  "global_load_dword v141, v[136:137], off\n\t" \
  "s_waitcnt lgkmcnt(7)\n\t" \
  "v_mfma_f32_32x32x16_bf16 v[144:159], v[92:95], v[80:83], v[144:159]\n\t" \
  "ds_read_b128 v[108:111], v49\n\t" \
  "ds_read_b64_tr_b16 v[120:121], v56 offset:7168\n\t" \
  "ds_read_b64_tr_b16 v[122:123], v56 offset:8960\n\t" \
  "s_xor_b32 s57, s85, 6\n\t" \
  "s_lshl_b32 s58, s57, 7\n\t" \
  "s_add_u32 s4, s40, s58\n\t" \
  "s_addc_u32 s5, s41, 0\n\t" \
  "ds_read_b32 v32, v23\n\t" \
  "ds_read_b32 v33, v23 offset:128\n\t" \
  "ds_read_b32 v34, v23 offset:256\n\t" \
  "ds_read_b32 v35, v23 offset:384\n\t" \
  "s_waitcnt lgkmcnt(13)\n\t" \
  "v_mfma_f32_32x32x16_bf16 v[192:207], v[92:95], v[84:87], v[192:207]\n\t" \
  "ds_read_b128 v[112:115], v53\n\t" \
  "ds_read_b64_tr_b16 v[124:125], v56 offset:7232\n\t" \
  "ds_read_b64_tr_b16 v[126:127], v56 offset:9024\n\t" \
  "s_mul_i32 s58, s57, 229376\n\t" \
  "s_add_u32 s6, s42, s58\n\t" \
  "s_addc_u32 s7, s43, 0\n\t" \
  "v_mov_b32 v26, s49\n\t" \
  "s_mov_b32 s60, 0xaaaaaaaa\n\t" \
  "s_mov_b32 s61, 0xaaaaaaaa\n\t" \
  "s_waitcnt lgkmcnt(14)\n\t" \
  "v_mfma_f32_32x32x16_bf16 v[160:175], v[96:99], v[80:83], v[160:175]\n\t" \
  "ds_read_b64_tr_b16 v[128:129], v56 offset:7296\n\t" \
  "ds_read_b64_tr_b16 v[130:131], v56 offset:9088\n\t" \
  "v_add_f32_dpp v28, v28, v28 quad_perm:[1,0,3,2] row_mask:0xf bank_mask:0xf\n\t" \
  "v_add_f32_dpp v29, v29, v29 quad_perm:[1,0,3,2] row_mask:0xf bank_mask:0xf\n\t" \
  "v_mfma_f32_32x32x16_bf16 v[208:223], v[96:99], v[84:87], v[208:223]\n\t" \
  "ds_read_b64_tr_b16 v[116:117], v57 offset:7168\n\t" \
  "ds_read_b64_tr_b16 v[118:119], v57 offset:8960\n\t" \
  "v_add_f32_dpp v28, v28, v28 quad_perm:[2,3,0,1] row_mask:0xf bank_mask:0xf\n\t" \
  "v_add_f32_dpp v29, v29, v29 quad_perm:[2,3,0,1] row_mask:0xf bank_mask:0xf\n\t" \
  "s_add_u32 m0, s47, 0\n\t" \
  "s_waitcnt lgkmcnt(14)\n\t" \
  "v_mfma_f32_32x32x16_bf16 v[176:191], v[100:103], v[80:83], v[176:191]\n\t" \
  "global_load_lds_dwordx4 v64, s[4:5] offset:0\n\t" \
  "s_waitcnt lgkmcnt(9)\n\t" \
  "v_add_f32 v32, v32, v33\n\t" \
  "s_waitcnt lgkmcnt(7)\n\t" \
  "v_add_f32 v34, v34, v35\n\t" \
  "v_mov_b32 v33, s49\n\t" \
  "v_add_f32 v32, v32, v34\n\t" \
  "v_mfma_f32_32x32x16_bf16 v[224:239], v[100:103], v[84:87], v[224:239]\n\t" \
  "global_load_lds_dwordx4 v65, s[4:5] offset:1024\n\t" \
  "v_mfma_f32_32x32x16_bf16 v[240:255], v[88:91], v[80:83], v[240:255]\n\t" \
  "global_load_lds_dwordx4 v66, s[4:5] offset:2048\n\t" \
  "s_mov_b64 exec, 0xff\n\t" \
  "global_store_dwordx2 v22, v[32:33], s[44:45] sc1\n\t" \
  "s_mov_b64 exec, -1\n\t" \
  "s_waitcnt lgkmcnt(0)\n\t" \
  "v_mfma_f32_32x32x16_bf16 v[144:159], v[120:123], v[108:111], v[144:159]\n\t" \
  "ds_read_b128 v[80:83], v50\n\t" \
  "ds_read_b64_tr_b16 v[92:93], v56 offset:14336\n\t" \
  "ds_read_b64_tr_b16 v[94:95], v56 offset:16128\n\t" \
  "s_mov_b32 s55, 0xb000\n\t" \
  "v_mfma_f32_32x32x16_bf16 v[192:207], v[120:123], v[112:115], v[192:207]\n\t" \
  "ds_read_b128 v[84:87], v54\n\t" \
  "ds_read_b64_tr_b16 v[96:97], v56 offset:14400\n\t" \
  "ds_read_b64_tr_b16 v[98:99], v56 offset:16192\n\t" \
  "v_mfma_f32_32x32x16_bf16 v[160:175], v[124:127], v[108:111], v[160:175]\n\t" \
  "ds_read_b64_tr_b16 v[100:101], v56 offset:14464\n\t" \
  "ds_read_b64_tr_b16 v[102:103], v56 offset:16256\n\t" \
  "v_add_f32_dpp v28, v28, v28 row_half_mirror row_mask:0xf bank_mask:0xf\n\t" \
  "v_add_f32_dpp v29, v29, v29 row_half_mirror row_mask:0xf bank_mask:0xf\n\t" \
  "v_mfma_f32_32x32x16_bf16 v[208:223], v[124:127], v[112:115], v[208:223]\n\t" \
  "ds_read_b64_tr_b16 v[88:89], v57 offset:14336\n\t" \
  "ds_read_b64_tr_b16 v[90:91], v57 offset:16128\n\t" \
  "v_add_f32_dpp v28, v28, v28 row_mirror row_mask:0xf bank_mask:0xf\n\t" \
  "v_add_f32_dpp v29, v29, v29 row_mirror row_mask:0xf bank_mask:0xf\n\t" \
  "v_mfma_f32_32x32x16_bf16 v[176:191], v[128:131], v[108:111], v[176:191]\n\t" \
  "global_load_lds_dwordx4 v67, s[4:5] offset:3072\n\t" \
  "v_cndmask_b32 v27, v28, v29, s[60:61]\n\t" \
  "s_mov_b32 s60, 0x00030003\n\t" \
  "s_mov_b32 s61, 0x00030003\n\t" \
  "s_add_u32 m0, s48, 0\n\t" \
  "v_mfma_f32_32x32x16_bf16 v[224:239], v[128:131], v[112:115], v[224:239]\n\t" \
  "global_load_lds_dwordx4 v68, s[6:7] offset:0\n\t" \
  "s_mov_b64 exec, s[60:61]\n\t" \
  "global_store_dwordx2 v142, v[26:27], s[44:45] sc1\n\t" \
  "s_mov_b64 exec, -1\n\t" \
  "v_mfma_f32_32x32x16_bf16 v[240:255], v[116:119], v[108:111], v[240:255]\n\t" \
  "global_load_lds_dwordx4 v69, s[6:7] offset:1024\n\t" \
  "s_waitcnt lgkmcnt(0)\n\t" \
  "v_mfma_f32_32x32x16_bf16 v[144:159], v[92:95], v[80:83], v[144:159]\n\t" \
  "ds_read_b128 v[108:111], v51\n\t" \
  "ds_read_b64_tr_b16 v[120:121], v56 offset:21504\n\t" \
  "ds_read_b64_tr_b16 v[122:123], v56 offset:23296\n\t" \
  "v_mfma_f32_32x32x16_bf16 v[192:207], v[92:95], v[84:87], v[192:207]\n\t" \
  "ds_read_b128 v[112:115], v55\n\t" \
  "ds_read_b64_tr_b16 v[124:125], v56 offset:21568\n\t" \
  "ds_read_b64_tr_b16 v[126:127], v56 offset:23360\n\t" \
  "v_mfma_f32_32x32x16_bf16 v[160:175], v[96:99], v[80:83], v[160:175]\n\t" \
  "ds_read_b64_tr_b16 v[128:129], v56 offset:21632\n\t" \
  "ds_read_b64_tr_b16 v[130:131], v56 offset:23424\n\t" \
  "v_mfma_f32_32x32x16_bf16 v[208:223], v[96:99], v[84:87], v[208:223]\n\t" \
  "ds_read_b64_tr_b16 v[116:117], v57 offset:21504\n\t" \
  "ds_read_b64_tr_b16 v[118:119], v57 offset:23296\n\t" \
  "v_mfma_f32_32x32x16_bf16 v[176:191], v[100:103], v[80:83], v[176:191]\n\t" \
  "global_load_lds_dwordx4 v70, s[6:7] offset:2048\n\t" \
  "v_bfe_u32 v78, v0, 6, 2\n\t" \
  "v_lshrrev_b32 v79, 9, v0\n\t" \
  "v_lshl_add_u32 v75, v79, 2, v78\n\t" \
  "v_bfe_u32 v58, v0, 4, 2\n\t" \
  "v_lshlrev_b32 v58, 3, v58\n\t" \
  "v_mfma_f32_32x32x16_bf16 v[224:239], v[100:103], v[84:87], v[224:239]\n\t" \
  "global_load_lds_dwordx4 v71, s[6:7] offset:3072\n\t" \
  "v_lshl_add_u32 v75, v75, 8, v58\n\t" \
  "v_bfe_u32 v58, v0, 8, 1\n\t" \
  "v_lshl_add_u32 v75, v58, 5, v75\n\t" \
  "s_add_u32 s33, s97, 1\n\t" \
  "s_add_u32 m0, s48, 4096\n\t" \
  "v_mfma_f32_32x32x16_bf16 v[240:255], v[88:91], v[80:83], v[240:255]\n\t" \
  "global_load_lds_dwordx4 v72, s[6:7] offset:0\n\t" \
  "s_and_b32 s33, s33, 3\n\t" \
  "v_xor_b32 v58, s33, v78\n\t" \
  "v_lshl_add_u32 v10, v58, 6, v75\n\t" \
  "v_add_u32 v1, 90112, v10\n\t" \
  "v_add_u32 v5, 24576, v10\n\t" \
  "s_waitcnt lgkmcnt(0)\n\t" \
  "v_mfma_f32_32x32x16_bf16 v[144:159], v[120:123], v[108:111], v[144:159]\n\t" \
  "global_load_lds_dwordx4 v73, s[6:7] offset:1024\n\t" \
  "v_add_u32 v48, s55, v48\n\t" \
  "v_add_u32 v49, s55, v49\n\t" \
  "v_add_u32 v50, s55, v50\n\t" \
  "v_add_u32 v51, s55, v51\n\t" \
  "v_add_u32 v52, s55, v52\n\t" \
  "v_mfma_f32_32x32x16_bf16 v[192:207], v[120:123], v[112:115], v[192:207]\n\t" \
  "global_load_lds_dwordx4 v74, s[6:7] offset:2048\n\t" \
  "v_add_u32 v53, s55, v53\n\t" \
  "v_add_u32 v54, s55, v54\n\t" \
  "v_add_u32 v55, s55, v55\n\t" \
  "v_add_u32 v56, s55, v56\n\t" \
  "v_add_u32 v57, s55, v57\n\t" \
  "v_mfma_f32_32x32x16_bf16 v[160:175], v[124:127], v[108:111], v[160:175]\n\t" \
  "s_waitcnt vmcnt(14)\n\t" \
  "s_waitcnt lgkmcnt(0)\n\t" \
  "s_barrier\n\t" \
  "ds_read_b128 v[80:83], v48\n\t" \
  "ds_read_b64_tr_b16 v[92:93], v56 offset:0\n\t" \
  "ds_read_b64_tr_b16 v[94:95], v56 offset:1792\n\t" \
  "v_mfma_f32_32x32x16_bf16 v[208:223], v[124:127], v[112:115], v[208:223]\n\t" \
  "ds_read_b128 v[84:87], v52\n\t" \
  "ds_read_b64_tr_b16 v[96:97], v56 offset:64\n\t" \
  "ds_read_b64_tr_b16 v[98:99], v56 offset:1856\n\t" \
  "v_mfma_f32_32x32x16_bf16 v[176:191], v[128:131], v[108:111], v[176:191]\n\t" \
  "ds_read_b64_tr_b16 v[100:101], v56 offset:128\n\t" \
  "ds_read_b64_tr_b16 v[102:103], v56 offset:1920\n\t" \
  "v_mfma_f32_32x32x16_bf16 v[224:239], v[128:131], v[112:115], v[224:239]\n\t" \
  "ds_read_b64_tr_b16 v[88:89], v57 offset:0\n\t" \
  "ds_read_b64_tr_b16 v[90:91], v57 offset:1792\n\t" \
  "v_mfma_f32_32x32x16_bf16 v[240:255], v[116:119], v[108:111], v[240:255]\n\t" \
  "s_add_u32 s33, s97, 2\n\t" \
  "s_and_b32 s33, s33, 3\n\t" \
  "v_xor_b32 v58, s33, v78\n\t" \
  "v_lshl_add_u32 v11, v58, 6, v75\n\t" \
  "s_waitcnt lgkmcnt(7)\n\t" \
  "v_mfma_f32_32x32x16_bf16 v[144:159], v[92:95], v[80:83], v[144:159]\n\t" \
  "ds_read_b128 v[108:111], v49\n\t" \
  "ds_read_b64_tr_b16 v[120:121], v56 offset:7168\n\t" \
  "ds_read_b64_tr_b16 v[122:123], v56 offset:8960\n\t" \
  "s_xor_b32 s57, s85, 7\n\t" \
  "s_lshl_b32 s58, s57, 7\n\t" \
  "s_add_u32 s4, s40, s58\n\t" \
  "s_addc_u32 s5, s41, 0\n\t" \
  "s_waitcnt lgkmcnt(9)\n\t" \
  "v_mfma_f32_32x32x16_bf16 v[192:207], v[92:95], v[84:87], v[192:207]\n\t" \
  "ds_read_b128 v[112:115], v53\n\t" \
  "ds_read_b64_tr_b16 v[124:125], v56 offset:7232\n\t" \
  "ds_read_b64_tr_b16 v[126:127], v56 offset:9024\n\t" \
  "s_mul_i32 s58, s57, 229376\n\t" \
  "s_add_u32 s6, s42, s58\n\t" \
  "s_addc_u32 s7, s43, 0\n\t" \
  "s_waitcnt lgkmcnt(10)\n\t" \
  "v_mfma_f32_32x32x16_bf16 v[160:175], v[96:99], v[80:83], v[160:175]\n\t" \
  "ds_read_b64_tr_b16 v[128:129], v56 offset:7296\n\t" \
  "ds_read_b64_tr_b16 v[130:131], v56 offset:9088\n\t" \
  "v_mfma_f32_32x32x16_bf16 v[208:223], v[96:99], v[84:87], v[208:223]\n\t" \
  "ds_read_b64_tr_b16 v[116:117], v57 offset:7168\n\t" \
  "ds_read_b64_tr_b16 v[118:119], v57 offset:8960\n\t" \
  "s_add_u32 m0, s47, 45056\n\t" \
  "s_waitcnt lgkmcnt(12)\n\t" \
  "v_mfma_f32_32x32x16_bf16 v[176:191], v[100:103], v[80:83], v[176:191]\n\t" \
  "global_load_lds_dwordx4 v64, s[4:5] offset:0\n\t" \
  "v_add_u32 v2, 90112, v11\n\t" \
  "v_add_u32 v6, 24576, v11\n\t" \
  "s_add_u32 s33, s97, 3\n\t" \
  "s_and_b32 s33, s33, 3\n\t" \
  "v_mfma_f32_32x32x16_bf16 v[224:239], v[100:103], v[84:87], v[224:239]\n\t" \
  "global_load_lds_dwordx4 v65, s[4:5] offset:1024\n\t" \
  "v_xor_b32 v58, s33, v78\n\t" \
  "v_lshl_add_u32 v15, v58, 6, v75\n\t" \
  "v_add_u32 v3, 90112, v15\n\t" \
  "v_add_u32 v7, 24576, v15\n\t" \
  "s_add_u32 s33, s97, 4\n\t" \
  "s_waitcnt lgkmcnt(10)\n\t" \
  "v_mfma_f32_32x32x16_bf16 v[240:255], v[88:91], v[80:83], v[240:255]\n\t" \
  "global_load_lds_dwordx4 v66, s[4:5] offset:2048\n\t" \
  "s_and_b32 s33, s33, 3\n\t" \
  "v_xor_b32 v58, s33, v78\n\t" \
  "v_lshl_add_u32 v16, v58, 6, v75\n\t" \
  "v_add_u32 v4, 90112, v16\n\t" \
  "s_waitcnt lgkmcnt(0)\n\t" \
  "v_mfma_f32_32x32x16_bf16 v[144:159], v[120:123], v[108:111], v[144:159]\n\t" \
  "ds_read_b128 v[80:83], v50\n\t" \
  "ds_read_b64_tr_b16 v[92:93], v56 offset:14336\n\t" \
  "ds_read_b64_tr_b16 v[94:95], v56 offset:16128\n\t" \
  "s_mov_b32 s55, 0xfffea000\n\t" \
  "v_mfma_f32_32x32x16_bf16 v[192:207], v[120:123], v[112:115], v[192:207]\n\t" \
  "ds_read_b128 v[84:87], v54\n\t" \
  "ds_read_b64_tr_b16 v[96:97], v56 offset:14400\n\t" \
  "ds_read_b64_tr_b16 v[98:99], v56 offset:16192\n\t" \
  "v_mfma_f32_32x32x16_bf16 v[160:175], v[124:127], v[108:111], v[160:175]\n\t" \
  "ds_read_b64_tr_b16 v[100:101], v56 offset:14464\n\t" \
  "ds_read_b64_tr_b16 v[102:103], v56 offset:16256\n\t" \
  "v_mfma_f32_32x32x16_bf16 v[208:223], v[124:127], v[112:115], v[208:223]\n\t" \
  "ds_read_b64_tr_b16 v[88:89], v57 offset:14336\n\t" \
  "ds_read_b64_tr_b16 v[90:91], v57 offset:16128\n\t" \
  "v_mfma_f32_32x32x16_bf16 v[176:191], v[128:131], v[108:111], v[176:191]\n\t" \
  "global_load_lds_dwordx4 v67, s[4:5] offset:3072\n\t" \
  "v_add_u32 v8, 24576, v16\n\t" \
  "v_lshrrev_b32 v79, 9, v0\n\t" \
  "s_lshl2_add_u32 s34, s87, s86\n\t" \
  "s_lshl2_add_u32 s33, s34, s97\n\t" \
  "s_add_u32 m0, s48, 45056\n\t" \
  "v_mfma_f32_32x32x16_bf16 v[224:239], v[128:131], v[112:115], v[224:239]\n\t" \
  "global_load_lds_dwordx4 v68, s[6:7] offset:0\n\t" \
  "s_mul_i32 s33, s33, 36864\n\t" \
  "s_add_u32 s33, s33, 262144\n\t" \
  "s_add_u32 s74, s44, s33\n\t" \
  "s_addc_u32 s75, s45, 0\n\t" \
  "s_lshl_b32 s33, s96, 1\n\t" \
  "s_sub_u32 s33, s33, s97\n\t" \
  "v_mfma_f32_32x32x16_bf16 v[240:255], v[116:119], v[108:111], v[240:255]\n\t" \
  "global_load_lds_dwordx4 v69, s[6:7] offset:1024\n\t" \
  "s_mul_i32 s33, s33, 36864\n\t" \
  "s_add_u32 s33, s33, 262144\n\t" \
  "s_mul_i32 s34, s91, 147456\n\t" \
  "s_add_u32 s34, s33, s34\n\t" \
  "s_waitcnt lgkmcnt(0)\n\t" \
  "v_mfma_f32_32x32x16_bf16 v[144:159], v[92:95], v[80:83], v[144:159]\n\t" \
  "ds_read_b128 v[108:111], v51\n\t" \
  "ds_read_b64_tr_b16 v[120:121], v56 offset:21504\n\t" \
  "ds_read_b64_tr_b16 v[122:123], v56 offset:23296\n\t" \
  "v_mfma_f32_32x32x16_bf16 v[192:207], v[92:95], v[84:87], v[192:207]\n\t" \
  "ds_read_b128 v[112:115], v55\n\t" \
  "ds_read_b64_tr_b16 v[124:125], v56 offset:21568\n\t" \
  "ds_read_b64_tr_b16 v[126:127], v56 offset:23360\n\t" \
  "v_mfma_f32_32x32x16_bf16 v[160:175], v[96:99], v[80:83], v[160:175]\n\t" \
  "ds_read_b64_tr_b16 v[128:129], v56 offset:21632\n\t" \
  "ds_read_b64_tr_b16 v[130:131], v56 offset:23424\n\t" \
  "v_mfma_f32_32x32x16_bf16 v[208:223], v[96:99], v[84:87], v[208:223]\n\t" \
  "ds_read_b64_tr_b16 v[116:117], v57 offset:21504\n\t" \
  "ds_read_b64_tr_b16 v[118:119], v57 offset:23296\n\t" \
  "v_mfma_f32_32x32x16_bf16 v[176:191], v[100:103], v[80:83], v[176:191]\n\t" \
  "global_load_lds_dwordx4 v70, s[6:7] offset:2048\n\t" \
  "s_lshl_b32 s35, s99, 11\n\t" \
  "s_add_u32 s35, s34, s35\n\t" \
  "s_add_u32 s70, s44, s35\n\t" \
  "s_addc_u32 s71, s45, 0\n\t" \
  "s_mul_i32 s35, s91, 294912\n\t" \
  "v_mfma_f32_32x32x16_bf16 v[224:239], v[100:103], v[84:87], v[224:239]\n\t" \
  "global_load_lds_dwordx4 v71, s[6:7] offset:3072\n\t" \
  "s_sub_u32 s35, 147456, s35\n\t" \
  "s_ashr_i32 s36, s35, 31\n\t" \
  "s_add_u32 s72, s70, s35\n\t" \
  "s_addc_u32 s73, s71, s36\n\t" \
  "s_lshr_b32 s35, s99, 1\n\t" \
  "s_lshl_b32 s35, s35, 11\n\t" \
  "s_add_u32 m0, s48, 49152\n\t" \
  "v_mfma_f32_32x32x16_bf16 v[240:255], v[88:91], v[80:83], v[240:255]\n\t" \
  "global_load_lds_dwordx4 v72, s[6:7] offset:0\n\t" \
  "s_add_u32 s34, s34, s35\n\t" \
  "s_add_u32 s34, s34, 24576\n\t" \
  "s_add_u32 s78, s44, s34\n\t" \
  "s_addc_u32 s79, s45, 0\n\t" \
  "s_mul_i32 s33, s91, 384\n\t" \
  "s_waitcnt lgkmcnt(0)\n\t" \
  "v_mfma_f32_32x32x16_bf16 v[144:159], v[120:123], v[108:111], v[144:159]\n\t" \
  "global_load_lds_dwordx4 v73, s[6:7] offset:1024\n\t" \
  "v_add_u32 v48, s55, v48\n\t" \
  "v_add_u32 v49, s55, v49\n\t" \
  "v_add_u32 v50, s55, v50\n\t" \
  "v_add_u32 v51, s55, v51\n\t" \
  "v_add_u32 v52, s55, v52\n\t" \
  "v_mfma_f32_32x32x16_bf16 v[192:207], v[120:123], v[112:115], v[192:207]\n\t" \
  "global_load_lds_dwordx4 v74, s[6:7] offset:2048\n\t" \
  "v_add_u32 v53, s55, v53\n\t" \
  "v_add_u32 v54, s55, v54\n\t" \
  "v_add_u32 v55, s55, v55\n\t" \
  "v_add_u32 v56, s55, v56\n\t" \
  "v_add_u32 v57, s55, v57\n\t" \
  "v_mfma_f32_32x32x16_bf16 v[160:175], v[124:127], v[108:111], v[160:175]\n\t" \
  "s_waitcnt vmcnt(11)\n\t" \
  "s_waitcnt lgkmcnt(0)\n\t" \
  "s_barrier\n\t" \
  "ds_read_b128 v[80:83], v48\n\t" \
  "ds_read_b64_tr_b16 v[92:93], v56 offset:0\n\t" \
  "ds_read_b64_tr_b16 v[94:95], v56 offset:1792\n\t" \
  "v_mfma_f32_32x32x16_bf16 v[208:223], v[124:127], v[112:115], v[208:223]\n\t" \
  "ds_read_b128 v[84:87], v52\n\t" \
  "ds_read_b64_tr_b16 v[96:97], v56 offset:64\n\t" \
  "ds_read_b64_tr_b16 v[98:99], v56 offset:1856\n\t" \
  "v_mfma_f32_32x32x16_bf16 v[176:191], v[128:131], v[108:111], v[176:191]\n\t" \
  "ds_read_b64_tr_b16 v[100:101], v56 offset:128\n\t" \
  "ds_read_b64_tr_b16 v[102:103], v56 offset:1920\n\t" \
  "v_mfma_f32_32x32x16_bf16 v[224:239], v[128:131], v[112:115], v[224:239]\n\t" \
  "ds_read_b64_tr_b16 v[88:89], v57 offset:0\n\t" \
  "ds_read_b64_tr_b16 v[90:91], v57 offset:1792\n\t" \
  "s_mov_b32 s56, 0x10000\n\t" \
  "v_mov_b32 v64, v76\n\t" \
  "v_mov_b32 v65, v77\n\t" \
  "v_mfma_f32_32x32x16_bf16 v[240:255], v[116:119], v[108:111], v[240:255]\n\t" \
  "s_add_u32 s33, s33, 157696\n\t" \
  "v_lshl_add_u32 v17, v79, 4, s33\n\t" \
  "v_mov_b32 v18, 158464\n\t" \
  "v_lshl_add_u32 v18, v79, 4, v18\n\t" \
  "global_load_dwordx4 v[24:27], v60, s[44:45] sc1\n\t" \
  "global_load_dwordx4 v[28:31], v60, s[44:45] offset:16 sc1\n\t" \
  "global_load_dwordx4 v[32:35], v61, s[44:45] sc1\n\t" \
  "global_load_dwordx4 v[36:39], v61, s[44:45] offset:16 sc1\n\t" \
  "global_load_dwordx4 v[76:79], v62, s[44:45] sc1\n\t" \
  "s_waitcnt lgkmcnt(7)\n\t" \
  "v_mfma_f32_32x32x16_bf16 v[144:159], v[92:95], v[80:83], v[144:159]\n\t" \
  "ds_read_b128 v[108:111], v49\n\t" \
  "ds_read_b64_tr_b16 v[120:121], v56 offset:7168\n\t" \
  "ds_read_b64_tr_b16 v[122:123], v56 offset:8960\n\t" \
  "s_waitcnt lgkmcnt(9)\n\t" \
  "v_mfma_f32_32x32x16_bf16 v[192:207], v[92:95], v[84:87], v[192:207]\n\t" \
  "ds_read_b128 v[112:115], v53\n\t" \
  "ds_read_b64_tr_b16 v[124:125], v56 offset:7232\n\t" \
  "ds_read_b64_tr_b16 v[126:127], v56 offset:9024\n\t" \
  "s_waitcnt lgkmcnt(10)\n\t" \
  "v_mfma_f32_32x32x16_bf16 v[160:175], v[96:99], v[80:83], v[160:175]\n\t" \
  "ds_read_b64_tr_b16 v[128:129], v56 offset:7296\n\t" \
  "ds_read_b64_tr_b16 v[130:131], v56 offset:9088\n\t" \
  "v_mfma_f32_32x32x16_bf16 v[208:223], v[96:99], v[84:87], v[208:223]\n\t" \
  "ds_read_b64_tr_b16 v[116:117], v57 offset:7168\n\t" \
  "ds_read_b64_tr_b16 v[118:119], v57 offset:8960\n\t" \
  "s_waitcnt lgkmcnt(12)\n\t" \
  "v_mfma_f32_32x32x16_bf16 v[176:191], v[100:103], v[80:83], v[176:191]\n\t" \
  "s_add_u32 s92, s66, 0\n\t" \
  "s_addc_u32 s93, s67, 0\n\t" \
  "s_add_u32 m0, s83, 90112\n\t" \
  "s_nop 0\n\t" \
  "global_load_lds_dwordx4 v64, s[92:93]\n\t" \
  "global_load_lds_dwordx4 v65, s[92:93] offset:1024\n\t" \
  "v_mfma_f32_32x32x16_bf16 v[224:239], v[100:103], v[84:87], v[224:239]\n\t" \
  "s_add_u32 s94, s66, 98304\n\t" \
  "s_addc_u32 s95, s67, 0\n\t" \
  "s_add_u32 m0, s83, 98304\n\t" \
  "s_nop 0\n\t" \
  "global_load_lds_dwordx4 v64, s[94:95]\n\t" \
  "s_waitcnt lgkmcnt(10)\n\t" \
  "v_mfma_f32_32x32x16_bf16 v[240:255], v[88:91], v[80:83], v[240:255]\n\t" \
  "global_load_lds_dwordx4 v65, s[94:95] offset:1024\n\t" \
  "s_add_u32 s92, s66, 32768\n\t" \
  "s_addc_u32 s93, s67, 0\n\t" \
  "s_add_u32 m0, s83, 106496\n\t" \
  "s_nop 0\n\t" \
  "global_load_lds_dwordx4 v64, s[92:93]\n\t" \
  "s_waitcnt lgkmcnt(0)\n\t" \
  "v_mfma_f32_32x32x16_bf16 v[144:159], v[120:123], v[108:111], v[144:159]\n\t" \
  "ds_read_b128 v[80:83], v50\n\t" \
  "ds_read_b64_tr_b16 v[92:93], v56 offset:14336\n\t" \
  "ds_read_b64_tr_b16 v[94:95], v56 offset:16128\n\t" \
  "s_mov_b32 s55, 0xb000\n\t" \
  "v_mfma_f32_32x32x16_bf16 v[192:207], v[120:123], v[112:115], v[192:207]\n\t" \
  "ds_read_b128 v[84:87], v54\n\t" \
  "ds_read_b64_tr_b16 v[96:97], v56 offset:14400\n\t" \
  "ds_read_b64_tr_b16 v[98:99], v56 offset:16192\n\t" \
  "v_mfma_f32_32x32x16_bf16 v[160:175], v[124:127], v[108:111], v[160:175]\n\t" \
  "ds_read_b64_tr_b16 v[100:101], v56 offset:14464\n\t" \
  "ds_read_b64_tr_b16 v[102:103], v56 offset:16256\n\t" \
  "v_mfma_f32_32x32x16_bf16 v[208:223], v[124:127], v[112:115], v[208:223]\n\t" \
  "ds_read_b64_tr_b16 v[88:89], v57 offset:14336\n\t" \
  "ds_read_b64_tr_b16 v[90:91], v57 offset:16128\n\t" \
  "v_mfma_f32_32x32x16_bf16 v[176:191], v[128:131], v[108:111], v[176:191]\n\t" \
  "global_load_lds_dwordx4 v65, s[92:93] offset:1024\n\t" \
  "v_mfma_f32_32x32x16_bf16 v[224:239], v[128:131], v[112:115], v[224:239]\n\t" \
  "s_add_u32 s94, s66, 131072\n\t" \
  "s_addc_u32 s95, s67, 0\n\t" \
  "s_add_u32 m0, s83, 114688\n\t" \
  "s_nop 0\n\t" \
  "global_load_lds_dwordx4 v64, s[94:95]\n\t" \
  "global_load_lds_dwordx4 v65, s[94:95] offset:1024\n\t" \
  "v_mfma_f32_32x32x16_bf16 v[240:255], v[116:119], v[108:111], v[240:255]\n\t" \
  "s_add_u32 s92, s20, 0\n\t" \
  "s_addc_u32 s93, s21, 0\n\t" \
  "s_add_u32 m0, s83, 122880\n\t" \
  "s_nop 0\n\t" \
  "global_load_lds_dwordx4 v64, s[92:93]\n\t" \
  "s_waitcnt lgkmcnt(0)\n\t" \
  "v_mfma_f32_32x32x16_bf16 v[144:159], v[92:95], v[80:83], v[144:159]\n\t" \
  "ds_read_b128 v[108:111], v51\n\t" \
  "ds_read_b64_tr_b16 v[120:121], v56 offset:21504\n\t" \
  "ds_read_b64_tr_b16 v[122:123], v56 offset:23296\n\t" \
  "v_mfma_f32_32x32x16_bf16 v[192:207], v[92:95], v[84:87], v[192:207]\n\t" \
  "ds_read_b128 v[112:115], v55\n\t" \
  "ds_read_b64_tr_b16 v[124:125], v56 offset:21568\n\t" \
  "ds_read_b64_tr_b16 v[126:127], v56 offset:23360\n\t" \
  "v_mfma_f32_32x32x16_bf16 v[160:175], v[96:99], v[80:83], v[160:175]\n\t" \
  "ds_read_b64_tr_b16 v[128:129], v56 offset:21632\n\t" \
  "ds_read_b64_tr_b16 v[130:131], v56 offset:23424\n\t" \
  "v_mfma_f32_32x32x16_bf16 v[208:223], v[96:99], v[84:87], v[208:223]\n\t" \
  "ds_read_b64_tr_b16 v[116:117], v57 offset:21504\n\t" \
  "ds_read_b64_tr_b16 v[118:119], v57 offset:23296\n\t" \
  "v_mfma_f32_32x32x16_bf16 v[176:191], v[100:103], v[80:83], v[176:191]\n\t" \
  "global_load_lds_dwordx4 v65, s[92:93] offset:1024\n\t" \
  "s_add_u32 s94, s20, 98304\n\t" \
  "s_addc_u32 s95, s21, 0\n\t" \
  "s_add_u32 m0, s83, 131072\n\t" \
  "s_nop 0\n\t" \
  "global_load_lds_dwordx4 v64, s[94:95]\n\t" \
  "v_mfma_f32_32x32x16_bf16 v[224:239], v[100:103], v[84:87], v[224:239]\n\t" \
  "global_load_lds_dwordx4 v65, s[94:95] offset:1024\n\t" \
  "v_mfma_f32_32x32x16_bf16 v[240:255], v[88:91], v[80:83], v[240:255]\n\t" \
  "s_add_u32 s92, s20, 32768\n\t" \
  "s_addc_u32 s93, s21, 0\n\t" \
  "s_add_u32 m0, s83, 139264\n\t" \
  "s_nop 0\n\t" \
  "global_load_lds_dwordx4 v64, s[92:93]\n\t" \
  "global_load_lds_dwordx4 v65, s[92:93] offset:1024\n\t" \
  "s_waitcnt lgkmcnt(0)\n\t" \
  "v_mfma_f32_32x32x16_bf16 v[144:159], v[120:123], v[108:111], v[144:159]\n\t" \
  "v_add_u32 v48, s55, v48\n\t" \
  "v_add_u32 v49, s55, v49\n\t" \
  "v_add_u32 v50, s55, v50\n\t" \
  "v_add_u32 v51, s55, v51\n\t" \
  "v_add_u32 v52, s55, v52\n\t" \
  "s_add_u32 s94, s20, 131072\n\t" \
  "s_addc_u32 s95, s21, 0\n\t" \
  "s_add_u32 m0, s83, 147456\n\t" \
  "s_nop 0\n\t" \
  "global_load_lds_dwordx4 v64, s[94:95]\n\t" \
  "v_mfma_f32_32x32x16_bf16 v[192:207], v[120:123], v[112:115], v[192:207]\n\t" \
  "v_add_u32 v53, s55, v53\n\t" \
  "v_add_u32 v54, s55, v54\n\t" \
  "v_add_u32 v55, s55, v55\n\t" \
  "v_add_u32 v56, s55, v56\n\t" \
  "v_add_u32 v57, s55, v57\n\t" \
  "global_load_lds_dwordx4 v65, s[94:95] offset:1024\n\t" \
  "v_mfma_f32_32x32x16_bf16 v[160:175], v[124:127], v[108:111], v[160:175]\n\t" \
  "s_waitcnt vmcnt(21)\n\t" \
  "s_waitcnt lgkmcnt(0)\n\t" \
  "s_barrier\n\t" \
  "ds_read_b128 v[80:83], v48\n\t" \
  "ds_read_b64_tr_b16 v[92:93], v56 offset:0\n\t" \
  "ds_read_b64_tr_b16 v[94:95], v56 offset:1792\n\t" \
  "v_mfma_f32_32x32x16_bf16 v[208:223], v[124:127], v[112:115], v[208:223]\n\t" \
  "ds_read_b128 v[84:87], v52\n\t" \
  "ds_read_b64_tr_b16 v[96:97], v56 offset:64\n\t" \
  "ds_read_b64_tr_b16 v[98:99], v56 offset:1856\n\t" \
  "v_mfma_f32_32x32x16_bf16 v[176:191], v[128:131], v[108:111], v[176:191]\n\t" \
  "ds_read_b64_tr_b16 v[100:101], v56 offset:128\n\t" \
  "ds_read_b64_tr_b16 v[102:103], v56 offset:1920\n\t" \
  "v_mfma_f32_32x32x16_bf16 v[224:239], v[128:131], v[112:115], v[224:239]\n\t" \
  "ds_read_b64_tr_b16 v[88:89], v57 offset:0\n\t" \
  "ds_read_b64_tr_b16 v[90:91], v57 offset:1792\n\t" \
  "v_mfma_f32_32x32x16_bf16 v[240:255], v[116:119], v[108:111], v[240:255]\n\t" \
  "s_waitcnt lgkmcnt(7)\n\t" \
  "v_mfma_f32_32x32x16_bf16 v[144:159], v[92:95], v[80:83], v[144:159]\n\t" \
  "ds_read_b128 v[108:111], v49\n\t" \
  "ds_read_b64_tr_b16 v[120:121], v56 offset:7168\n\t" \
  "ds_read_b64_tr_b16 v[122:123], v56 offset:8960\n\t" \
  "s_waitcnt lgkmcnt(9)\n\t" \
  "v_mfma_f32_32x32x16_bf16 v[192:207], v[92:95], v[84:87], v[192:207]\n\t" \
  "ds_read_b128 v[112:115], v53\n\t" \
  "ds_read_b64_tr_b16 v[124:125], v56 offset:7232\n\t" \
  "ds_read_b64_tr_b16 v[126:127], v56 offset:9024\n\t" \
  "s_waitcnt lgkmcnt(10)\n\t" \
  "v_mfma_f32_32x32x16_bf16 v[160:175], v[96:99], v[80:83], v[160:175]\n\t" \
  "ds_read_b64_tr_b16 v[128:129], v56 offset:7296\n\t" \
  "ds_read_b64_tr_b16 v[130:131], v56 offset:9088\n\t" \
  "v_mfma_f32_32x32x16_bf16 v[208:223], v[96:99], v[84:87], v[208:223]\n\t" \
  "ds_read_b64_tr_b16 v[116:117], v57 offset:7168\n\t" \
  "ds_read_b64_tr_b16 v[118:119], v57 offset:8960\n\t" \
  "s_waitcnt lgkmcnt(12)\n\t" \
  "v_mfma_f32_32x32x16_bf16 v[176:191], v[100:103], v[80:83], v[176:191]\n\t" \
  "s_waitcnt vmcnt(16)\n\t" \
  "KCF%=:\n\t" \
  "v_and_b32 v58, v24, v26\n\t" \
  "v_or_b32 v75, v24, v26\n\t" \
  "v_bitop3_b32 v58, v58, v28, v30 bitop3:0x80\n\t" \
  "v_or3_b32 v75, v75, v28, v30\n\t" \
  "v_bitop3_b32 v58, v58, v32, v34 bitop3:0x80\n\t" \
  "v_or3_b32 v75, v75, v32, v34\n\t" \
  "v_bitop3_b32 v58, v58, v36, v38 bitop3:0x80\n\t" \
  "v_or3_b32 v75, v75, v36, v38\n\t" \
  "v_bitop3_b32 v58, v58, v77, v79 bitop3:0x80\n\t" \
  "v_or3_b32 v75, v75, v77, v79\n\t" \
  "v_cmp_ne_u32 vcc, s49, v75\n\t" \
  "v_cmp_ne_u32 s[58:59], s49, v58\n\t" \
  "s_nop 1\n\t" \
  "s_or_b64 vcc, vcc, s[58:59]\n\t" \
  "s_cbranch_scc1 KRF%=\n\t" \
  "KOF%=:\n\t" \
  "v_add_f32 v76, v76, v78\n\t" \
  "v_mov_b32 v136, 0x3727c5ac\n\t" \
  "ds_write_b32 v63, v76\n\t" \
  "v_mfma_f32_32x32x16_bf16 v[224:239], v[100:103], v[84:87], v[224:239]\n\t" \
  "s_add_u32 s92, s20, 163840\n\t" \
  "s_addc_u32 s93, s21, 0\n\t" \
  "s_add_u32 m0, s83, 24576\n\t" \
  "s_nop 0\n\t" \
  "global_load_lds_dwordx4 v64, s[92:93]\n\t" \
  "s_waitcnt lgkmcnt(11)\n\t" \
  "v_mfma_f32_32x32x16_bf16 v[240:255], v[88:91], v[80:83], v[240:255]\n\t" \
  "v_add_f32 v58, v25, v29\n\t" \
  "v_add_f32 v59, v27, v31\n\t" \
  "global_load_lds_dwordx4 v65, s[92:93] offset:1024\n\t" \
  "s_waitcnt lgkmcnt(1)\n\t" \
  "v_mfma_f32_32x32x16_bf16 v[144:159], v[120:123], v[108:111], v[144:159]\n\t" \
  "ds_read_b128 v[80:83], v50\n\t" \
  "ds_read_b64_tr_b16 v[92:93], v56 offset:14336\n\t" \
  "ds_read_b64_tr_b16 v[94:95], v56 offset:16128\n\t" \
  "s_mov_b32 s55, 0xb000\n\t" \
  "v_mfma_f32_32x32x16_bf16 v[192:207], v[120:123], v[112:115], v[192:207]\n\t" \
  "ds_read_b128 v[84:87], v54\n\t" \
  "ds_read_b64_tr_b16 v[96:97], v56 offset:14400\n\t" \
  "ds_read_b64_tr_b16 v[98:99], v56 offset:16192\n\t" \
  "v_mfma_f32_32x32x16_bf16 v[160:175], v[124:127], v[108:111], v[160:175]\n\t" \
  "ds_read_b64_tr_b16 v[100:101], v56 offset:14464\n\t" \
  "ds_read_b64_tr_b16 v[102:103], v56 offset:16256\n\t" \
  "v_mfma_f32_32x32x16_bf16 v[208:223], v[124:127], v[112:115], v[208:223]\n\t" \
  "ds_read_b64_tr_b16 v[88:89], v57 offset:14336\n\t" \
  "ds_read_b64_tr_b16 v[90:91], v57 offset:16128\n\t" \
  "v_mfma_f32_32x32x16_bf16 v[176:191], v[128:131], v[108:111], v[176:191]\n\t" \
  "v_mul_f32 v30, 0xbb000000, v58\n\t" \
  "v_fmamk_f32 v59, v59, 0x3b000000, v136\n\t" \
  "s_add_u32 s94, s20, 196608\n\t" \
  "s_addc_u32 s95, s21, 0\n\t" \
  "s_add_u32 m0, s83, 32768\n\t" \
  "s_nop 0\n\t" \
  "global_load_lds_dwordx4 v64, s[94:95]\n\t" \
  "v_mfma_f32_32x32x16_bf16 v[224:239], v[128:131], v[112:115], v[224:239]\n\t" \
  "v_fma_f32 v59, -v30, v30, v59\n\t" \
  "v_max_f32 v59, v59, v136\n\t" \
  "global_load_lds_dwordx4 v65, s[94:95] offset:1024\n\t" \
  "v_mfma_f32_32x32x16_bf16 v[240:255], v[116:119], v[108:111], v[240:255]\n\t" \
  "v_rsq_f32 v29, v59\n\t" \
  "s_add_u32 s92, s62, 65536\n\t" \
  "s_addc_u32 s93, s63, 0\n\t" \
  "s_add_u32 m0, s83, 0\n\t" \
  "s_nop 0\n\t" \
  "global_load_lds_dwordx4 v64, s[92:93]\n\t" \
  "s_waitcnt lgkmcnt(0)\n\t" \
  "v_mfma_f32_32x32x16_bf16 v[144:159], v[92:95], v[80:83], v[144:159]\n\t" \
  "ds_read_b128 v[108:111], v51\n\t" \
  "ds_read_b64_tr_b16 v[120:121], v56 offset:21504\n\t" \
  "ds_read_b64_tr_b16 v[122:123], v56 offset:23296\n\t" \
  "v_mfma_f32_32x32x16_bf16 v[192:207], v[92:95], v[84:87], v[192:207]\n\t" \
  "ds_read_b128 v[112:115], v55\n\t" \
  "ds_read_b64_tr_b16 v[124:125], v56 offset:21568\n\t" \
  "ds_read_b64_tr_b16 v[126:127], v56 offset:23360\n\t" \
  "v_mfma_f32_32x32x16_bf16 v[160:175], v[96:99], v[80:83], v[160:175]\n\t" \
  "ds_read_b64_tr_b16 v[128:129], v56 offset:21632\n\t" \
  "ds_read_b64_tr_b16 v[130:131], v56 offset:23424\n\t" \
  "v_mfma_f32_32x32x16_bf16 v[208:223], v[96:99], v[84:87], v[208:223]\n\t" \
  "ds_read_b64_tr_b16 v[116:117], v57 offset:21504\n\t" \
  "ds_read_b64_tr_b16 v[118:119], v57 offset:23296\n\t" \
  "v_mfma_f32_32x32x16_bf16 v[176:191], v[100:103], v[80:83], v[176:191]\n\t" \
  "v_mul_f32 v31, v30, v29\n\t" \
  "global_load_lds_dwordx4 v65, s[92:93] offset:1024\n\t" \
  "v_mfma_f32_32x32x16_bf16 v[224:239], v[100:103], v[84:87], v[224:239]\n\t" \
  "v_add_f32 v58, v33, v37\n\t" \
  "v_add_f32 v59, v35, v39\n\t" \
  "s_add_u32 s94, s62, 98304\n\t" \
  "s_addc_u32 s95, s63, 0\n\t" \
  "s_add_u32 m0, s83, 8192\n\t" \
  "s_nop 0\n\t" \
  "global_load_lds_dwordx4 v64, s[94:95]\n\t" \
  "v_mfma_f32_32x32x16_bf16 v[240:255], v[88:91], v[80:83], v[240:255]\n\t" \
  "v_mul_f32 v34, 0xbb000000, v58\n\t" \
  "v_fmamk_f32 v59, v59, 0x3b000000, v136\n\t" \
  "global_load_lds_dwordx4 v65, s[94:95] offset:1024\n\t" \
  "s_waitcnt lgkmcnt(0)\n\t" \
  "v_mfma_f32_32x32x16_bf16 v[144:159], v[120:123], v[108:111], v[144:159]\n\t" \
  "s_waitcnt lgkmcnt(0)\n\t" \
  "s_barrier\n\t" \
  "v_fma_f32 v59, -v34, v34, v59\n\t" \
  "v_max_f32 v59, v59, v136\n\t" \
  "v_mfma_f32_32x32x16_bf16 v[192:207], v[120:123], v[112:115], v[192:207]\n\t" \
  "ds_read_b128 v[68:71], v17 offset:0\n\t" \
  "ds_read_b128 v[72:75], v17 offset:32\n\t" \
  "ds_read_b128 v[76:79], v17 offset:64\n\t" \
  "ds_read_b128 v[80:83], v17 offset:96\n\t" \
  "v_mfma_f32_32x32x16_bf16 v[160:175], v[124:127], v[108:111], v[160:175]\n\t" \
  "ds_read_b128 v[84:87], v17 offset:128\n\t" \
  "ds_read_b128 v[88:91], v17 offset:160\n\t" \
  "ds_read_b128 v[92:95], v17 offset:192\n\t" \
  "ds_read_b128 v[96:99], v17 offset:224\n\t" \
  "v_mfma_f32_32x32x16_bf16 v[208:223], v[124:127], v[112:115], v[208:223]\n\t" \
  "v_rsq_f32 v33, v59\n\t" \
  "v_mfma_f32_32x32x16_bf16 v[176:191], v[128:131], v[108:111], v[176:191]\n\t" \
  "v_mul_f32 v35, v34, v33\n\t" \
  "v_mfma_f32_32x32x16_bf16 v[224:239], v[128:131], v[112:115], v[224:239]\n\t" \
  "v_mfma_f32_32x32x16_bf16 v[240:255], v[116:119], v[108:111], v[240:255]\n\t" \
  "v_mul_f32 v29, 0xbfb8aa3b, v29\n\t" \
  "v_mul_f32 v31, 0xbfb8aa3b, v31\n\t" \
  "v_mul_f32 v33, 0xbfb8aa3b, v33\n\t" \
  "v_mul_f32 v35, 0xbfb8aa3b, v35\n\t" \
  "v_add_u32 v132, 8192, v0\n\t" \
  "v_add_u32 v133, 16384, v0\n\t" \
  "s_waitcnt lgkmcnt(7)\n\t" \
  "v_pk_mul_f32 v[100:101], v[68:69], v[30:31] op_sel:[0,1] op_sel_hi:[1,1]\n\t" \
  "v_pk_mul_f32 v[102:103], v[70:71], v[30:31] op_sel:[0,1] op_sel_hi:[1,1]\n\t" \
  "s_waitcnt lgkmcnt(6)\n\t" \
  "v_pk_mul_f32 v[104:105], v[72:73], v[30:31] op_sel:[0,1] op_sel_hi:[1,1]\n\t" \
  "v_pk_mul_f32 v[106:107], v[74:75], v[30:31] op_sel:[0,1] op_sel_hi:[1,1]\n\t" \
  "v_pk_fma_f32 v[144:145], v[144:145], v[28:29], v[100:101] op_sel:[0,1,0] op_sel_hi:[1,1,1]\n\t" \
  "v_pk_fma_f32 v[146:147], v[146:147], v[28:29], v[102:103] op_sel:[0,1,0] op_sel_hi:[1,1,1]\n\t" \
  "v_pk_fma_f32 v[148:149], v[148:149], v[28:29], v[104:105] op_sel:[0,1,0] op_sel_hi:[1,1,1]\n\t" \
  "v_pk_fma_f32 v[150:151], v[150:151], v[28:29], v[106:107] op_sel:[0,1,0] op_sel_hi:[1,1,1]\n\t" \
  "s_waitcnt lgkmcnt(5)\n\t" \
  "v_pk_mul_f32 v[100:101], v[76:77], v[30:31] op_sel:[0,1] op_sel_hi:[1,1]\n\t" \
  "v_pk_mul_f32 v[102:103], v[78:79], v[30:31] op_sel:[0,1] op_sel_hi:[1,1]\n\t" \
  "s_waitcnt lgkmcnt(4)\n\t" \
  "v_pk_mul_f32 v[104:105], v[80:81], v[30:31] op_sel:[0,1] op_sel_hi:[1,1]\n\t" \
  "v_pk_mul_f32 v[106:107], v[82:83], v[30:31] op_sel:[0,1] op_sel_hi:[1,1]\n\t" \
  "v_pk_fma_f32 v[152:153], v[152:153], v[28:29], v[100:101] op_sel:[0,1,0] op_sel_hi:[1,1,1]\n\t" \
  "v_pk_fma_f32 v[154:155], v[154:155], v[28:29], v[102:103] op_sel:[0,1,0] op_sel_hi:[1,1,1]\n\t" \
  "v_pk_fma_f32 v[156:157], v[156:157], v[28:29], v[104:105] op_sel:[0,1,0] op_sel_hi:[1,1,1]\n\t" \
  "v_pk_fma_f32 v[158:159], v[158:159], v[28:29], v[106:107] op_sel:[0,1,0] op_sel_hi:[1,1,1]\n\t" \
  "v_exp_f32 v108, v144\n\t" \
  "v_exp_f32 v109, v145\n\t" \
  "v_exp_f32 v110, v146\n\t" \
  "v_exp_f32 v111, v147\n\t" \
  "v_exp_f32 v112, v148\n\t" \
  "v_exp_f32 v113, v149\n\t" \
  "v_exp_f32 v114, v150\n\t" \
  "v_exp_f32 v115, v151\n\t" \
  "v_pk_add_f32 v[108:109], v[108:109], 1.0 op_sel_hi:[1,0]\n\t" \
  "v_pk_add_f32 v[110:111], v[110:111], 1.0 op_sel_hi:[1,0]\n\t" \
  "v_pk_add_f32 v[112:113], v[112:113], 1.0 op_sel_hi:[1,0]\n\t" \
  "v_pk_add_f32 v[114:115], v[114:115], 1.0 op_sel_hi:[1,0]\n\t" \
  "v_rcp_f32 v108, v108\n\t" \
  "v_rcp_f32 v109, v109\n\t" \
  "v_rcp_f32 v110, v110\n\t" \
  "v_rcp_f32 v111, v111\n\t" \
  "v_rcp_f32 v112, v112\n\t" \
  "v_rcp_f32 v113, v113\n\t" \
  "v_rcp_f32 v114, v114\n\t" \
  "v_rcp_f32 v115, v115\n\t" \
  "v_pk_mul_f32 v[144:145], v[144:145], v[108:109]\n\t" \
  "v_pk_mul_f32 v[146:147], v[146:147], v[110:111]\n\t" \
  "v_pk_mul_f32 v[148:149], v[148:149], v[112:113]\n\t" \
  "v_pk_mul_f32 v[150:151], v[150:151], v[114:115]\n\t" \
  "v_cvt_pk_bf16_f32 v116, v144, v145\n\t" \
  "v_cvt_pk_bf16_f32 v117, v146, v147\n\t" \
  "v_cvt_pk_bf16_f32 v118, v148, v149\n\t" \
  "v_cvt_pk_bf16_f32 v119, v150, v151\n\t" \
  "global_store_dwordx4 v0, v[116:119], s[70:71] offset:0 sc1\n\t" \
  "v_exp_f32 v108, v152\n\t" \
  "v_exp_f32 v109, v153\n\t" \
  "v_exp_f32 v110, v154\n\t" \
  "v_exp_f32 v111, v155\n\t" \
  "v_exp_f32 v112, v156\n\t" \
  "v_exp_f32 v113, v157\n\t" \
  "v_exp_f32 v114, v158\n\t" \
  "v_exp_f32 v115, v159\n\t" \
  "v_pk_add_f32 v[108:109], v[108:109], 1.0 op_sel_hi:[1,0]\n\t" \
  "v_pk_add_f32 v[110:111], v[110:111], 1.0 op_sel_hi:[1,0]\n\t" \
  "v_pk_add_f32 v[112:113], v[112:113], 1.0 op_sel_hi:[1,0]\n\t" \
  "v_pk_add_f32 v[114:115], v[114:115], 1.0 op_sel_hi:[1,0]\n\t" \
  "v_rcp_f32 v108, v108\n\t" \
  "v_rcp_f32 v109, v109\n\t" \
  "v_rcp_f32 v110, v110\n\t" \
  "v_rcp_f32 v111, v111\n\t" \
  "v_rcp_f32 v112, v112\n\t" \
  "v_rcp_f32 v113, v113\n\t" \
  "v_rcp_f32 v114, v114\n\t" \
  "v_rcp_f32 v115, v115\n\t" \
  "v_pk_mul_f32 v[152:153], v[152:153], v[108:109]\n\t" \
  "v_pk_mul_f32 v[154:155], v[154:155], v[110:111]\n\t" \
  "v_pk_mul_f32 v[156:157], v[156:157], v[112:113]\n\t" \
  "v_pk_mul_f32 v[158:159], v[158:159], v[114:115]\n\t" \
  "v_cvt_pk_bf16_f32 v120, v152, v153\n\t" \
  "v_cvt_pk_bf16_f32 v121, v154, v155\n\t" \
  "v_cvt_pk_bf16_f32 v122, v156, v157\n\t" \
  "v_cvt_pk_bf16_f32 v123, v158, v159\n\t" \
  "global_store_dwordx4 v0, v[120:123], s[70:71] offset:1024 sc1\n\t" \
  "v_pk_mul_f32 v[100:101], v[68:69], v[34:35] op_sel:[0,1] op_sel_hi:[1,1]\n\t" \
  "v_pk_mul_f32 v[102:103], v[70:71], v[34:35] op_sel:[0,1] op_sel_hi:[1,1]\n\t" \
  "v_pk_mul_f32 v[104:105], v[72:73], v[34:35] op_sel:[0,1] op_sel_hi:[1,1]\n\t" \
  "v_pk_mul_f32 v[106:107], v[74:75], v[34:35] op_sel:[0,1] op_sel_hi:[1,1]\n\t" \
  "v_pk_fma_f32 v[192:193], v[192:193], v[32:33], v[100:101] op_sel:[0,1,0] op_sel_hi:[1,1,1]\n\t" \
  "v_pk_fma_f32 v[194:195], v[194:195], v[32:33], v[102:103] op_sel:[0,1,0] op_sel_hi:[1,1,1]\n\t" \
  "v_pk_fma_f32 v[196:197], v[196:197], v[32:33], v[104:105] op_sel:[0,1,0] op_sel_hi:[1,1,1]\n\t" \
  "v_pk_fma_f32 v[198:199], v[198:199], v[32:33], v[106:107] op_sel:[0,1,0] op_sel_hi:[1,1,1]\n\t" \
  "v_pk_mul_f32 v[100:101], v[76:77], v[34:35] op_sel:[0,1] op_sel_hi:[1,1]\n\t" \
  "v_pk_mul_f32 v[102:103], v[78:79], v[34:35] op_sel:[0,1] op_sel_hi:[1,1]\n\t" \
  "v_pk_mul_f32 v[104:105], v[80:81], v[34:35] op_sel:[0,1] op_sel_hi:[1,1]\n\t" \
  "v_pk_mul_f32 v[106:107], v[82:83], v[34:35] op_sel:[0,1] op_sel_hi:[1,1]\n\t" \
  "v_pk_fma_f32 v[200:201], v[200:201], v[32:33], v[100:101] op_sel:[0,1,0] op_sel_hi:[1,1,1]\n\t" \
  "v_pk_fma_f32 v[202:203], v[202:203], v[32:33], v[102:103] op_sel:[0,1,0] op_sel_hi:[1,1,1]\n\t" \
  "v_pk_fma_f32 v[204:205], v[204:205], v[32:33], v[104:105] op_sel:[0,1,0] op_sel_hi:[1,1,1]\n\t" \
  "v_pk_fma_f32 v[206:207], v[206:207], v[32:33], v[106:107] op_sel:[0,1,0] op_sel_hi:[1,1,1]\n\t" \
  "ds_read_b128 v[68:71], v17 offset:256\n\t" \
  "ds_read_b128 v[72:75], v17 offset:288\n\t" \
  "ds_read_b128 v[76:79], v17 offset:320\n\t" \
  "ds_read_b128 v[80:83], v17 offset:352\n\t" \
  "v_exp_f32 v108, v192\n\t" \
  "v_exp_f32 v109, v193\n\t" \
  "v_exp_f32 v110, v194\n\t" \
  "v_exp_f32 v111, v195\n\t" \
  "v_exp_f32 v112, v196\n\t" \
  "v_exp_f32 v113, v197\n\t" \
  "v_exp_f32 v114, v198\n\t" \
  "v_exp_f32 v115, v199\n\t" \
  "v_pk_add_f32 v[108:109], v[108:109], 1.0 op_sel_hi:[1,0]\n\t" \
  "v_pk_add_f32 v[110:111], v[110:111], 1.0 op_sel_hi:[1,0]\n\t" \
  "v_pk_add_f32 v[112:113], v[112:113], 1.0 op_sel_hi:[1,0]\n\t" \
  "v_pk_add_f32 v[114:115], v[114:115], 1.0 op_sel_hi:[1,0]\n\t" \
  "v_rcp_f32 v108, v108\n\t" \
  "v_rcp_f32 v109, v109\n\t" \
  "v_rcp_f32 v110, v110\n\t" \
  "v_rcp_f32 v111, v111\n\t" \
  "v_rcp_f32 v112, v112\n\t" \
  "v_rcp_f32 v113, v113\n\t" \
  "v_rcp_f32 v114, v114\n\t" \
  "v_rcp_f32 v115, v115\n\t" \
  "v_pk_mul_f32 v[192:193], v[192:193], v[108:109]\n\t" \
  "v_pk_mul_f32 v[194:195], v[194:195], v[110:111]\n\t" \
  "v_pk_mul_f32 v[196:197], v[196:197], v[112:113]\n\t" \
  "v_pk_mul_f32 v[198:199], v[198:199], v[114:115]\n\t" \
  "v_cvt_pk_bf16_f32 v124, v192, v193\n\t" \
  "v_cvt_pk_bf16_f32 v125, v194, v195\n\t" \
  "v_cvt_pk_bf16_f32 v126, v196, v197\n\t" \
  "v_cvt_pk_bf16_f32 v127, v198, v199\n\t" \
  "global_store_dwordx4 v0, v[124:127], s[72:73] offset:0 sc1\n\t" \
  "v_exp_f32 v108, v200\n\t" \
  "v_exp_f32 v109, v201\n\t" \
  "v_exp_f32 v110, v202\n\t" \
  "v_exp_f32 v111, v203\n\t" \
  "v_exp_f32 v112, v204\n\t" \
  "v_exp_f32 v113, v205\n\t" \
  "v_exp_f32 v114, v206\n\t" \
  "v_exp_f32 v115, v207\n\t" \
  "v_pk_add_f32 v[108:109], v[108:109], 1.0 op_sel_hi:[1,0]\n\t" \
  "v_pk_add_f32 v[110:111], v[110:111], 1.0 op_sel_hi:[1,0]\n\t" \
  "v_pk_add_f32 v[112:113], v[112:113], 1.0 op_sel_hi:[1,0]\n\t" \
  "v_pk_add_f32 v[114:115], v[114:115], 1.0 op_sel_hi:[1,0]\n\t" \
  "v_rcp_f32 v108, v108\n\t" \
  "v_rcp_f32 v109, v109\n\t" \
  "v_rcp_f32 v110, v110\n\t" \
  "v_rcp_f32 v111, v111\n\t" \
  "v_rcp_f32 v112, v112\n\t" \
  "v_rcp_f32 v113, v113\n\t" \
  "v_rcp_f32 v114, v114\n\t" \
  "v_rcp_f32 v115, v115\n\t" \
  "v_pk_mul_f32 v[200:201], v[200:201], v[108:109]\n\t" \
  "v_pk_mul_f32 v[202:203], v[202:203], v[110:111]\n\t" \
  "v_pk_mul_f32 v[204:205], v[204:205], v[112:113]\n\t" \
  "v_pk_mul_f32 v[206:207], v[206:207], v[114:115]\n\t" \
  "v_cvt_pk_bf16_f32 v128, v200, v201\n\t" \
  "v_cvt_pk_bf16_f32 v129, v202, v203\n\t" \
  "v_cvt_pk_bf16_f32 v130, v204, v205\n\t" \
  "v_cvt_pk_bf16_f32 v131, v206, v207\n\t" \
  "global_store_dwordx4 v0, v[128:131], s[72:73] offset:1024 sc1\n\t" \
  "s_waitcnt lgkmcnt(7)\n\t" \
  "v_pk_mul_f32 v[100:101], v[84:85], v[30:31] op_sel:[0,1] op_sel_hi:[1,1]\n\t" \
  "v_pk_mul_f32 v[102:103], v[86:87], v[30:31] op_sel:[0,1] op_sel_hi:[1,1]\n\t" \
  "s_waitcnt lgkmcnt(6)\n\t" \
  "v_pk_mul_f32 v[104:105], v[88:89], v[30:31] op_sel:[0,1] op_sel_hi:[1,1]\n\t" \
  "v_pk_mul_f32 v[106:107], v[90:91], v[30:31] op_sel:[0,1] op_sel_hi:[1,1]\n\t" \
  "v_pk_fma_f32 v[160:161], v[160:161], v[28:29], v[100:101] op_sel:[0,1,0] op_sel_hi:[1,1,1]\n\t" \
  "v_pk_fma_f32 v[162:163], v[162:163], v[28:29], v[102:103] op_sel:[0,1,0] op_sel_hi:[1,1,1]\n\t" \
  "v_pk_fma_f32 v[164:165], v[164:165], v[28:29], v[104:105] op_sel:[0,1,0] op_sel_hi:[1,1,1]\n\t" \
  "v_pk_fma_f32 v[166:167], v[166:167], v[28:29], v[106:107] op_sel:[0,1,0] op_sel_hi:[1,1,1]\n\t" \
  "s_waitcnt lgkmcnt(5)\n\t" \
  "v_pk_mul_f32 v[100:101], v[92:93], v[30:31] op_sel:[0,1] op_sel_hi:[1,1]\n\t" \
  "v_pk_mul_f32 v[102:103], v[94:95], v[30:31] op_sel:[0,1] op_sel_hi:[1,1]\n\t" \
  "s_waitcnt lgkmcnt(4)\n\t" \
  "v_pk_mul_f32 v[104:105], v[96:97], v[30:31] op_sel:[0,1] op_sel_hi:[1,1]\n\t" \
  "v_pk_mul_f32 v[106:107], v[98:99], v[30:31] op_sel:[0,1] op_sel_hi:[1,1]\n\t" \
  "v_pk_fma_f32 v[168:169], v[168:169], v[28:29], v[100:101] op_sel:[0,1,0] op_sel_hi:[1,1,1]\n\t" \
  "v_pk_fma_f32 v[170:171], v[170:171], v[28:29], v[102:103] op_sel:[0,1,0] op_sel_hi:[1,1,1]\n\t" \
  "v_pk_fma_f32 v[172:173], v[172:173], v[28:29], v[104:105] op_sel:[0,1,0] op_sel_hi:[1,1,1]\n\t" \
  "v_pk_fma_f32 v[174:175], v[174:175], v[28:29], v[106:107] op_sel:[0,1,0] op_sel_hi:[1,1,1]\n\t" \
  "v_exp_f32 v108, v160\n\t" \
  "v_exp_f32 v109, v161\n\t" \
  "v_exp_f32 v110, v162\n\t" \
  "v_exp_f32 v111, v163\n\t" \
  "v_exp_f32 v112, v164\n\t" \
  "v_exp_f32 v113, v165\n\t" \
  "v_exp_f32 v114, v166\n\t" \
  "v_exp_f32 v115, v167\n\t" \
  "v_pk_add_f32 v[108:109], v[108:109], 1.0 op_sel_hi:[1,0]\n\t" \
  "v_pk_add_f32 v[110:111], v[110:111], 1.0 op_sel_hi:[1,0]\n\t" \
  "v_pk_add_f32 v[112:113], v[112:113], 1.0 op_sel_hi:[1,0]\n\t" \
  "v_pk_add_f32 v[114:115], v[114:115], 1.0 op_sel_hi:[1,0]\n\t" \
  "v_rcp_f32 v108, v108\n\t" \
  "v_rcp_f32 v109, v109\n\t" \
  "v_rcp_f32 v110, v110\n\t" \
  "v_rcp_f32 v111, v111\n\t" \
  "v_rcp_f32 v112, v112\n\t" \
  "v_rcp_f32 v113, v113\n\t" \
  "v_rcp_f32 v114, v114\n\t" \
  "v_rcp_f32 v115, v115\n\t" \
  "v_pk_mul_f32 v[160:161], v[160:161], v[108:109]\n\t" \
  "v_pk_mul_f32 v[162:163], v[162:163], v[110:111]\n\t" \
  "v_pk_mul_f32 v[164:165], v[164:165], v[112:113]\n\t" \
  "v_pk_mul_f32 v[166:167], v[166:167], v[114:115]\n\t" \
  "v_cvt_pk_bf16_f32 v116, v160, v161\n\t" \
  "v_cvt_pk_bf16_f32 v117, v162, v163\n\t" \
  "v_cvt_pk_bf16_f32 v118, v164, v165\n\t" \
  "v_cvt_pk_bf16_f32 v119, v166, v167\n\t" \
  "global_store_dwordx4 v132, v[116:119], s[70:71] offset:0 sc1\n\t" \
  "v_exp_f32 v108, v168\n\t" \
  "v_exp_f32 v109, v169\n\t" \
  "v_exp_f32 v110, v170\n\t" \
  "v_exp_f32 v111, v171\n\t" \
  "v_exp_f32 v112, v172\n\t" \
  "v_exp_f32 v113, v173\n\t" \
  "v_exp_f32 v114, v174\n\t" \
  "v_exp_f32 v115, v175\n\t" \
  "v_pk_add_f32 v[108:109], v[108:109], 1.0 op_sel_hi:[1,0]\n\t" \
  "v_pk_add_f32 v[110:111], v[110:111], 1.0 op_sel_hi:[1,0]\n\t" \
  "v_pk_add_f32 v[112:113], v[112:113], 1.0 op_sel_hi:[1,0]\n\t" \
  "v_pk_add_f32 v[114:115], v[114:115], 1.0 op_sel_hi:[1,0]\n\t" \
  "v_rcp_f32 v108, v108\n\t" \
  "v_rcp_f32 v109, v109\n\t" \
  "v_rcp_f32 v110, v110\n\t" \
  "v_rcp_f32 v111, v111\n\t" \
  "v_rcp_f32 v112, v112\n\t" \
  "v_rcp_f32 v113, v113\n\t" \
  "v_rcp_f32 v114, v114\n\t" \
  "v_rcp_f32 v115, v115\n\t" \
  "v_pk_mul_f32 v[168:169], v[168:169], v[108:109]\n\t" \
  "v_pk_mul_f32 v[170:171], v[170:171], v[110:111]\n\t" \
  "v_pk_mul_f32 v[172:173], v[172:173], v[112:113]\n\t" \
  "v_pk_mul_f32 v[174:175], v[174:175], v[114:115]\n\t" \
  "v_cvt_pk_bf16_f32 v120, v168, v169\n\t" \
  "v_cvt_pk_bf16_f32 v121, v170, v171\n\t" \
  "v_cvt_pk_bf16_f32 v122, v172, v173\n\t" \
  "v_cvt_pk_bf16_f32 v123, v174, v175\n\t" \
  "global_store_dwordx4 v132, v[120:123], s[70:71] offset:1024 sc1\n\t" \
  "v_pk_mul_f32 v[100:101], v[84:85], v[34:35] op_sel:[0,1] op_sel_hi:[1,1]\n\t" \
  "v_pk_mul_f32 v[102:103], v[86:87], v[34:35] op_sel:[0,1] op_sel_hi:[1,1]\n\t" \
  "v_pk_mul_f32 v[104:105], v[88:89], v[34:35] op_sel:[0,1] op_sel_hi:[1,1]\n\t" \
  "v_pk_mul_f32 v[106:107], v[90:91], v[34:35] op_sel:[0,1] op_sel_hi:[1,1]\n\t" \
  "v_pk_fma_f32 v[208:209], v[208:209], v[32:33], v[100:101] op_sel:[0,1,0] op_sel_hi:[1,1,1]\n\t" \
  "v_pk_fma_f32 v[210:211], v[210:211], v[32:33], v[102:103] op_sel:[0,1,0] op_sel_hi:[1,1,1]\n\t" \
  "v_pk_fma_f32 v[212:213], v[212:213], v[32:33], v[104:105] op_sel:[0,1,0] op_sel_hi:[1,1,1]\n\t" \
  "v_pk_fma_f32 v[214:215], v[214:215], v[32:33], v[106:107] op_sel:[0,1,0] op_sel_hi:[1,1,1]\n\t" \
  "v_pk_mul_f32 v[100:101], v[92:93], v[34:35] op_sel:[0,1] op_sel_hi:[1,1]\n\t" \
  "v_pk_mul_f32 v[102:103], v[94:95], v[34:35] op_sel:[0,1] op_sel_hi:[1,1]\n\t" \
  "v_pk_mul_f32 v[104:105], v[96:97], v[34:35] op_sel:[0,1] op_sel_hi:[1,1]\n\t" \
  "v_pk_mul_f32 v[106:107], v[98:99], v[34:35] op_sel:[0,1] op_sel_hi:[1,1]\n\t" \
  "v_pk_fma_f32 v[216:217], v[216:217], v[32:33], v[100:101] op_sel:[0,1,0] op_sel_hi:[1,1,1]\n\t" \
  "v_pk_fma_f32 v[218:219], v[218:219], v[32:33], v[102:103] op_sel:[0,1,0] op_sel_hi:[1,1,1]\n\t" \
  "v_pk_fma_f32 v[220:221], v[220:221], v[32:33], v[104:105] op_sel:[0,1,0] op_sel_hi:[1,1,1]\n\t" \
  "v_pk_fma_f32 v[222:223], v[222:223], v[32:33], v[106:107] op_sel:[0,1,0] op_sel_hi:[1,1,1]\n\t" \
  "ds_read_b128 v[84:87], v18 offset:0\n\t" \
  "ds_read_b128 v[88:91], v18 offset:32\n\t" \
  "ds_read_b128 v[92:95], v18 offset:64\n\t" \
  "ds_read_b128 v[96:99], v18 offset:96\n\t" \
  "v_exp_f32 v108, v208\n\t" \
  "v_exp_f32 v109, v209\n\t" \
  "v_exp_f32 v110, v210\n\t" \
  "v_exp_f32 v111, v211\n\t" \
  "v_exp_f32 v112, v212\n\t" \
  "v_exp_f32 v113, v213\n\t" \
  "v_exp_f32 v114, v214\n\t" \
  "v_exp_f32 v115, v215\n\t" \
  "v_pk_add_f32 v[108:109], v[108:109], 1.0 op_sel_hi:[1,0]\n\t" \
  "v_pk_add_f32 v[110:111], v[110:111], 1.0 op_sel_hi:[1,0]\n\t" \
  "v_pk_add_f32 v[112:113], v[112:113], 1.0 op_sel_hi:[1,0]\n\t" \
  "v_pk_add_f32 v[114:115], v[114:115], 1.0 op_sel_hi:[1,0]\n\t" \
  "v_rcp_f32 v108, v108\n\t" \
  "v_rcp_f32 v109, v109\n\t" \
  "v_rcp_f32 v110, v110\n\t" \
  "v_rcp_f32 v111, v111\n\t" \
  "v_rcp_f32 v112, v112\n\t" \
  "v_rcp_f32 v113, v113\n\t" \
  "v_rcp_f32 v114, v114\n\t" \
  "v_rcp_f32 v115, v115\n\t" \
  "v_pk_mul_f32 v[208:209], v[208:209], v[108:109]\n\t" \
  "v_pk_mul_f32 v[210:211], v[210:211], v[110:111]\n\t" \
  "v_pk_mul_f32 v[212:213], v[212:213], v[112:113]\n\t" \
  "v_pk_mul_f32 v[214:215], v[214:215], v[114:115]\n\t" \
  "v_cvt_pk_bf16_f32 v124, v208, v209\n\t" \
  "v_cvt_pk_bf16_f32 v125, v210, v211\n\t" \
  "v_cvt_pk_bf16_f32 v126, v212, v213\n\t" \
  "v_cvt_pk_bf16_f32 v127, v214, v215\n\t" \
  "global_store_dwordx4 v132, v[124:127], s[72:73] offset:0 sc1\n\t" \
  "v_exp_f32 v108, v216\n\t" \
  "v_exp_f32 v109, v217\n\t" \
  "v_exp_f32 v110, v218\n\t" \
  "v_exp_f32 v111, v219\n\t" \
  "v_exp_f32 v112, v220\n\t" \
  "v_exp_f32 v113, v221\n\t" \
  "v_exp_f32 v114, v222\n\t" \
  "v_exp_f32 v115, v223\n\t" \
  "v_pk_add_f32 v[108:109], v[108:109], 1.0 op_sel_hi:[1,0]\n\t" \
  "v_pk_add_f32 v[110:111], v[110:111], 1.0 op_sel_hi:[1,0]\n\t" \
  "v_pk_add_f32 v[112:113], v[112:113], 1.0 op_sel_hi:[1,0]\n\t" \
  "v_pk_add_f32 v[114:115], v[114:115], 1.0 op_sel_hi:[1,0]\n\t" \
  "v_rcp_f32 v108, v108\n\t" \
  "v_rcp_f32 v109, v109\n\t" \
  "v_rcp_f32 v110, v110\n\t" \
  "v_rcp_f32 v111, v111\n\t" \
  "v_rcp_f32 v112, v112\n\t" \
  "v_rcp_f32 v113, v113\n\t" \
  "v_rcp_f32 v114, v114\n\t" \
  "v_rcp_f32 v115, v115\n\t" \
  "v_pk_mul_f32 v[216:217], v[216:217], v[108:109]\n\t" \
  "v_pk_mul_f32 v[218:219], v[218:219], v[110:111]\n\t" \
  "v_pk_mul_f32 v[220:221], v[220:221], v[112:113]\n\t" \
  "v_pk_mul_f32 v[222:223], v[222:223], v[114:115]\n\t" \
  "v_cvt_pk_bf16_f32 v128, v216, v217\n\t" \
  "v_cvt_pk_bf16_f32 v129, v218, v219\n\t" \
  "v_cvt_pk_bf16_f32 v130, v220, v221\n\t" \
  "v_cvt_pk_bf16_f32 v131, v222, v223\n\t" \
  "global_store_dwordx4 v132, v[128:131], s[72:73] offset:1024 sc1\n\t" \
  "s_waitcnt lgkmcnt(7)\n\t" \
  "v_pk_mul_f32 v[100:101], v[68:69], v[30:31] op_sel:[0,1] op_sel_hi:[1,1]\n\t" \
  "v_pk_mul_f32 v[102:103], v[70:71], v[30:31] op_sel:[0,1] op_sel_hi:[1,1]\n\t" \
  "s_waitcnt lgkmcnt(6)\n\t" \
  "v_pk_mul_f32 v[104:105], v[72:73], v[30:31] op_sel:[0,1] op_sel_hi:[1,1]\n\t" \
  "v_pk_mul_f32 v[106:107], v[74:75], v[30:31] op_sel:[0,1] op_sel_hi:[1,1]\n\t" \
  "v_pk_fma_f32 v[176:177], v[176:177], v[28:29], v[100:101] op_sel:[0,1,0] op_sel_hi:[1,1,1]\n\t" \
  "v_pk_fma_f32 v[178:179], v[178:179], v[28:29], v[102:103] op_sel:[0,1,0] op_sel_hi:[1,1,1]\n\t" \
  "v_pk_fma_f32 v[180:181], v[180:181], v[28:29], v[104:105] op_sel:[0,1,0] op_sel_hi:[1,1,1]\n\t" \
  "v_pk_fma_f32 v[182:183], v[182:183], v[28:29], v[106:107] op_sel:[0,1,0] op_sel_hi:[1,1,1]\n\t" \
  "s_waitcnt lgkmcnt(5)\n\t" \
  "v_pk_mul_f32 v[100:101], v[76:77], v[30:31] op_sel:[0,1] op_sel_hi:[1,1]\n\t" \
  "v_pk_mul_f32 v[102:103], v[78:79], v[30:31] op_sel:[0,1] op_sel_hi:[1,1]\n\t" \
  "s_waitcnt lgkmcnt(4)\n\t" \
  "v_pk_mul_f32 v[104:105], v[80:81], v[30:31] op_sel:[0,1] op_sel_hi:[1,1]\n\t" \
  "v_pk_mul_f32 v[106:107], v[82:83], v[30:31] op_sel:[0,1] op_sel_hi:[1,1]\n\t" \
  "v_pk_fma_f32 v[184:185], v[184:185], v[28:29], v[100:101] op_sel:[0,1,0] op_sel_hi:[1,1,1]\n\t" \
  "v_pk_fma_f32 v[186:187], v[186:187], v[28:29], v[102:103] op_sel:[0,1,0] op_sel_hi:[1,1,1]\n\t" \
  "v_pk_fma_f32 v[188:189], v[188:189], v[28:29], v[104:105] op_sel:[0,1,0] op_sel_hi:[1,1,1]\n\t" \
  "v_pk_fma_f32 v[190:191], v[190:191], v[28:29], v[106:107] op_sel:[0,1,0] op_sel_hi:[1,1,1]\n\t" \
  "v_exp_f32 v108, v176\n\t" \
  "v_exp_f32 v109, v177\n\t" \
  "v_exp_f32 v110, v178\n\t" \
  "v_exp_f32 v111, v179\n\t" \
  "v_exp_f32 v112, v180\n\t" \
  "v_exp_f32 v113, v181\n\t" \
  "v_exp_f32 v114, v182\n\t" \
  "v_exp_f32 v115, v183\n\t" \
  "v_pk_add_f32 v[108:109], v[108:109], 1.0 op_sel_hi:[1,0]\n\t" \
  "v_pk_add_f32 v[110:111], v[110:111], 1.0 op_sel_hi:[1,0]\n\t" \
  "v_pk_add_f32 v[112:113], v[112:113], 1.0 op_sel_hi:[1,0]\n\t" \
  "v_pk_add_f32 v[114:115], v[114:115], 1.0 op_sel_hi:[1,0]\n\t" \
  "v_rcp_f32 v108, v108\n\t" \
  "v_rcp_f32 v109, v109\n\t" \
  "v_rcp_f32 v110, v110\n\t" \
  "v_rcp_f32 v111, v111\n\t" \
  "v_rcp_f32 v112, v112\n\t" \
  "v_rcp_f32 v113, v113\n\t" \
  "v_rcp_f32 v114, v114\n\t" \
  "v_rcp_f32 v115, v115\n\t" \
  "v_pk_mul_f32 v[176:177], v[176:177], v[108:109]\n\t" \
  "v_pk_mul_f32 v[178:179], v[178:179], v[110:111]\n\t" \
  "v_pk_mul_f32 v[180:181], v[180:181], v[112:113]\n\t" \
  "v_pk_mul_f32 v[182:183], v[182:183], v[114:115]\n\t" \
  "v_cvt_pk_bf16_f32 v116, v176, v177\n\t" \
  "v_cvt_pk_bf16_f32 v117, v178, v179\n\t" \
  "v_cvt_pk_bf16_f32 v118, v180, v181\n\t" \
  "v_cvt_pk_bf16_f32 v119, v182, v183\n\t" \
  "global_store_dwordx4 v133, v[116:119], s[70:71] offset:0 sc1\n\t" \
  "v_exp_f32 v108, v184\n\t" \
  "v_exp_f32 v109, v185\n\t" \
  "v_exp_f32 v110, v186\n\t" \
  "v_exp_f32 v111, v187\n\t" \
  "v_exp_f32 v112, v188\n\t" \
  "v_exp_f32 v113, v189\n\t" \
  "v_exp_f32 v114, v190\n\t" \
  "v_exp_f32 v115, v191\n\t" \
  "v_pk_add_f32 v[108:109], v[108:109], 1.0 op_sel_hi:[1,0]\n\t" \
  "v_pk_add_f32 v[110:111], v[110:111], 1.0 op_sel_hi:[1,0]\n\t" \
  "v_pk_add_f32 v[112:113], v[112:113], 1.0 op_sel_hi:[1,0]\n\t" \
  "v_pk_add_f32 v[114:115], v[114:115], 1.0 op_sel_hi:[1,0]\n\t" \
  "v_rcp_f32 v108, v108\n\t" \
  "v_rcp_f32 v109, v109\n\t" \
  "v_rcp_f32 v110, v110\n\t" \
  "v_rcp_f32 v111, v111\n\t" \
  "v_rcp_f32 v112, v112\n\t" \
  "v_rcp_f32 v113, v113\n\t" \
  "v_rcp_f32 v114, v114\n\t" \
  "v_rcp_f32 v115, v115\n\t" \
  "v_pk_mul_f32 v[184:185], v[184:185], v[108:109]\n\t" \
  "v_pk_mul_f32 v[186:187], v[186:187], v[110:111]\n\t" \
  "v_pk_mul_f32 v[188:189], v[188:189], v[112:113]\n\t" \
  "v_pk_mul_f32 v[190:191], v[190:191], v[114:115]\n\t" \
  "v_cvt_pk_bf16_f32 v120, v184, v185\n\t" \
  "v_cvt_pk_bf16_f32 v121, v186, v187\n\t" \
  "v_cvt_pk_bf16_f32 v122, v188, v189\n\t" \
  "v_cvt_pk_bf16_f32 v123, v190, v191\n\t" \
  "global_store_dwordx4 v133, v[120:123], s[70:71] offset:1024 sc1\n\t" \
  "v_pk_mul_f32 v[100:101], v[68:69], v[34:35] op_sel:[0,1] op_sel_hi:[1,1]\n\t" \
  "v_pk_mul_f32 v[102:103], v[70:71], v[34:35] op_sel:[0,1] op_sel_hi:[1,1]\n\t" \
  "v_pk_mul_f32 v[104:105], v[72:73], v[34:35] op_sel:[0,1] op_sel_hi:[1,1]\n\t" \
  "v_pk_mul_f32 v[106:107], v[74:75], v[34:35] op_sel:[0,1] op_sel_hi:[1,1]\n\t" \
  "v_pk_fma_f32 v[224:225], v[224:225], v[32:33], v[100:101] op_sel:[0,1,0] op_sel_hi:[1,1,1]\n\t" \
  "v_pk_fma_f32 v[226:227], v[226:227], v[32:33], v[102:103] op_sel:[0,1,0] op_sel_hi:[1,1,1]\n\t" \
  "v_pk_fma_f32 v[228:229], v[228:229], v[32:33], v[104:105] op_sel:[0,1,0] op_sel_hi:[1,1,1]\n\t" \
  "v_pk_fma_f32 v[230:231], v[230:231], v[32:33], v[106:107] op_sel:[0,1,0] op_sel_hi:[1,1,1]\n\t" \
  "v_pk_mul_f32 v[100:101], v[76:77], v[34:35] op_sel:[0,1] op_sel_hi:[1,1]\n\t" \
  "v_pk_mul_f32 v[102:103], v[78:79], v[34:35] op_sel:[0,1] op_sel_hi:[1,1]\n\t" \
  "v_pk_mul_f32 v[104:105], v[80:81], v[34:35] op_sel:[0,1] op_sel_hi:[1,1]\n\t" \
  "v_pk_mul_f32 v[106:107], v[82:83], v[34:35] op_sel:[0,1] op_sel_hi:[1,1]\n\t" \
  "v_pk_fma_f32 v[232:233], v[232:233], v[32:33], v[100:101] op_sel:[0,1,0] op_sel_hi:[1,1,1]\n\t" \
  "v_pk_fma_f32 v[234:235], v[234:235], v[32:33], v[102:103] op_sel:[0,1,0] op_sel_hi:[1,1,1]\n\t" \
  "v_pk_fma_f32 v[236:237], v[236:237], v[32:33], v[104:105] op_sel:[0,1,0] op_sel_hi:[1,1,1]\n\t" \
  "v_pk_fma_f32 v[238:239], v[238:239], v[32:33], v[106:107] op_sel:[0,1,0] op_sel_hi:[1,1,1]\n\t" \
  "v_exp_f32 v108, v224\n\t" \
  "v_exp_f32 v109, v225\n\t" \
  "v_exp_f32 v110, v226\n\t" \
  "v_exp_f32 v111, v227\n\t" \
  "v_exp_f32 v112, v228\n\t" \
  "v_exp_f32 v113, v229\n\t" \
  "v_exp_f32 v114, v230\n\t" \
  "v_exp_f32 v115, v231\n\t" \
  "v_pk_add_f32 v[108:109], v[108:109], 1.0 op_sel_hi:[1,0]\n\t" \
  "s_waitcnt vmcnt(2)\n\t" \
  "v_or_b32 v138, 2, v138\n\t" \
  "s_mov_b64 exec, 1\n\t" \
  "global_store_dwordx2 v12, v[138:139], s[44:45] sc1\n\t" \
  "s_mov_b64 exec, -1\n\t" \
  "s_nop 0\n\t" \
  "v_and_b32 v138, -8, v138\n\t" \
  "s_barrier\n\t" \
  "v_pk_add_f32 v[110:111], v[110:111], 1.0 op_sel_hi:[1,0]\n\t" \
  "v_pk_add_f32 v[112:113], v[112:113], 1.0 op_sel_hi:[1,0]\n\t" \
  "v_pk_add_f32 v[114:115], v[114:115], 1.0 op_sel_hi:[1,0]\n\t" \
  "v_rcp_f32 v108, v108\n\t" \
  "v_rcp_f32 v109, v109\n\t" \
  "v_rcp_f32 v110, v110\n\t" \
  "s_lshl_b32 s33, s19, 12\n\t" \
  "s_add_u32 s33, s33, 0\n\t" \
  "s_add_u32 s76, s74, s33\n\t" \
  "s_addc_u32 s77, s75, 0\n\t" \
  "global_load_dwordx4 a[0:3], v0, s[76:77] offset:0 sc1\n\t" \
  "global_load_dwordx4 a[4:7], v0, s[76:77] offset:1024 sc1\n\t" \
  "v_rcp_f32 v111, v111\n\t" \
  "v_rcp_f32 v112, v112\n\t" \
  "v_rcp_f32 v113, v113\n\t" \
  "v_rcp_f32 v114, v114\n\t" \
  "v_rcp_f32 v115, v115\n\t" \
  "v_pk_mul_f32 v[224:225], v[224:225], v[108:109]\n\t" \
  "global_load_dwordx4 a[8:11], v0, s[76:77] offset:2048 sc1\n\t" \
  "global_load_dwordx4 a[12:15], v0, s[76:77] offset:3072 sc1\n\t" \
  "v_pk_mul_f32 v[226:227], v[226:227], v[110:111]\n\t" \
  "v_pk_mul_f32 v[228:229], v[228:229], v[112:113]\n\t" \
  "v_pk_mul_f32 v[230:231], v[230:231], v[114:115]\n\t" \
  "v_cvt_pk_bf16_f32 v124, v224, v225\n\t" \
  "v_cvt_pk_bf16_f32 v125, v226, v227\n\t" \
  "v_cvt_pk_bf16_f32 v126, v228, v229\n\t" \
  "global_load_dwordx2 v[142:143], v13, s[44:45] sc1\n\t" \
  "s_lshl_b32 s33, s19, 12\n\t" \
  "s_add_u32 s33, s33, 8192\n\t" \
  "s_add_u32 s76, s74, s33\n\t" \
  "s_addc_u32 s77, s75, 0\n\t" \
  "global_load_dwordx4 a[16:19], v0, s[76:77] offset:0 sc1\n\t" \
  "global_load_dwordx4 a[20:23], v0, s[76:77] offset:1024 sc1\n\t" \
  "v_cvt_pk_bf16_f32 v127, v230, v231\n\t" \
  "global_store_dwordx4 v133, v[124:127], s[72:73] offset:0 sc1\n\t" \
  "v_exp_f32 v108, v232\n\t" \
  "v_exp_f32 v109, v233\n\t" \
  "v_exp_f32 v110, v234\n\t" \
  "v_exp_f32 v111, v235\n\t" \
  "global_load_dwordx4 a[24:27], v0, s[76:77] offset:2048 sc1\n\t" \
  "global_load_dwordx4 a[28:31], v0, s[76:77] offset:3072 sc1\n\t" \
  "v_exp_f32 v112, v236\n\t" \
  "v_exp_f32 v113, v237\n\t" \
  "v_exp_f32 v114, v238\n\t" \
  "v_exp_f32 v115, v239\n\t" \
  "v_pk_add_f32 v[108:109], v[108:109], 1.0 op_sel_hi:[1,0]\n\t" \
  "v_pk_add_f32 v[110:111], v[110:111], 1.0 op_sel_hi:[1,0]\n\t" \
  "v_pk_add_f32 v[112:113], v[112:113], 1.0 op_sel_hi:[1,0]\n\t" \
  "v_pk_add_f32 v[114:115], v[114:115], 1.0 op_sel_hi:[1,0]\n\t" \
  "v_rcp_f32 v108, v108\n\t" \
  "v_rcp_f32 v109, v109\n\t" \
  "v_rcp_f32 v110, v110\n\t" \
  "v_rcp_f32 v111, v111\n\t" \
  "v_rcp_f32 v112, v112\n\t" \
  "v_rcp_f32 v113, v113\n\t" \
  "v_rcp_f32 v114, v114\n\t" \
  "v_rcp_f32 v115, v115\n\t" \
  "v_pk_mul_f32 v[232:233], v[232:233], v[108:109]\n\t" \
  "v_pk_mul_f32 v[234:235], v[234:235], v[110:111]\n\t" \
  "v_pk_mul_f32 v[236:237], v[236:237], v[112:113]\n\t" \
  "v_pk_mul_f32 v[238:239], v[238:239], v[114:115]\n\t" \
  "v_cvt_pk_bf16_f32 v128, v232, v233\n\t" \
  "v_cvt_pk_bf16_f32 v129, v234, v235\n\t" \
  "v_cvt_pk_bf16_f32 v130, v236, v237\n\t" \
  "v_cvt_pk_bf16_f32 v131, v238, v239\n\t" \
  "global_store_dwordx4 v133, v[128:131], s[72:73] offset:1024 sc1\n\t" \
  "s_waitcnt lgkmcnt(3)\n\t" \
  "v_pk_mul_f32 v[100:101], v[84:85], v[30:31] op_sel:[0,1] op_sel_hi:[1,1]\n\t" \
  "v_pk_mul_f32 v[102:103], v[86:87], v[30:31] op_sel:[0,1] op_sel_hi:[1,1]\n\t" \
  "s_waitcnt lgkmcnt(2)\n\t" \
  "v_pk_mul_f32 v[104:105], v[88:89], v[30:31] op_sel:[0,1] op_sel_hi:[1,1]\n\t" \
  "v_pk_mul_f32 v[106:107], v[90:91], v[30:31] op_sel:[0,1] op_sel_hi:[1,1]\n\t" \
  "v_pk_fma_f32 v[240:241], v[240:241], v[28:29], v[100:101] op_sel:[0,1,0] op_sel_hi:[1,1,1]\n\t" \
  "v_pk_fma_f32 v[242:243], v[242:243], v[28:29], v[102:103] op_sel:[0,1,0] op_sel_hi:[1,1,1]\n\t" \
  "v_pk_fma_f32 v[244:245], v[244:245], v[28:29], v[104:105] op_sel:[0,1,0] op_sel_hi:[1,1,1]\n\t" \
  "v_pk_fma_f32 v[246:247], v[246:247], v[28:29], v[106:107] op_sel:[0,1,0] op_sel_hi:[1,1,1]\n\t" \
  "s_waitcnt lgkmcnt(1)\n\t" \
  "v_pk_mul_f32 v[100:101], v[92:93], v[30:31] op_sel:[0,1] op_sel_hi:[1,1]\n\t" \
  "v_pk_mul_f32 v[102:103], v[94:95], v[30:31] op_sel:[0,1] op_sel_hi:[1,1]\n\t" \
  "s_waitcnt lgkmcnt(0)\n\t" \
  "v_pk_mul_f32 v[104:105], v[96:97], v[30:31] op_sel:[0,1] op_sel_hi:[1,1]\n\t" \
  "v_pk_mul_f32 v[106:107], v[98:99], v[30:31] op_sel:[0,1] op_sel_hi:[1,1]\n\t" \
  "v_pk_fma_f32 v[248:249], v[248:249], v[28:29], v[100:101] op_sel:[0,1,0] op_sel_hi:[1,1,1]\n\t" \
  "v_pk_fma_f32 v[250:251], v[250:251], v[28:29], v[102:103] op_sel:[0,1,0] op_sel_hi:[1,1,1]\n\t" \
  "v_pk_fma_f32 v[252:253], v[252:253], v[28:29], v[104:105] op_sel:[0,1,0] op_sel_hi:[1,1,1]\n\t" \
  "v_pk_fma_f32 v[254:255], v[254:255], v[28:29], v[106:107] op_sel:[0,1,0] op_sel_hi:[1,1,1]\n\t" \
  "v_exp_f32 v108, v240\n\t" \
  "v_exp_f32 v109, v241\n\t" \
  "v_exp_f32 v110, v242\n\t" \
  "v_exp_f32 v111, v243\n\t" \
  "v_exp_f32 v112, v244\n\t" \
  "v_exp_f32 v113, v245\n\t" \
  "v_exp_f32 v114, v246\n\t" \
  "v_exp_f32 v115, v247\n\t" \
  "v_pk_add_f32 v[108:109], v[108:109], 1.0 op_sel_hi:[1,0]\n\t" \
  "v_pk_add_f32 v[110:111], v[110:111], 1.0 op_sel_hi:[1,0]\n\t" \
  "v_pk_add_f32 v[112:113], v[112:113], 1.0 op_sel_hi:[1,0]\n\t" \
  "v_pk_add_f32 v[114:115], v[114:115], 1.0 op_sel_hi:[1,0]\n\t" \
  "v_rcp_f32 v108, v108\n\t" \
  "v_rcp_f32 v109, v109\n\t" \
  "v_rcp_f32 v110, v110\n\t" \
  "v_rcp_f32 v111, v111\n\t" \
  "v_rcp_f32 v112, v112\n\t" \
  "v_rcp_f32 v113, v113\n\t" \
  "v_rcp_f32 v114, v114\n\t" \
  "v_rcp_f32 v115, v115\n\t" \
  "v_pk_mul_f32 v[240:241], v[240:241], v[108:109]\n\t" \
  "v_pk_mul_f32 v[242:243], v[242:243], v[110:111]\n\t" \
  "v_pk_mul_f32 v[244:245], v[244:245], v[112:113]\n\t" \
  "v_pk_mul_f32 v[246:247], v[246:247], v[114:115]\n\t" \
  "v_cvt_pk_bf16_f32 v116, v240, v241\n\t" \
  "v_cvt_pk_bf16_f32 v117, v242, v243\n\t" \
  "v_cvt_pk_bf16_f32 v118, v244, v245\n\t" \
  "v_cvt_pk_bf16_f32 v119, v246, v247\n\t" \
  "global_store_dwordx4 v0, v[116:119], s[78:79] offset:0 sc1\n\t" \
  "v_exp_f32 v108, v248\n\t" \
  "v_exp_f32 v109, v249\n\t" \
  "v_exp_f32 v110, v250\n\t" \
  "v_exp_f32 v111, v251\n\t" \
  "v_exp_f32 v112, v252\n\t" \
  "v_exp_f32 v113, v253\n\t" \
  "v_exp_f32 v114, v254\n\t" \
  "v_exp_f32 v115, v255\n\t" \
  "v_pk_add_f32 v[108:109], v[108:109], 1.0 op_sel_hi:[1,0]\n\t" \
  "v_pk_add_f32 v[110:111], v[110:111], 1.0 op_sel_hi:[1,0]\n\t" \
  "v_pk_add_f32 v[112:113], v[112:113], 1.0 op_sel_hi:[1,0]\n\t" \
  "v_pk_add_f32 v[114:115], v[114:115], 1.0 op_sel_hi:[1,0]\n\t" \
  "v_rcp_f32 v108, v108\n\t" \
  "v_rcp_f32 v109, v109\n\t" \
  "v_rcp_f32 v110, v110\n\t" \
  "v_rcp_f32 v111, v111\n\t" \
  "v_rcp_f32 v112, v112\n\t" \
  "v_rcp_f32 v113, v113\n\t" \
  "v_rcp_f32 v114, v114\n\t" \
  "v_rcp_f32 v115, v115\n\t" \
  "v_pk_mul_f32 v[248:249], v[248:249], v[108:109]\n\t" \
  "v_pk_mul_f32 v[250:251], v[250:251], v[110:111]\n\t" \
  "v_pk_mul_f32 v[252:253], v[252:253], v[112:113]\n\t" \
  "v_pk_mul_f32 v[254:255], v[254:255], v[114:115]\n\t" \
  "v_cvt_pk_bf16_f32 v120, v248, v249\n\t" \
  "v_cvt_pk_bf16_f32 v121, v250, v251\n\t" \
  "v_cvt_pk_bf16_f32 v122, v252, v253\n\t" \
  "v_cvt_pk_bf16_f32 v123, v254, v255\n\t" \
  "global_store_dwordx4 v0, v[120:123], s[78:79] offset:1024 sc1\n\t" \
  "s_waitcnt vmcnt(8)\n\t" \
  "s_or_b32 s12, s12, 2\n\t" \
  "v_cmp_ge_u64 s[58:59], s[68:69], v[142:143]\n\t" \
  "v_cmp_le_u64 vcc, s[12:13], v[142:143]\n\t" \
  "s_nop 1\n\t" \
  "s_and_b32 s58, s58, vcc_lo\n\t" \
  "s_and_b32 s12, s12, -8\n\t" \
  "s_and_b32 s58, s58, 240\n\t" \
  "s_cmp_eq_u32 s58, 240\n\t" \
  "s_cbranch_scc0 P0FS%=\n\t" \
  "P0FB%=:\n\t" \
  "s_xor_b32 s33, s19, 1\n\t" \
  "s_lshl_b32 s33, s33, 12\n\t" \
  "s_add_u32 s33, s33, 0\n\t" \
  "s_add_u32 s76, s74, s33\n\t" \
  "s_addc_u32 s77, s75, 0\n\t" \
  "global_load_dwordx4 a[32:35], v0, s[76:77] offset:0 sc1\n\t" \
  "global_load_dwordx4 a[36:39], v0, s[76:77] offset:1024 sc1\n\t" \
  "global_load_dwordx4 a[40:43], v0, s[76:77] offset:2048 sc1\n\t" \
  "global_load_dwordx4 a[44:47], v0, s[76:77] offset:3072 sc1\n\t" \
  "s_xor_b32 s33, s19, 1\n\t" \
  "s_lshl_b32 s33, s33, 12\n\t" \
  "s_add_u32 s33, s33, 8192\n\t" \
  "s_add_u32 s76, s74, s33\n\t" \
  "s_addc_u32 s77, s75, 0\n\t" \
  "global_load_dwordx4 a[48:51], v0, s[76:77] offset:0 sc1\n\t" \
  "global_load_dwordx4 a[52:55], v0, s[76:77] offset:1024 sc1\n\t" \
  "global_load_dwordx4 a[56:59], v0, s[76:77] offset:2048 sc1\n\t" \
  "global_load_dwordx4 a[60:63], v0, s[76:77] offset:3072 sc1\n\t" \
  "s_waitcnt lgkmcnt(0)\n\t" \
  "s_barrier\n\t" \
  "ds_read_b64_tr_b16 v[144:145], v1 offset:0\n\t" \
  "ds_read_b64_tr_b16 v[146:147], v1 offset:2048\n\t" \
  "ds_read_b64_tr_b16 v[148:149], v2 offset:0\n\t" \
  "ds_read_b64_tr_b16 v[150:151], v2 offset:2048\n\t" \
  "ds_read_b64_tr_b16 v[152:153], v3 offset:0\n\t" \
  "ds_read_b64_tr_b16 v[154:155], v3 offset:2048\n\t" \
  "ds_read_b64_tr_b16 v[156:157], v1 offset:4096\n\t" \
  "ds_read_b64_tr_b16 v[158:159], v1 offset:6144\n\t" \
  "ds_read_b64_tr_b16 v[160:161], v2 offset:4096\n\t" \
  "ds_read_b64_tr_b16 v[162:163], v2 offset:6144\n\t" \
  "ds_read_b64_tr_b16 v[164:165], v3 offset:4096\n\t" \
  "ds_read_b64_tr_b16 v[166:167], v3 offset:6144\n\t" \
  "s_waitcnt lgkmcnt(8)\n\t" \
  "v_mfma_f32_32x32x16_bf16 v[72:87], v[144:147], a[0:3], 0\n\t" \
  "ds_read_b64_tr_b16 v[168:169], v1 offset:8192\n\t" \
  "ds_read_b64_tr_b16 v[170:171], v1 offset:10240\n\t" \
  "v_lshrrev_b32 v57, 9, v0\n\t" \
  "v_bfe_u32 v59, v0, 8, 1\n\t" \
  "v_mfma_f32_32x32x16_bf16 v[88:103], v[148:151], a[0:3], 0\n\t" \
  "ds_read_b64_tr_b16 v[172:173], v2 offset:8192\n\t" \
  "ds_read_b64_tr_b16 v[174:175], v2 offset:10240\n\t" \
  "s_add_u32 s33, s97, 1\n\t" \
  "s_and_b32 s33, s33, 3\n\t" \
  "s_waitcnt lgkmcnt(8)\n\t" \
  "v_mfma_f32_32x32x16_bf16 v[104:119], v[152:155], a[0:3], 0\n\t" \
  "ds_read_b64_tr_b16 v[176:177], v3 offset:8192\n\t" \
  "ds_read_b64_tr_b16 v[178:179], v3 offset:10240\n\t" \
  "s_lshl2_add_u32 s33, s33, s26\n\t" \
  "s_lshl_b32 s34, s33, 11\n\t" \
  "v_mfma_f32_32x32x16_bf16 v[72:87], v[156:159], a[4:7], v[72:87]\n\t" \
  "ds_read_b64_tr_b16 v[180:181], v1 offset:12288\n\t" \
  "ds_read_b64_tr_b16 v[182:183], v1 offset:14336\n\t" \
  "s_add_u32 s92, s66, 65536\n\t" \
  "s_addc_u32 s93, s67, 0\n\t" \
  "s_add_u32 m0, s83, 57344\n\t" \
  "s_nop 0\n\t" \
  "global_load_lds_dwordx4 v64, s[92:93]\n\t" \
  "s_add_u32 s34, s34, 11534336\n\t" \
  "v_add_u32 v17, s34, v0\n\t" \
  "s_waitcnt lgkmcnt(8)\n\t" \
  "v_mfma_f32_32x32x16_bf16 v[88:103], v[160:163], a[4:7], v[88:103]\n\t" \
  "ds_read_b64_tr_b16 v[184:185], v2 offset:12288\n\t" \
  "ds_read_b64_tr_b16 v[186:187], v2 offset:14336\n\t" \
  "s_lshl_b32 s33, s33, 6\n\t" \
  "s_add_u32 s16, s33, 10485760\n\t" \
  "v_mfma_f32_32x32x16_bf16 v[104:119], v[164:167], a[4:7], v[104:119]\n\t" \
  "ds_read_b64_tr_b16 v[188:189], v3 offset:12288\n\t" \
  "ds_read_b64_tr_b16 v[190:191], v3 offset:14336\n\t" \
  "global_load_lds_dwordx4 v65, s[92:93] offset:1024\n\t" \
  "s_add_u32 s33, s97, 2\n\t" \
  "s_and_b32 s33, s33, 3\n\t" \
  "s_waitcnt lgkmcnt(8)\n\t" \
  "v_mfma_f32_32x32x16_bf16 v[72:87], v[168:171], a[8:11], v[72:87]\n\t" \
  "ds_read_b64_tr_b16 v[144:145], v1 offset:16384\n\t" \
  "ds_read_b64_tr_b16 v[146:147], v1 offset:18432\n\t" \
  "s_lshl2_add_u32 s33, s33, s26\n\t" \
  "s_lshl_b32 s34, s33, 11\n\t" \
  "v_mfma_f32_32x32x16_bf16 v[88:103], v[172:175], a[8:11], v[88:103]\n\t" \
  "ds_read_b64_tr_b16 v[148:149], v2 offset:16384\n\t" \
  "ds_read_b64_tr_b16 v[150:151], v2 offset:18432\n\t" \
  "s_add_u32 s94, s66, 163840\n\t" \
  "s_addc_u32 s95, s67, 0\n\t" \
  "s_add_u32 m0, s83, 65536\n\t" \
  "s_nop 0\n\t" \
  "global_load_lds_dwordx4 v64, s[94:95]\n\t" \
  "s_add_u32 s34, s34, 13631488\n\t" \
  "v_add_u32 v18, s34, v0\n\t" \
  "s_waitcnt lgkmcnt(8)\n\t" \
  "v_mfma_f32_32x32x16_bf16 v[104:119], v[176:179], a[8:11], v[104:119]\n\t" \
  "ds_read_b64_tr_b16 v[152:153], v3 offset:16384\n\t" \
  "ds_read_b64_tr_b16 v[154:155], v3 offset:18432\n\t" \
  "s_lshl_b32 s33, s33, 6\n\t" \
  "s_add_u32 s17, s33, 10485768\n\t" \
  "v_mfma_f32_32x32x16_bf16 v[72:87], v[180:183], a[12:15], v[72:87]\n\t" \
  "ds_read_b64_tr_b16 v[156:157], v1 offset:20480\n\t" \
  "ds_read_b64_tr_b16 v[158:159], v1 offset:22528\n\t" \
  "global_load_lds_dwordx4 v65, s[94:95] offset:1024\n\t" \
  "s_add_u32 s33, s97, 3\n\t" \
  "s_and_b32 s33, s33, 3\n\t" \
  "s_waitcnt lgkmcnt(8)\n\t" \
  "v_mfma_f32_32x32x16_bf16 v[88:103], v[184:187], a[12:15], v[88:103]\n\t" \
  "ds_read_b64_tr_b16 v[160:161], v2 offset:20480\n\t" \
  "ds_read_b64_tr_b16 v[162:163], v2 offset:22528\n\t" \
  "s_lshl2_add_u32 s33, s33, s26\n\t" \
  "s_lshl_b32 s34, s33, 11\n\t" \
  "v_mfma_f32_32x32x16_bf16 v[104:119], v[188:191], a[12:15], v[104:119]\n\t" \
  "ds_read_b64_tr_b16 v[164:165], v3 offset:20480\n\t" \
  "ds_read_b64_tr_b16 v[166:167], v3 offset:22528\n\t" \
  "s_add_u32 s92, s66, 196608\n\t" \
  "s_addc_u32 s93, s67, 0\n\t" \
  "s_add_u32 m0, s83, 73728\n\t" \
  "s_nop 0\n\t" \
  "global_load_lds_dwordx4 v64, s[92:93]\n\t" \
  "s_add_u32 s34, s34, 15728640\n\t" \
  "v_add_u32 v19, s34, v0\n\t" \
  "s_waitcnt lgkmcnt(8)\n\t" \
  "s_waitcnt vmcnt(20)\n\t" \
  "v_mfma_f32_32x32x16_bf16 v[72:87], v[144:147], a[16:19], v[72:87]\n\t" \
  "ds_read_b64_tr_b16 v[168:169], v1 offset:24576\n\t" \
  "ds_read_b64_tr_b16 v[170:171], v1 offset:26624\n\t" \
  "s_lshl_b32 s33, s33, 6\n\t" \
  "s_add_u32 s18, s33, 10485776\n\t" \
  "v_mfma_f32_32x32x16_bf16 v[88:103], v[148:151], a[16:19], v[88:103]\n\t" \
  "ds_read_b64_tr_b16 v[172:173], v2 offset:24576\n\t" \
  "ds_read_b64_tr_b16 v[174:175], v2 offset:26624\n\t" \
  "global_load_lds_dwordx4 v65, s[92:93] offset:1024\n\t" \
  "s_add_u32 s33, s97, 4\n\t" \
  "s_and_b32 s33, s33, 3\n\t" \
  "s_waitcnt lgkmcnt(8)\n\t" \
  "v_mfma_f32_32x32x16_bf16 v[104:119], v[152:155], a[16:19], v[104:119]\n\t" \
  "ds_read_b64_tr_b16 v[176:177], v3 offset:24576\n\t" \
  "ds_read_b64_tr_b16 v[178:179], v3 offset:26624\n\t" \
  "s_lshl2_add_u32 s33, s33, s26\n\t" \
  "s_lshl_b32 s34, s33, 11\n\t" \
  "s_waitcnt vmcnt(20)\n\t" \
  "v_mfma_f32_32x32x16_bf16 v[72:87], v[156:159], a[20:23], v[72:87]\n\t" \
  "ds_read_b64_tr_b16 v[180:181], v1 offset:28672\n\t" \
  "ds_read_b64_tr_b16 v[182:183], v1 offset:30720\n\t" \
  "s_add_u32 s94, s20, 65536\n\t" \
  "s_addc_u32 s95, s21, 0\n\t" \
  "s_add_u32 m0, s83, 81920\n\t" \
  "s_nop 0\n\t" \
  "global_load_lds_dwordx4 v64, s[94:95]\n\t" \
  "s_add_u32 s34, s34, 11534336\n\t" \
  "v_add_u32 v20, s34, v0\n\t" \
  "s_waitcnt lgkmcnt(8)\n\t" \
  "v_mfma_f32_32x32x16_bf16 v[88:103], v[160:163], a[20:23], v[88:103]\n\t" \
  "ds_read_b64_tr_b16 v[184:185], v2 offset:28672\n\t" \
  "ds_read_b64_tr_b16 v[186:187], v2 offset:30720\n\t" \
  "s_lshl2_add_u32 s33, s87, s86\n\t" \
  "s_lshl_b32 s33, s33, 15\n\t" \
  "v_mfma_f32_32x32x16_bf16 v[104:119], v[164:167], a[20:23], v[104:119]\n\t" \
  "ds_read_b64_tr_b16 v[188:189], v3 offset:28672\n\t" \
  "ds_read_b64_tr_b16 v[190:191], v3 offset:30720\n\t" \
  "global_load_lds_dwordx4 v65, s[94:95] offset:1024\n\t" \
  "s_lshl_b32 s34, s98, 8\n\t" \
  "s_add_u32 s33, s33, s34\n\t" \
  "s_waitcnt lgkmcnt(8)\n\t" \
  "s_waitcnt vmcnt(20)\n\t" \
  "v_mfma_f32_32x32x16_bf16 v[72:87], v[168:171], a[24:27], v[72:87]\n\t" \
  "ds_read_b64_tr_b16 v[144:145], v4 offset:0\n\t" \
  "ds_read_b64_tr_b16 v[146:147], v4 offset:2048\n\t" \
  "s_lshl_b32 s34, s97, 6\n\t" \
  "s_add_u32 s33, s33, s34\n\t" \
  "v_mfma_f32_32x32x16_bf16 v[88:103], v[172:175], a[24:27], v[88:103]\n\t" \
  "ds_read_b64_tr_b16 v[148:149], v4 offset:4096\n\t" \
  "ds_read_b64_tr_b16 v[150:151], v4 offset:6144\n\t" \
  "s_add_u32 s92, s62, 0\n\t" \
  "s_addc_u32 s93, s63, 0\n\t" \
  "s_add_u32 m0, s83, 40960\n\t" \
  "s_nop 0\n\t" \
  "global_load_lds_dwordx4 v64, s[92:93]\n\t" \
  "s_add_u32 s33, s33, 4096\n\t" \
  "v_and_b32 v58, 0xf0, v0\n\t" \
  "s_waitcnt lgkmcnt(8)\n\t" \
  "v_mfma_f32_32x32x16_bf16 v[104:119], v[176:179], a[24:27], v[104:119]\n\t" \
  "ds_read_b64_tr_b16 v[152:153], v4 offset:8192\n\t" \
  "ds_read_b64_tr_b16 v[154:155], v4 offset:10240\n\t" \
  "v_lshl_add_u32 v14, v58, 6, s33\n\t" \
  "v_lshl_add_u32 v14, v57, 4, v14\n\t" \
  "s_waitcnt vmcnt(20)\n\t" \
  "v_mfma_f32_32x32x16_bf16 v[72:87], v[180:183], a[28:31], v[72:87]\n\t" \
  "ds_read_b64_tr_b16 v[156:157], v4 offset:12288\n\t" \
  "ds_read_b64_tr_b16 v[158:159], v4 offset:14336\n\t" \
  "global_load_lds_dwordx4 v65, s[92:93] offset:1024\n\t" \
  "v_lshl_add_u32 v14, v59, 5, v14\n\t" \
  "s_sub_u32 s80, s80, 4096\n\t" \
  "s_subb_u32 s81, s81, 0\n\t" \
  "s_waitcnt lgkmcnt(8)\n\t" \
  "v_mfma_f32_32x32x16_bf16 v[88:103], v[184:187], a[28:31], v[88:103]\n\t" \
  "ds_read_b64_tr_b16 v[160:161], v4 offset:16384\n\t" \
  "ds_read_b64_tr_b16 v[162:163], v4 offset:18432\n\t" \
  "v_add_u32 v61, 16384, v14\n\t" \
  "s_lshl_b32 s33, s86, 12\n\t" \
  "v_mfma_f32_32x32x16_bf16 v[104:119], v[188:191], a[28:31], v[104:119]\n\t" \
  "ds_read_b64_tr_b16 v[164:165], v4 offset:20480\n\t" \
  "ds_read_b64_tr_b16 v[166:167], v4 offset:22528\n\t" \
  "s_add_u32 s94, s62, 32768\n\t" \
  "s_addc_u32 s95, s63, 0\n\t" \
  "s_add_u32 m0, s83, 49152\n\t" \
  "s_nop 0\n\t" \
  "global_load_lds_dwordx4 v64, s[94:95]\n\t" \
  "s_add_u32 s33, s33, 196608\n\t" \
  "v_bfe_u32 v58, v0, 4, 3\n\t" \
  "s_waitcnt lgkmcnt(8)\n\t" \
  "v_mfma_f32_32x32x16_bf16 v[120:135], v[144:147], a[0:3], 0\n\t" \
  "ds_read_b64_tr_b16 v[168:169], v4 offset:24576\n\t" \
  "ds_read_b64_tr_b16 v[170:171], v4 offset:26624\n\t" \
  "v_lshl_add_u32 v9, v58, 3, s33\n\t" \
  "v_and_b32 v58, 0x380, v0\n\t" \
  "v_mfma_f32_32x32x16_bf16 v[120:135], v[148:151], a[4:7], v[120:135]\n\t" \
  "ds_read_b64_tr_b16 v[172:173], v4 offset:28672\n\t" \
  "ds_read_b64_tr_b16 v[174:175], v4 offset:30720\n\t" \
  "global_load_lds_dwordx4 v65, s[94:95] offset:1024\n\t" \
  "v_add_u32 v9, v9, v58\n\t" \
  "v_bfe_u32 v57, v0, 4, 2\n\t" \
  "s_waitcnt lgkmcnt(8)\n\t" \
  "v_mfma_f32_32x32x16_bf16 v[120:135], v[152:155], a[8:11], v[120:135]\n\t" \
  "ds_read_b64_tr_b16 v[176:177], v1 offset:32768\n\t" \
  "ds_read_b64_tr_b16 v[178:179], v1 offset:34816\n\t" \
  "v_min_u32 v57, 2, v57\n\t" \
  "v_lshl_add_u32 v57, v57, 3, s27\n\t" \
  "v_mfma_f32_32x32x16_bf16 v[120:135], v[156:159], a[12:15], v[120:135]\n\t" \
  "ds_read_b64_tr_b16 v[180:181], v2 offset:32768\n\t" \
  "ds_read_b64_tr_b16 v[182:183], v2 offset:34816\n\t" \
  "v_add_u32 v58, 2097152, v20\n\t" \
  "v_add_u32 v59, 4194304, v20\n\t" \
  "s_waitcnt lgkmcnt(8)\n\t" \
  "v_mfma_f32_32x32x16_bf16 v[120:135], v[160:163], a[16:19], v[120:135]\n\t" \
  "ds_read_b64_tr_b16 v[184:185], v3 offset:32768\n\t" \
  "ds_read_b64_tr_b16 v[186:187], v3 offset:34816\n\t" \
  "s_mov_b32 s30, 0x3f80\n\t" \
  "s_mov_b32 s31, 0x3f800000\n\t" \
  "v_mfma_f32_32x32x16_bf16 v[120:135], v[164:167], a[20:23], v[120:135]\n\t" \
  "ds_read_b64_tr_b16 v[188:189], v1 offset:36864\n\t" \
  "ds_read_b64_tr_b16 v[190:191], v1 offset:38912\n\t" \
  "s_waitcnt lgkmcnt(8)\n\t" \
  "v_mfma_f32_32x32x16_bf16 v[120:135], v[168:171], a[24:27], v[120:135]\n\t" \
  "ds_read_b64_tr_b16 v[144:145], v2 offset:36864\n\t" \
  "ds_read_b64_tr_b16 v[146:147], v2 offset:38912\n\t" \
  "v_mfma_f32_32x32x16_bf16 v[120:135], v[172:175], a[28:31], v[120:135]\n\t" \
  "ds_read_b64_tr_b16 v[148:149], v3 offset:36864\n\t" \
  "ds_read_b64_tr_b16 v[150:151], v3 offset:38912\n\t" \
  "s_waitcnt vmcnt(20)\n\t" \
  "v_or_b32 v138, 4, v138\n\t" \
  "s_mov_b64 exec, 1\n\t" \
  "global_store_dwordx2 v12, v[138:139], s[44:45] sc1\n\t" \
  "s_mov_b64 exec, -1\n\t" \
  "s_nop 0\n\t" \
  "v_and_b32 v138, -8, v138\n\t" \
  "v_or_b32 v138, 4, v138\n\t" \
  "s_barrier\n\t" \
  "s_waitcnt lgkmcnt(8)\n\t" \
  "s_waitcnt vmcnt(20)\n\t" \
  "v_mfma_f32_32x32x16_bf16 v[72:87], v[176:179], a[32:35], v[72:87]\n\t" \
  "ds_read_b64_tr_b16 v[152:153], v1 offset:40960\n\t" \
  "ds_read_b64_tr_b16 v[154:155], v1 offset:43008\n\t" \
  "s_lshl_b32 s33, s19, 12\n\t" \
  "s_add_u32 s33, s33, 16384\n\t" \
  "s_add_u32 s76, s74, s33\n\t" \
  "s_addc_u32 s77, s75, 0\n\t" \
  "global_load_dwordx4 a[64:67], v0, s[76:77] offset:0 sc1\n\t" \
  "global_load_dwordx4 a[68:71], v0, s[76:77] offset:1024 sc1\n\t" \
  "v_mfma_f32_32x32x16_bf16 v[88:103], v[180:183], a[32:35], v[88:103]\n\t" \
  "ds_read_b64_tr_b16 v[156:157], v2 offset:40960\n\t" \
  "ds_read_b64_tr_b16 v[158:159], v2 offset:43008\n\t" \
  "global_load_dwordx4 a[72:75], v0, s[76:77] offset:2048 sc1\n\t" \
  "global_load_dwordx4 a[76:79], v0, s[76:77] offset:3072 sc1\n\t" \
  "s_waitcnt lgkmcnt(8)\n\t" \
  "v_mfma_f32_32x32x16_bf16 v[104:119], v[184:187], a[32:35], v[104:119]\n\t" \
  "ds_read_b64_tr_b16 v[160:161], v3 offset:40960\n\t" \
  "ds_read_b64_tr_b16 v[162:163], v3 offset:43008\n\t" \
  "s_lshl_b32 s33, s19, 11\n\t" \
  "s_add_u32 s33, s33, 24576\n\t" \
  "s_add_u32 s76, s74, s33\n\t" \
  "s_addc_u32 s77, s75, 0\n\t" \
  "global_load_dwordx4 a[80:83], v0, s[76:77] offset:0 sc1\n\t" \
  "global_load_dwordx4 a[84:87], v0, s[76:77] offset:1024 sc1\n\t" \
  "s_waitcnt vmcnt(25)\n\t" \
  "v_mfma_f32_32x32x16_bf16 v[72:87], v[188:191], a[36:39], v[72:87]\n\t" \
  "ds_read_b64_tr_b16 v[164:165], v1 offset:45056\n\t" \
  "ds_read_b64_tr_b16 v[166:167], v1 offset:47104\n\t" \
  "s_waitcnt lgkmcnt(8)\n\t" \
  "v_mfma_f32_32x32x16_bf16 v[88:103], v[144:147], a[36:39], v[88:103]\n\t" \
  "ds_read_b64_tr_b16 v[168:169], v2 offset:45056\n\t" \
  "ds_read_b64_tr_b16 v[170:171], v2 offset:47104\n\t" \
  "v_mfma_f32_32x32x16_bf16 v[104:119], v[148:151], a[36:39], v[104:119]\n\t" \
  "ds_read_b64_tr_b16 v[172:173], v3 offset:45056\n\t" \
  "ds_read_b64_tr_b16 v[174:175], v3 offset:47104\n\t" \
  "s_waitcnt lgkmcnt(8)\n\t" \
  "s_waitcnt vmcnt(24)\n\t" \
  "v_mfma_f32_32x32x16_bf16 v[72:87], v[152:155], a[40:43], v[72:87]\n\t" \
  "ds_read_b64_tr_b16 v[176:177], v1 offset:49152\n\t" \
  "ds_read_b64_tr_b16 v[178:179], v1 offset:51200\n\t" \
  "v_mfma_f32_32x32x16_bf16 v[88:103], v[156:159], a[40:43], v[88:103]\n\t" \
  "ds_read_b64_tr_b16 v[180:181], v2 offset:49152\n\t" \
  "ds_read_b64_tr_b16 v[182:183], v2 offset:51200\n\t" \
  "s_waitcnt lgkmcnt(8)\n\t" \
  "v_mfma_f32_32x32x16_bf16 v[104:119], v[160:163], a[40:43], v[104:119]\n\t" \
  "ds_read_b64_tr_b16 v[184:185], v3 offset:49152\n\t" \
  "ds_read_b64_tr_b16 v[186:187], v3 offset:51200\n\t" \
  "s_waitcnt vmcnt(23)\n\t" \
  "v_mfma_f32_32x32x16_bf16 v[72:87], v[164:167], a[44:47], v[72:87]\n\t" \
  "ds_read_b64_tr_b16 v[188:189], v1 offset:53248\n\t" \
  "ds_read_b64_tr_b16 v[190:191], v1 offset:55296\n\t" \
  "s_waitcnt lgkmcnt(8)\n\t" \
  "v_mfma_f32_32x32x16_bf16 v[88:103], v[168:171], a[44:47], v[88:103]\n\t" \
  "ds_read_b64_tr_b16 v[144:145], v2 offset:53248\n\t" \
  "ds_read_b64_tr_b16 v[146:147], v2 offset:55296\n\t" \
  "v_mfma_f32_32x32x16_bf16 v[104:119], v[172:175], a[44:47], v[104:119]\n\t" \
  "ds_read_b64_tr_b16 v[148:149], v3 offset:53248\n\t" \
  "ds_read_b64_tr_b16 v[150:151], v3 offset:55296\n\t" \
  "s_waitcnt lgkmcnt(8)\n\t" \
  "s_waitcnt vmcnt(22)\n\t" \
  "v_mfma_f32_32x32x16_bf16 v[72:87], v[176:179], a[48:51], v[72:87]\n\t" \
  "ds_read_b64_tr_b16 v[152:153], v1 offset:57344\n\t" \
  "ds_read_b64_tr_b16 v[154:155], v1 offset:59392\n\t" \
  "global_load_dwordx2 v[142:143], v13, s[44:45] sc1\n\t" \
  "v_mfma_f32_32x32x16_bf16 v[88:103], v[180:183], a[48:51], v[88:103]\n\t" \
  "ds_read_b64_tr_b16 v[156:157], v2 offset:57344\n\t" \
  "ds_read_b64_tr_b16 v[158:159], v2 offset:59392\n\t" \
  "s_waitcnt lgkmcnt(8)\n\t" \
  "v_mfma_f32_32x32x16_bf16 v[104:119], v[184:187], a[48:51], v[104:119]\n\t" \
  "ds_read_b64_tr_b16 v[160:161], v3 offset:57344\n\t" \
  "ds_read_b64_tr_b16 v[162:163], v3 offset:59392\n\t" \
  "s_waitcnt vmcnt(22)\n\t" \
  "v_mfma_f32_32x32x16_bf16 v[72:87], v[188:191], a[52:55], v[72:87]\n\t" \
  "ds_read_b64_tr_b16 v[164:165], v1 offset:61440\n\t" \
  "ds_read_b64_tr_b16 v[166:167], v1 offset:63488\n\t" \
  "s_waitcnt lgkmcnt(8)\n\t" \
  "v_mfma_f32_32x32x16_bf16 v[88:103], v[144:147], a[52:55], v[88:103]\n\t" \
  "ds_read_b64_tr_b16 v[168:169], v2 offset:61440\n\t" \
  "ds_read_b64_tr_b16 v[170:171], v2 offset:63488\n\t" \
  "v_mfma_f32_32x32x16_bf16 v[104:119], v[148:151], a[52:55], v[104:119]\n\t" \
  "ds_read_b64_tr_b16 v[172:173], v3 offset:61440\n\t" \
  "ds_read_b64_tr_b16 v[174:175], v3 offset:63488\n\t" \
  "s_waitcnt vmcnt(14)\n\t" \
  "s_barrier\n\t" \
  "s_waitcnt lgkmcnt(8)\n\t" \
  "v_mfma_f32_32x32x16_bf16 v[72:87], v[152:155], a[56:59], v[72:87]\n\t" \
  "ds_read_b64_tr_b16 v[176:177], v5 offset:32768\n\t" \
  "ds_read_b64_tr_b16 v[178:179], v5 offset:34816\n\t" \
  "v_mfma_f32_32x32x16_bf16 v[88:103], v[156:159], a[56:59], v[88:103]\n\t" \
  "ds_read_b64_tr_b16 v[180:181], v6 offset:32768\n\t" \
  "ds_read_b64_tr_b16 v[182:183], v6 offset:34816\n\t" \
  "s_waitcnt lgkmcnt(8)\n\t" \
  "v_mfma_f32_32x32x16_bf16 v[104:119], v[160:163], a[56:59], v[104:119]\n\t" \
  "ds_read_b64_tr_b16 v[184:185], v7 offset:32768\n\t" \
  "ds_read_b64_tr_b16 v[186:187], v7 offset:34816\n\t" \
  "v_mfma_f32_32x32x16_bf16 v[72:87], v[164:167], a[60:63], v[72:87]\n\t" \
  "ds_read_b64_tr_b16 v[188:189], v5 offset:36864\n\t" \
  "ds_read_b64_tr_b16 v[190:191], v5 offset:38912\n\t" \
  "s_waitcnt lgkmcnt(8)\n\t" \
  "v_mfma_f32_32x32x16_bf16 v[88:103], v[168:171], a[60:63], v[88:103]\n\t" \
  "ds_read_b64_tr_b16 v[144:145], v6 offset:36864\n\t" \
  "ds_read_b64_tr_b16 v[146:147], v6 offset:38912\n\t" \
  "v_mfma_f32_32x32x16_bf16 v[104:119], v[172:175], a[60:63], v[104:119]\n\t" \
  "ds_read_b64_tr_b16 v[148:149], v7 offset:36864\n\t" \
  "ds_read_b64_tr_b16 v[150:151], v7 offset:38912\n\t" \
  "s_waitcnt lgkmcnt(8)\n\t" \
  "s_waitcnt vmcnt(6)\n\t" \
  "v_mfma_f32_32x32x16_bf16 v[72:87], v[176:179], a[64:67], v[72:87]\n\t" \
  "ds_read_b64_tr_b16 v[152:153], v5 offset:40960\n\t" \
  "ds_read_b64_tr_b16 v[154:155], v5 offset:43008\n\t" \
  "global_load_dwordx2 v[136:137], v13, s[44:45] sc1\n\t" \
  "s_waitcnt vmcnt(1)\n\t" \
  "s_or_b32 s12, s12, 4\n\t" \
  "v_cmp_ge_u64 s[58:59], s[68:69], v[142:143]\n\t" \
  "v_cmp_le_u64 vcc, s[12:13], v[142:143]\n\t" \
  "s_nop 1\n\t" \
  "s_and_b32 s58, s58, vcc_lo\n\t" \
  "s_and_b32 s12, s12, -8\n\t" \
  "s_and_b32 s58, s58, 240\n\t" \
  "s_cmp_eq_u32 s58, 240\n\t" \
  "s_cbranch_scc0 P8FS%=\n\t" \
  "P8FB%=:\n\t" \
  "v_mfma_f32_32x32x16_bf16 v[88:103], v[180:183], a[64:67], v[88:103]\n\t" \
  "ds_read_b64_tr_b16 v[156:157], v6 offset:40960\n\t" \
  "ds_read_b64_tr_b16 v[158:159], v6 offset:43008\n\t" \
  "s_xor_b32 s33, s19, 1\n\t" \
  "s_lshl_b32 s33, s33, 12\n\t" \
  "s_add_u32 s33, s33, 16384\n\t" \
  "s_add_u32 s76, s74, s33\n\t" \
  "s_addc_u32 s77, s75, 0\n\t" \
  "global_load_dwordx4 a[88:91], v0, s[76:77] offset:0 sc1\n\t" \
  "global_load_dwordx4 a[92:95], v0, s[76:77] offset:1024 sc1\n\t" \
  "s_waitcnt lgkmcnt(8)\n\t" \
  "v_mfma_f32_32x32x16_bf16 v[104:119], v[184:187], a[64:67], v[104:119]\n\t" \
  "ds_read_b64_tr_b16 v[160:161], v7 offset:40960\n\t" \
  "ds_read_b64_tr_b16 v[162:163], v7 offset:43008\n\t" \
  "global_load_dwordx4 a[96:99], v0, s[76:77] offset:2048 sc1\n\t" \
  "global_load_dwordx4 a[100:103], v0, s[76:77] offset:3072 sc1\n\t" \
  "v_mfma_f32_32x32x16_bf16 v[72:87], v[188:191], a[68:71], v[72:87]\n\t" \
  "ds_read_b64_tr_b16 v[164:165], v5 offset:45056\n\t" \
  "ds_read_b64_tr_b16 v[166:167], v5 offset:47104\n\t" \
  "s_xor_b32 s33, s19, 1\n\t" \
  "s_lshl_b32 s33, s33, 11\n\t" \
  "s_add_u32 s33, s33, 24576\n\t" \
  "s_add_u32 s76, s74, s33\n\t" \
  "s_addc_u32 s77, s75, 0\n\t" \
  "global_load_dwordx4 a[104:107], v0, s[76:77] offset:0 sc1\n\t" \
  "global_load_dwordx4 a[108:111], v0, s[76:77] offset:1024 sc1\n\t" \
  "s_waitcnt lgkmcnt(8)\n\t" \
  "v_mfma_f32_32x32x16_bf16 v[88:103], v[144:147], a[68:71], v[88:103]\n\t" \
  "ds_read_b64_tr_b16 v[168:169], v6 offset:45056\n\t" \
  "ds_read_b64_tr_b16 v[170:171], v6 offset:47104\n\t" \
  "v_mfma_f32_32x32x16_bf16 v[104:119], v[148:151], a[68:71], v[104:119]\n\t" \
  "ds_read_b64_tr_b16 v[172:173], v7 offset:45056\n\t" \
  "ds_read_b64_tr_b16 v[174:175], v7 offset:47104\n\t" \
  "s_waitcnt lgkmcnt(8)\n\t" \
  "v_mfma_f32_32x32x16_bf16 v[72:87], v[152:155], a[72:75], v[72:87]\n\t" \
  "ds_read_b64_tr_b16 v[176:177], v5 offset:49152\n\t" \
  "ds_read_b64_tr_b16 v[178:179], v5 offset:51200\n\t" \
  "v_mfma_f32_32x32x16_bf16 v[88:103], v[156:159], a[72:75], v[88:103]\n\t" \
  "ds_read_b64_tr_b16 v[180:181], v6 offset:49152\n\t" \
  "ds_read_b64_tr_b16 v[182:183], v6 offset:51200\n\t" \
  "s_waitcnt lgkmcnt(8)\n\t" \
  "v_mfma_f32_32x32x16_bf16 v[104:119], v[160:163], a[72:75], v[104:119]\n\t" \
  "ds_read_b64_tr_b16 v[184:185], v7 offset:49152\n\t" \
  "ds_read_b64_tr_b16 v[186:187], v7 offset:51200\n\t" \
  "v_mfma_f32_32x32x16_bf16 v[72:87], v[164:167], a[76:79], v[72:87]\n\t" \
  "ds_read_b64_tr_b16 v[188:189], v5 offset:53248\n\t" \
  "ds_read_b64_tr_b16 v[190:191], v5 offset:55296\n\t" \
  "s_waitcnt lgkmcnt(8)\n\t" \
  "v_mfma_f32_32x32x16_bf16 v[88:103], v[168:171], a[76:79], v[88:103]\n\t" \
  "ds_read_b64_tr_b16 v[144:145], v6 offset:53248\n\t" \
  "ds_read_b64_tr_b16 v[146:147], v6 offset:55296\n\t" \
  "v_mfma_f32_32x32x16_bf16 v[104:119], v[172:175], a[76:79], v[104:119]\n\t" \
  "ds_read_b64_tr_b16 v[148:149], v7 offset:53248\n\t" \
  "ds_read_b64_tr_b16 v[150:151], v7 offset:55296\n\t" \
  "s_barrier\n\t" \
  "s_waitcnt lgkmcnt(8)\n\t" \
  "v_mfma_f32_32x32x16_bf16 v[72:87], v[176:179], a[80:83], v[72:87]\n\t" \
  "ds_read_b64_tr_b16 v[152:153], v5 offset:57344\n\t" \
  "ds_read_b64_tr_b16 v[154:155], v5 offset:59392\n\t" \
  "s_waitcnt vmcnt(6)\n\t" \
  "s_or_b32 s12, s12, 4\n\t" \
  "v_cmp_ge_u64 s[58:59], s[68:69], v[136:137]\n\t" \
  "v_cmp_le_u64 vcc, s[12:13], v[136:137]\n\t" \
  "s_nop 1\n\t" \
  "s_and_b32 s58, s58, vcc_lo\n\t" \
  "s_and_b32 s12, s12, -8\n\t" \
  "s_and_b32 s58, s58, 15\n\t" \
  "s_cmp_eq_u32 s58, 15\n\t" \
  "s_cbranch_scc0 P14FS%=\n\t" \
  "P14FB%=:\n\t" \
  "v_mfma_f32_32x32x16_bf16 v[88:103], v[180:183], a[80:83], v[88:103]\n\t" \
  "ds_read_b64_tr_b16 v[156:157], v6 offset:57344\n\t" \
  "ds_read_b64_tr_b16 v[158:159], v6 offset:59392\n\t" \
  "s_add_u32 s76, s74, 28672\n\t" \
  "s_addc_u32 s77, s75, 0\n\t" \
  "global_load_dwordx4 a[112:115], v0, s[76:77] offset:0 sc1\n\t" \
  "global_load_dwordx4 a[116:119], v0, s[76:77] offset:1024 sc1\n\t" \
  "s_waitcnt lgkmcnt(8)\n\t" \
  "v_mfma_f32_32x32x16_bf16 v[104:119], v[184:187], a[80:83], v[104:119]\n\t" \
  "ds_read_b64_tr_b16 v[160:161], v7 offset:57344\n\t" \
  "ds_read_b64_tr_b16 v[162:163], v7 offset:59392\n\t" \
  "global_load_dwordx4 a[120:123], v0, s[76:77] offset:2048 sc1\n\t" \
  "global_load_dwordx4 a[124:127], v0, s[76:77] offset:3072 sc1\n\t" \
  "global_load_dwordx4 v[24:27], v14, s[40:41]\n\t" \
  "global_load_dwordx4 v[28:31], v61, s[40:41]\n\t" \
  "v_mfma_f32_32x32x16_bf16 v[72:87], v[188:191], a[84:87], v[72:87]\n\t" \
  "ds_read_b64_tr_b16 v[164:165], v5 offset:61440\n\t" \
  "ds_read_b64_tr_b16 v[166:167], v5 offset:63488\n\t" \
  "s_add_u32 s76, s74, 32768\n\t" \
  "s_addc_u32 s77, s75, 0\n\t" \
  "global_load_dwordx4 a[128:131], v0, s[76:77] offset:0 sc1\n\t" \
  "global_load_dwordx4 a[132:135], v0, s[76:77] offset:1024 sc1\n\t" \
  "s_waitcnt lgkmcnt(8)\n\t" \
  "v_mfma_f32_32x32x16_bf16 v[88:103], v[144:147], a[84:87], v[88:103]\n\t" \
  "ds_read_b64_tr_b16 v[168:169], v6 offset:61440\n\t" \
  "ds_read_b64_tr_b16 v[170:171], v6 offset:63488\n\t" \
  "global_load_dwordx4 a[136:139], v0, s[76:77] offset:2048 sc1\n\t" \
  "global_load_dwordx4 a[140:143], v0, s[76:77] offset:3072 sc1\n\t" \
  "v_mfma_f32_32x32x16_bf16 v[104:119], v[148:151], a[84:87], v[104:119]\n\t" \
  "ds_read_b64_tr_b16 v[172:173], v7 offset:61440\n\t" \
  "ds_read_b64_tr_b16 v[174:175], v7 offset:63488\n\t" \
  "s_waitcnt lgkmcnt(8)\n\t" \
  "s_waitcnt vmcnt(15)\n\t" \
  "v_mfma_f32_32x32x16_bf16 v[72:87], v[152:155], a[88:91], v[72:87]\n\t" \
  "ds_read_b64_tr_b16 v[176:177], v5 offset:0\n\t" \
  "ds_read_b64_tr_b16 v[178:179], v5 offset:2048\n\t" \
  "v_mfma_f32_32x32x16_bf16 v[88:103], v[156:159], a[88:91], v[88:103]\n\t" \
  "ds_read_b64_tr_b16 v[180:181], v6 offset:0\n\t" \
  "ds_read_b64_tr_b16 v[182:183], v6 offset:2048\n\t" \
  "s_waitcnt lgkmcnt(8)\n\t" \
  "v_mfma_f32_32x32x16_bf16 v[104:119], v[160:163], a[88:91], v[104:119]\n\t" \
  "ds_read_b64_tr_b16 v[184:185], v7 offset:0\n\t" \
  "ds_read_b64_tr_b16 v[186:187], v7 offset:2048\n\t" \
  "s_waitcnt vmcnt(14)\n\t" \
  "v_mfma_f32_32x32x16_bf16 v[72:87], v[164:167], a[92:95], v[72:87]\n\t" \
  "ds_read_b64_tr_b16 v[188:189], v5 offset:4096\n\t" \
  "ds_read_b64_tr_b16 v[190:191], v5 offset:6144\n\t" \
  "s_waitcnt lgkmcnt(8)\n\t" \
  "v_mfma_f32_32x32x16_bf16 v[88:103], v[168:171], a[92:95], v[88:103]\n\t" \
  "ds_read_b64_tr_b16 v[144:145], v6 offset:4096\n\t" \
  "ds_read_b64_tr_b16 v[146:147], v6 offset:6144\n\t" \
  "v_mfma_f32_32x32x16_bf16 v[104:119], v[172:175], a[92:95], v[104:119]\n\t" \
  "ds_read_b64_tr_b16 v[148:149], v7 offset:4096\n\t" \
  "ds_read_b64_tr_b16 v[150:151], v7 offset:6144\n\t" \
  "s_waitcnt lgkmcnt(8)\n\t" \
  "s_waitcnt vmcnt(13)\n\t" \
  "v_mfma_f32_32x32x16_bf16 v[72:87], v[176:179], a[96:99], v[72:87]\n\t" \
  "ds_read_b64_tr_b16 v[152:153], v5 offset:8192\n\t" \
  "ds_read_b64_tr_b16 v[154:155], v5 offset:10240\n\t" \
  "v_mfma_f32_32x32x16_bf16 v[88:103], v[180:183], a[96:99], v[88:103]\n\t" \
  "ds_read_b64_tr_b16 v[156:157], v6 offset:8192\n\t" \
  "ds_read_b64_tr_b16 v[158:159], v6 offset:10240\n\t" \
  "s_waitcnt lgkmcnt(8)\n\t" \
  "v_mfma_f32_32x32x16_bf16 v[104:119], v[184:187], a[96:99], v[104:119]\n\t" \
  "ds_read_b64_tr_b16 v[160:161], v7 offset:8192\n\t" \
  "ds_read_b64_tr_b16 v[162:163], v7 offset:10240\n\t" \
  "s_waitcnt vmcnt(12)\n\t" \
  "v_mfma_f32_32x32x16_bf16 v[72:87], v[188:191], a[100:103], v[72:87]\n\t" \
  "ds_read_b64_tr_b16 v[164:165], v5 offset:12288\n\t" \
  "ds_read_b64_tr_b16 v[166:167], v5 offset:14336\n\t" \
  "s_waitcnt lgkmcnt(8)\n\t" \
  "v_mfma_f32_32x32x16_bf16 v[88:103], v[144:147], a[100:103], v[88:103]\n\t" \
  "ds_read_b64_tr_b16 v[168:169], v6 offset:12288\n\t" \
  "ds_read_b64_tr_b16 v[170:171], v6 offset:14336\n\t" \
  "v_mfma_f32_32x32x16_bf16 v[104:119], v[148:151], a[100:103], v[104:119]\n\t" \
  "ds_read_b64_tr_b16 v[172:173], v7 offset:12288\n\t" \
  "ds_read_b64_tr_b16 v[174:175], v7 offset:14336\n\t" \
  "s_waitcnt lgkmcnt(8)\n\t" \
  "s_waitcnt vmcnt(11)\n\t" \
  "v_mfma_f32_32x32x16_bf16 v[72:87], v[152:155], a[104:107], v[72:87]\n\t" \
  "ds_read_b64_tr_b16 v[176:177], v5 offset:16384\n\t" \
  "ds_read_b64_tr_b16 v[178:179], v5 offset:18432\n\t" \
  "v_mfma_f32_32x32x16_bf16 v[88:103], v[156:159], a[104:107], v[88:103]\n\t" \
  "ds_read_b64_tr_b16 v[180:181], v5 offset:20480\n\t" \
  "ds_read_b64_tr_b16 v[182:183], v5 offset:22528\n\t" \
  "s_waitcnt lgkmcnt(8)\n\t" \
  "v_mfma_f32_32x32x16_bf16 v[104:119], v[160:163], a[104:107], v[104:119]\n\t" \
  "ds_read_b64_tr_b16 v[184:185], v5 offset:24576\n\t" \
  "ds_read_b64_tr_b16 v[186:187], v5 offset:26624\n\t" \
  "s_waitcnt vmcnt(10)\n\t" \
  "v_mfma_f32_32x32x16_bf16 v[72:87], v[164:167], a[108:111], v[72:87]\n\t" \
  "ds_read_b64_tr_b16 v[188:189], v5 offset:28672\n\t" \
  "ds_read_b64_tr_b16 v[190:191], v5 offset:30720\n\t" \
  "s_waitcnt lgkmcnt(8)\n\t" \
  "v_mfma_f32_32x32x16_bf16 v[88:103], v[168:171], a[108:111], v[88:103]\n\t" \
  "ds_read_b64_tr_b16 v[144:145], v10 offset:0\n\t" \
  "ds_read_b64_tr_b16 v[146:147], v10 offset:2048\n\t" \
  "v_mfma_f32_32x32x16_bf16 v[104:119], v[172:175], a[108:111], v[104:119]\n\t" \
  "ds_read_b64_tr_b16 v[148:149], v10 offset:4096\n\t" \
  "ds_read_b64_tr_b16 v[150:151], v10 offset:6144\n\t" \
  "s_waitcnt lgkmcnt(8)\n\t" \
  "s_waitcnt vmcnt(9)\n\t" \
  "v_mfma_f32_32x32x16_bf16 v[72:87], v[176:179], a[112:115], v[72:87]\n\t" \
  "ds_read_b64_tr_b16 v[152:153], v10 offset:8192\n\t" \
  "ds_read_b64_tr_b16 v[154:155], v10 offset:10240\n\t" \
  "s_waitcnt vmcnt(8)\n\t" \
  "v_mfma_f32_32x32x16_bf16 v[72:87], v[180:183], a[116:119], v[72:87]\n\t" \
  "ds_read_b64_tr_b16 v[156:157], v10 offset:12288\n\t" \
  "ds_read_b64_tr_b16 v[158:159], v10 offset:14336\n\t" \
  "s_waitcnt lgkmcnt(8)\n\t" \
  "s_waitcnt vmcnt(7)\n\t" \
  "v_mfma_f32_32x32x16_bf16 v[72:87], v[184:187], a[120:123], v[72:87]\n\t" \
  "ds_read_b64_tr_b16 v[160:161], v6 offset:16384\n\t" \
  "ds_read_b64_tr_b16 v[162:163], v6 offset:18432\n\t" \
  "s_waitcnt vmcnt(4)\n\t" \
  "v_permlane16_swap_b32 v24, v28\n\t" \
  "v_permlane16_swap_b32 v25, v29\n\t" \
  "v_mfma_f32_32x32x16_bf16 v[72:87], v[188:191], a[124:127], v[72:87]\n\t" \
  "ds_read_b64_tr_b16 v[164:165], v6 offset:20480\n\t" \
  "ds_read_b64_tr_b16 v[166:167], v6 offset:22528\n\t" \
  "v_permlane16_swap_b32 v26, v30\n\t" \
  "v_permlane16_swap_b32 v27, v31\n\t" \
  "s_waitcnt lgkmcnt(8)\n\t" \
  "s_waitcnt vmcnt(3)\n\t" \
  "v_mfma_f32_32x32x16_bf16 v[72:87], v[144:147], a[128:131], v[72:87]\n\t" \
  "ds_read_b64_tr_b16 v[168:169], v6 offset:24576\n\t" \
  "ds_read_b64_tr_b16 v[170:171], v6 offset:26624\n\t" \
  "v_permlane32_swap_b32 v24, v26\n\t" \
  "v_permlane32_swap_b32 v25, v27\n\t" \
  "s_waitcnt vmcnt(2)\n\t" \
  "v_mfma_f32_32x32x16_bf16 v[72:87], v[148:151], a[132:135], v[72:87]\n\t" \
  "ds_read_b64_tr_b16 v[172:173], v6 offset:28672\n\t" \
  "ds_read_b64_tr_b16 v[174:175], v6 offset:30720\n\t" \
  "v_permlane32_swap_b32 v28, v30\n\t" \
  "v_permlane32_swap_b32 v29, v31\n\t" \
  "s_waitcnt lgkmcnt(8)\n\t" \
  "s_waitcnt vmcnt(1)\n\t" \
  "v_mfma_f32_32x32x16_bf16 v[72:87], v[152:155], a[136:139], v[72:87]\n\t" \
  "ds_read_b64_tr_b16 v[176:177], v11 offset:0\n\t" \
  "ds_read_b64_tr_b16 v[178:179], v11 offset:2048\n\t" \
  "v_lshlrev_b32 v32, 16, v24\n\t" \
  "v_and_b32 v33, 0xffff0000, v24\n\t" \
  "s_waitcnt vmcnt(0)\n\t" \
  "v_mfma_f32_32x32x16_bf16 v[72:87], v[156:159], a[140:143], v[72:87]\n\t" \
  "ds_read_b64_tr_b16 v[180:181], v11 offset:4096\n\t" \
  "ds_read_b64_tr_b16 v[182:183], v11 offset:6144\n\t" \
  "v_lshlrev_b32 v34, 16, v25\n\t" \
  "v_and_b32 v35, 0xffff0000, v25\n\t" \
  "s_waitcnt lgkmcnt(8)\n\t" \
  "v_mfma_f32_32x32x16_bf16 v[88:103], v[160:163], a[112:115], v[88:103]\n\t" \
  "ds_read_b64_tr_b16 v[184:185], v11 offset:8192\n\t" \
  "ds_read_b64_tr_b16 v[186:187], v11 offset:10240\n\t" \
  "v_lshlrev_b32 v36, 16, v26\n\t" \
  "v_and_b32 v37, 0xffff0000, v26\n\t" \
  "s_cmp_eq_u32 s84, 0\n\t" \
  "s_cbranch_scc0 GSF%=\n\t" \
  "global_load_dwordx2 v[48:49], v9, s[44:45] offset:0 sc1\n\t" \
  "global_load_dwordx2 v[50:51], v9, s[44:45] offset:1024 sc1\n\t" \
  "global_load_dwordx2 v[52:53], v9, s[44:45] offset:2048 sc1\n\t" \
  "global_load_dwordx2 v[54:55], v9, s[44:45] offset:3072 sc1\n\t" \
  "GSF%=:\n\t" \
  "v_mfma_f32_32x32x16_bf16 v[88:103], v[164:167], a[116:119], v[88:103]\n\t" \
  "ds_read_b64_tr_b16 v[188:189], v11 offset:12288\n\t" \
  "ds_read_b64_tr_b16 v[190:191], v11 offset:14336\n\t" \
  "v_lshlrev_b32 v38, 16, v27\n\t" \
  "v_and_b32 v39, 0xffff0000, v27\n\t" \
  "s_waitcnt lgkmcnt(8)\n\t" \
  "v_mfma_f32_32x32x16_bf16 v[88:103], v[168:171], a[120:123], v[88:103]\n\t" \
  "ds_read_b64_tr_b16 v[144:145], v7 offset:16384\n\t" \
  "ds_read_b64_tr_b16 v[146:147], v7 offset:18432\n\t" \
  "v_lshlrev_b32 v40, 16, v28\n\t" \
  "v_and_b32 v41, 0xffff0000, v28\n\t" \
  "v_mfma_f32_32x32x16_bf16 v[88:103], v[172:175], a[124:127], v[88:103]\n\t" \
  "ds_read_b64_tr_b16 v[148:149], v7 offset:20480\n\t" \
  "ds_read_b64_tr_b16 v[150:151], v7 offset:22528\n\t" \
  "v_lshlrev_b32 v42, 16, v29\n\t" \
  "v_and_b32 v43, 0xffff0000, v29\n\t" \
  "v_cvt_pk_bf16_f32 v204, v72, v73\n\t" \
  "v_cvt_pk_bf16_f32 v205, v74, v75\n\t" \
  "s_waitcnt lgkmcnt(8)\n\t" \
  "v_mfma_f32_32x32x16_bf16 v[88:103], v[176:179], a[128:131], v[88:103]\n\t" \
  "ds_read_b64_tr_b16 v[152:153], v7 offset:24576\n\t" \
  "ds_read_b64_tr_b16 v[154:155], v7 offset:26624\n\t" \
  "v_lshlrev_b32 v44, 16, v30\n\t" \
  "v_and_b32 v45, 0xffff0000, v30\n\t" \
  "v_cvt_pk_bf16_f32 v206, v76, v77\n\t" \
  "v_cvt_pk_bf16_f32 v207, v78, v79\n\t" \
  "v_mfma_f32_32x32x16_bf16 v[88:103], v[180:183], a[132:135], v[88:103]\n\t" \
  "ds_read_b64_tr_b16 v[156:157], v7 offset:28672\n\t" \
  "ds_read_b64_tr_b16 v[158:159], v7 offset:30720\n\t" \
  "v_lshlrev_b32 v46, 16, v31\n\t" \
  "v_and_b32 v47, 0xffff0000, v31\n\t" \
  "global_store_dwordx4 v17, v[204:207], s[44:45] offset:0 sc1\n\t" \
  "s_waitcnt lgkmcnt(8)\n\t" \
  "v_mfma_f32_32x32x16_bf16 v[88:103], v[184:187], a[136:139], v[88:103]\n\t" \
  "ds_read_b64_tr_b16 v[160:161], v15 offset:0\n\t" \
  "ds_read_b64_tr_b16 v[162:163], v15 offset:2048\n\t" \
  "v_cvt_pk_bf16_f32 v208, v80, v81\n\t" \
  "v_cvt_pk_bf16_f32 v209, v82, v83\n\t" \
  "v_mfma_f32_32x32x16_bf16 v[88:103], v[188:191], a[140:143], v[88:103]\n\t" \
  "ds_read_b64_tr_b16 v[164:165], v15 offset:4096\n\t" \
  "ds_read_b64_tr_b16 v[166:167], v15 offset:6144\n\t" \
  "v_cvt_pk_bf16_f32 v210, v84, v85\n\t" \
  "v_cvt_pk_bf16_f32 v211, v86, v87\n\t" \
  "s_waitcnt lgkmcnt(8)\n\t" \
  "v_mfma_f32_32x32x16_bf16 v[104:119], v[144:147], a[112:115], v[104:119]\n\t" \
  "ds_read_b64_tr_b16 v[168:169], v15 offset:8192\n\t" \
  "ds_read_b64_tr_b16 v[170:171], v15 offset:10240\n\t" \
  "global_store_dwordx4 v17, v[208:211], s[44:45] offset:1024 sc1\n\t" \
  "v_mfma_f32_32x32x16_bf16 v[104:119], v[148:151], a[116:119], v[104:119]\n\t" \
  "ds_read_b64_tr_b16 v[172:173], v15 offset:12288\n\t" \
  "ds_read_b64_tr_b16 v[174:175], v15 offset:14336\n\t" \
  "s_waitcnt lgkmcnt(8)\n\t" \
  "v_mfma_f32_32x32x16_bf16 v[104:119], v[152:155], a[120:123], v[104:119]\n\t" \
  "ds_read_b64_tr_b16 v[176:177], v4 offset:32768\n\t" \
  "ds_read_b64_tr_b16 v[178:179], v4 offset:34816\n\t" \
  "v_mfma_f32_32x32x16_bf16 v[104:119], v[156:159], a[124:127], v[104:119]\n\t" \
  "ds_read_b64_tr_b16 v[180:181], v4 offset:36864\n\t" \
  "ds_read_b64_tr_b16 v[182:183], v4 offset:38912\n\t" \
  "v_cvt_pk_bf16_f32 v212, v88, v89\n\t" \
  "v_cvt_pk_bf16_f32 v213, v90, v91\n\t" \
  "s_waitcnt lgkmcnt(8)\n\t" \
  "v_mfma_f32_32x32x16_bf16 v[104:119], v[160:163], a[128:131], v[104:119]\n\t" \
  "ds_read_b64_tr_b16 v[184:185], v4 offset:40960\n\t" \
  "ds_read_b64_tr_b16 v[186:187], v4 offset:43008\n\t" \
  "v_cvt_pk_bf16_f32 v214, v92, v93\n\t" \
  "v_cvt_pk_bf16_f32 v215, v94, v95\n\t" \
  "v_mfma_f32_32x32x16_bf16 v[104:119], v[164:167], a[132:135], v[104:119]\n\t" \
  "ds_read_b64_tr_b16 v[188:189], v4 offset:45056\n\t" \
  "ds_read_b64_tr_b16 v[190:191], v4 offset:47104\n\t" \
  "global_store_dwordx4 v18, v[212:215], s[44:45] offset:0 sc1\n\t" \
  "s_waitcnt lgkmcnt(8)\n\t" \
  "v_mfma_f32_32x32x16_bf16 v[104:119], v[168:171], a[136:139], v[104:119]\n\t" \
  "ds_read_b64_tr_b16 v[144:145], v4 offset:49152\n\t" \
  "ds_read_b64_tr_b16 v[146:147], v4 offset:51200\n\t" \
  "v_cvt_pk_bf16_f32 v216, v96, v97\n\t" \
  "v_cvt_pk_bf16_f32 v217, v98, v99\n\t" \
  "v_mfma_f32_32x32x16_bf16 v[104:119], v[172:175], a[140:143], v[104:119]\n\t" \
  "ds_read_b64_tr_b16 v[148:149], v4 offset:53248\n\t" \
  "ds_read_b64_tr_b16 v[150:151], v4 offset:55296\n\t" \
  "v_cvt_pk_bf16_f32 v218, v100, v101\n\t" \
  "v_cvt_pk_bf16_f32 v219, v102, v103\n\t" \
  "s_waitcnt lgkmcnt(8)\n\t" \
  "v_mfma_f32_32x32x16_bf16 v[120:135], v[176:179], a[32:35], v[120:135]\n\t" \
  "ds_read_b64_tr_b16 v[152:153], v4 offset:57344\n\t" \
  "ds_read_b64_tr_b16 v[154:155], v4 offset:59392\n\t" \
  "global_store_dwordx4 v18, v[216:219], s[44:45] offset:1024 sc1\n\t" \
  "v_mfma_f32_32x32x16_bf16 v[120:135], v[180:183], a[36:39], v[120:135]\n\t" \
  "ds_read_b64_tr_b16 v[156:157], v4 offset:61440\n\t" \
  "ds_read_b64_tr_b16 v[158:159], v4 offset:63488\n\t" \
  "s_waitcnt lgkmcnt(8)\n\t" \
  "v_mfma_f32_32x32x16_bf16 v[120:135], v[184:187], a[40:43], v[120:135]\n\t" \
  "ds_read_b64_tr_b16 v[160:161], v8 offset:32768\n\t" \
  "ds_read_b64_tr_b16 v[162:163], v8 offset:34816\n\t" \
  "v_mfma_f32_32x32x16_bf16 v[120:135], v[188:191], a[44:47], v[120:135]\n\t" \
  "ds_read_b64_tr_b16 v[164:165], v8 offset:36864\n\t" \
  "ds_read_b64_tr_b16 v[166:167], v8 offset:38912\n\t" \
  "v_cvt_pk_bf16_f32 v220, v104, v105\n\t" \
  "v_cvt_pk_bf16_f32 v221, v106, v107\n\t" \
  "s_waitcnt lgkmcnt(8)\n\t" \
  "v_mfma_f32_32x32x16_bf16 v[120:135], v[144:147], a[48:51], v[120:135]\n\t" \
  "ds_read_b64_tr_b16 v[168:169], v8 offset:40960\n\t" \
  "ds_read_b64_tr_b16 v[170:171], v8 offset:43008\n\t" \
  "v_cvt_pk_bf16_f32 v222, v108, v109\n\t" \
  "v_cvt_pk_bf16_f32 v223, v110, v111\n\t" \
  "v_mfma_f32_32x32x16_bf16 v[120:135], v[148:151], a[52:55], v[120:135]\n\t" \
  "ds_read_b64_tr_b16 v[172:173], v8 offset:45056\n\t" \
  "ds_read_b64_tr_b16 v[174:175], v8 offset:47104\n\t" \
  "global_store_dwordx4 v19, v[220:223], s[44:45] offset:0 sc1\n\t" \
  "s_waitcnt lgkmcnt(8)\n\t" \
  "v_mfma_f32_32x32x16_bf16 v[120:135], v[152:155], a[56:59], v[120:135]\n\t" \
  "ds_read_b64_tr_b16 v[176:177], v8 offset:49152\n\t" \
  "ds_read_b64_tr_b16 v[178:179], v8 offset:51200\n\t" \
  "v_cvt_pk_bf16_f32 v224, v112, v113\n\t" \
  "v_cvt_pk_bf16_f32 v225, v114, v115\n\t" \
  "v_mfma_f32_32x32x16_bf16 v[120:135], v[156:159], a[60:63], v[120:135]\n\t" \
  "ds_read_b64_tr_b16 v[180:181], v8 offset:53248\n\t" \
  "ds_read_b64_tr_b16 v[182:183], v8 offset:55296\n\t" \
  "v_cvt_pk_bf16_f32 v226, v116, v117\n\t" \
  "v_cvt_pk_bf16_f32 v227, v118, v119\n\t" \
  "s_waitcnt lgkmcnt(8)\n\t" \
  "v_mfma_f32_32x32x16_bf16 v[120:135], v[160:163], a[64:67], v[120:135]\n\t" \
  "ds_read_b64_tr_b16 v[184:185], v8 offset:57344\n\t" \
  "ds_read_b64_tr_b16 v[186:187], v8 offset:59392\n\t" \
  "global_store_dwordx4 v19, v[224:227], s[44:45] offset:1024 sc1\n\t" \
  "v_mfma_f32_32x32x16_bf16 v[120:135], v[164:167], a[68:71], v[120:135]\n\t" \
  "ds_read_b64_tr_b16 v[188:189], v8 offset:61440\n\t" \
  "ds_read_b64_tr_b16 v[190:191], v8 offset:63488\n\t" \
  "s_waitcnt lgkmcnt(8)\n\t" \
  "v_mfma_f32_32x32x16_bf16 v[120:135], v[168:171], a[72:75], v[120:135]\n\t" \
  "ds_read_b64_tr_b16 v[144:145], v8 offset:0\n\t" \
  "ds_read_b64_tr_b16 v[146:147], v8 offset:2048\n\t" \
  "v_mfma_f32_32x32x16_bf16 v[120:135], v[172:175], a[76:79], v[120:135]\n\t" \
  "ds_read_b64_tr_b16 v[148:149], v8 offset:4096\n\t" \
  "ds_read_b64_tr_b16 v[150:151], v8 offset:6144\n\t" \
  "s_waitcnt lgkmcnt(8)\n\t" \
  "v_mfma_f32_32x32x16_bf16 v[120:135], v[176:179], a[80:83], v[120:135]\n\t" \
  "ds_read_b64_tr_b16 v[152:153], v8 offset:8192\n\t" \
  "ds_read_b64_tr_b16 v[154:155], v8 offset:10240\n\t" \
  "v_mfma_f32_32x32x16_bf16 v[120:135], v[180:183], a[84:87], v[120:135]\n\t" \
  "ds_read_b64_tr_b16 v[156:157], v8 offset:12288\n\t" \
  "ds_read_b64_tr_b16 v[158:159], v8 offset:14336\n\t" \
  "s_waitcnt lgkmcnt(8)\n\t" \
  "v_mfma_f32_32x32x16_bf16 v[120:135], v[184:187], a[88:91], v[120:135]\n\t" \
  "ds_read_b64_tr_b16 v[160:161], v8 offset:16384\n\t" \
  "ds_read_b64_tr_b16 v[162:163], v8 offset:18432\n\t" \
  "s_waitcnt vmcnt(4)\n\t" \
  "v_mov_b32 v56, s16\n\t" \
  "s_mov_b64 exec, 1\n\t" \
  "global_store_dwordx2 v56, v[138:139], s[44:45] sc1\n\t" \
  "s_mov_b64 exec, -1\n\t" \
  "v_mfma_f32_32x32x16_bf16 v[120:135], v[188:191], a[92:95], v[120:135]\n\t" \
  "ds_read_b64_tr_b16 v[164:165], v8 offset:20480\n\t" \
  "ds_read_b64_tr_b16 v[166:167], v8 offset:22528\n\t" \
  "s_waitcnt lgkmcnt(8)\n\t" \
  "v_mfma_f32_32x32x16_bf16 v[120:135], v[144:147], a[96:99], v[120:135]\n\t" \
  "ds_read_b64_tr_b16 v[168:169], v8 offset:24576\n\t" \
  "ds_read_b64_tr_b16 v[170:171], v8 offset:26624\n\t" \
  "v_mfma_f32_32x32x16_bf16 v[120:135], v[148:151], a[100:103], v[120:135]\n\t" \
  "ds_read_b64_tr_b16 v[172:173], v8 offset:28672\n\t" \
  "ds_read_b64_tr_b16 v[174:175], v8 offset:30720\n\t" \
  "s_waitcnt lgkmcnt(8)\n\t" \
  "v_mfma_f32_32x32x16_bf16 v[120:135], v[152:155], a[104:107], v[120:135]\n\t" \
  "ds_read_b64_tr_b16 v[176:177], v16 offset:0\n\t" \
  "ds_read_b64_tr_b16 v[178:179], v16 offset:2048\n\t" \
  "v_mfma_f32_32x32x16_bf16 v[120:135], v[156:159], a[108:111], v[120:135]\n\t" \
  "ds_read_b64_tr_b16 v[180:181], v16 offset:4096\n\t" \
  "ds_read_b64_tr_b16 v[182:183], v16 offset:6144\n\t" \
  "s_waitcnt lgkmcnt(8)\n\t" \
  "v_mfma_f32_32x32x16_bf16 v[120:135], v[160:163], a[112:115], v[120:135]\n\t" \
  "ds_read_b64_tr_b16 v[184:185], v16 offset:8192\n\t" \
  "ds_read_b64_tr_b16 v[186:187], v16 offset:10240\n\t" \
  "v_mfma_f32_32x32x16_bf16 v[120:135], v[164:167], a[116:119], v[120:135]\n\t" \
  "ds_read_b64_tr_b16 v[188:189], v16 offset:12288\n\t" \
  "ds_read_b64_tr_b16 v[190:191], v16 offset:14336\n\t" \
  "s_waitcnt lgkmcnt(8)\n\t" \
  "v_mfma_f32_32x32x16_bf16 v[120:135], v[168:171], a[120:123], v[120:135]\n\t" \
  "s_waitcnt vmcnt(3)\n\t" \
  "v_mov_b32 v56, s17\n\t" \
  "s_mov_b64 exec, 1\n\t" \
  "global_store_dwordx2 v56, v[138:139], s[44:45] sc1\n\t" \
  "s_mov_b64 exec, -1\n\t" \
  "v_mfma_f32_32x32x16_bf16 v[120:135], v[172:175], a[124:127], v[120:135]\n\t" \
  "s_waitcnt lgkmcnt(4)\n\t" \
  "v_mfma_f32_32x32x16_bf16 v[120:135], v[176:179], a[128:131], v[120:135]\n\t" \
  "v_mfma_f32_32x32x16_bf16 v[120:135], v[180:183], a[132:135], v[120:135]\n\t" \
  "s_waitcnt lgkmcnt(0)\n\t" \
  "v_mfma_f32_32x32x16_bf16 v[120:135], v[184:187], a[136:139], v[120:135]\n\t" \
  "v_mfma_f32_32x32x16_bf16 v[120:135], v[188:191], a[140:143], v[120:135]\n\t" \
  "s_waitcnt vmcnt(2)\n\t" \
  "v_mov_b32 v56, s18\n\t" \
  "s_mov_b64 exec, 1\n\t" \
  "global_store_dwordx2 v56, v[138:139], s[44:45] sc1\n\t" \
  "s_mov_b64 exec, -1\n\t" \
  "global_load_dwordx2 v[142:143], v57, s[44:45] sc1\n\t" \
  "s_waitcnt vmcnt(0)\n\t" \
  "v_cmp_eq_u64 vcc, s[68:69], v[142:143]\n\t" \
  "s_nop 1\n\t" \
  "s_and_b32 s58, vcc_lo, 7\n\t" \
  "s_cmp_eq_u32 s58, 7\n\t" \
  "s_cbranch_scc0 RFS%=\n\t" \
  "RFB%=:\n\t" \
  "global_load_dwordx4 v[228:231], v20, s[44:45] offset:0 sc1\n\t" \
  "global_load_dwordx4 v[232:235], v20, s[44:45] offset:1024 sc1\n\t" \
  "global_load_dwordx4 v[236:239], v58, s[44:45] offset:0 sc1\n\t" \
  "global_load_dwordx4 v[240:243], v58, s[44:45] offset:1024 sc1\n\t" \
  "global_load_dwordx4 v[244:247], v59, s[44:45] offset:0 sc1\n\t" \
  "global_load_dwordx4 v[248:251], v59, s[44:45] offset:1024 sc1\n\t" \
  "s_cmp_eq_u32 s84, 0\n\t" \
  "s_cbranch_scc0 GDF%=\n\t" \
  "s_mov_b32 s56, 0x10000\n\t" \
  "GLF%=:\n\t" \
  "v_cmp_eq_u64 vcc, s[68:69], v[48:49]\n\t" \
  "v_cmp_eq_u64 s[58:59], s[68:69], v[50:51]\n\t" \
  "v_cmp_eq_u64 s[60:61], s[68:69], v[52:53]\n\t" \
  "v_cmp_eq_u64 s[62:63], s[68:69], v[54:55]\n\t" \
  "s_nop 1\n\t" \
  "s_and_b64 vcc, vcc, s[58:59]\n\t" \
  "s_and_b64 s[60:61], s[60:61], s[62:63]\n\t" \
  "s_and_b64 vcc, vcc, s[60:61]\n\t" \
  "s_cmp_eq_u64 vcc, -1\n\t" \
  "s_cbranch_scc1 GKF%=\n\t" \
  "s_sleep 1\n\t" \
  "global_load_dwordx2 v[48:49], v9, s[44:45] offset:0 sc1\n\t" \
  "global_load_dwordx2 v[50:51], v9, s[44:45] offset:1024 sc1\n\t" \
  "global_load_dwordx2 v[52:53], v9, s[44:45] offset:2048 sc1\n\t" \
  "global_load_dwordx2 v[54:55], v9, s[44:45] offset:3072 sc1\n\t" \
  "s_waitcnt vmcnt(0)\n\t" \
  "s_sub_u32 s56, s56, 1\n\t" \
  "s_cbranch_scc0 GLF%=\n\t" \
  "GKF%=:\n\t" \
  "s_barrier\n\t" \
  "s_cmp_eq_u32 s86, 0\n\t" \
  "s_cbranch_scc0 GDF%=\n\t" \
  "v_mov_b32 v56, 0\n\t" \
  "v_mov_b32 v62, s14\n\t" \
  "v_mov_b32 v63, s15\n\t" \
  "s_mov_b64 exec, 1\n\t" \
  "global_store_dwordx2 v56, v[62:63], s[44:45] sc1\n\t" \
  "s_mov_b64 exec, -1\n\t" \
  "GDF%=:\n\t" \
  "s_waitcnt vmcnt(5)\n\t" \
  "v_dot2c_f32_bf16 v120, s30, v228\n\t" \
  "v_dot2c_f32_bf16 v121, s31, v228\n\t" \
  "v_dot2c_f32_bf16 v122, s30, v229\n\t" \
  "v_dot2c_f32_bf16 v123, s31, v229\n\t" \
  "v_dot2c_f32_bf16 v124, s30, v230\n\t" \
  "v_dot2c_f32_bf16 v125, s31, v230\n\t" \
  "v_dot2c_f32_bf16 v126, s30, v231\n\t" \
  "v_dot2c_f32_bf16 v127, s31, v231\n\t" \
  "s_waitcnt vmcnt(4)\n\t" \
  "v_dot2c_f32_bf16 v128, s30, v232\n\t" \
  "v_dot2c_f32_bf16 v129, s31, v232\n\t" \
  "v_dot2c_f32_bf16 v130, s30, v233\n\t" \
  "v_dot2c_f32_bf16 v131, s31, v233\n\t" \
  "v_dot2c_f32_bf16 v132, s30, v234\n\t" \
  "v_dot2c_f32_bf16 v133, s31, v234\n\t" \
  "v_dot2c_f32_bf16 v134, s30, v235\n\t" \
  "v_dot2c_f32_bf16 v135, s31, v235\n\t" \
  "s_waitcnt vmcnt(3)\n\t" \
  "v_dot2c_f32_bf16 v120, s30, v236\n\t" \
  "v_dot2c_f32_bf16 v121, s31, v236\n\t" \
  "v_dot2c_f32_bf16 v122, s30, v237\n\t" \
  "v_dot2c_f32_bf16 v123, s31, v237\n\t" \
  "v_dot2c_f32_bf16 v124, s30, v238\n\t" \
  "v_dot2c_f32_bf16 v125, s31, v238\n\t" \
  "v_dot2c_f32_bf16 v126, s30, v239\n\t" \
  "v_dot2c_f32_bf16 v127, s31, v239\n\t" \
  "s_waitcnt vmcnt(2)\n\t" \
  "v_dot2c_f32_bf16 v128, s30, v240\n\t" \
  "v_dot2c_f32_bf16 v129, s31, v240\n\t" \
  "v_dot2c_f32_bf16 v130, s30, v241\n\t" \
  "v_dot2c_f32_bf16 v131, s31, v241\n\t" \
  "v_dot2c_f32_bf16 v132, s30, v242\n\t" \
  "v_dot2c_f32_bf16 v133, s31, v242\n\t" \
  "v_dot2c_f32_bf16 v134, s30, v243\n\t" \
  "v_dot2c_f32_bf16 v135, s31, v243\n\t" \
  "s_waitcnt vmcnt(1)\n\t" \
  "v_dot2c_f32_bf16 v120, s30, v244\n\t" \
  "v_dot2c_f32_bf16 v121, s31, v244\n\t" \
  "v_dot2c_f32_bf16 v122, s30, v245\n\t" \
  "v_dot2c_f32_bf16 v123, s31, v245\n\t" \
  "v_dot2c_f32_bf16 v124, s30, v246\n\t" \
  "v_dot2c_f32_bf16 v125, s31, v246\n\t" \
  "v_dot2c_f32_bf16 v126, s30, v247\n\t" \
  "v_dot2c_f32_bf16 v127, s31, v247\n\t" \
  "s_waitcnt vmcnt(0)\n\t" \
  "v_dot2c_f32_bf16 v128, s30, v248\n\t" \
  "v_dot2c_f32_bf16 v129, s31, v248\n\t" \
  "v_dot2c_f32_bf16 v130, s30, v249\n\t" \
  "v_dot2c_f32_bf16 v131, s31, v249\n\t" \
  "v_dot2c_f32_bf16 v132, s30, v250\n\t" \
  "v_dot2c_f32_bf16 v133, s31, v250\n\t" \
  "v_dot2c_f32_bf16 v134, s30, v251\n\t" \
  "v_dot2c_f32_bf16 v135, s31, v251\n\t" \
  "s_mov_b32 s58, 0xbf317218\n\t" \
  "v_pk_fma_f32 v[120:121], v[120:121], s[58:59], v[32:33] op_sel_hi:[1,0,1]\n\t" \
  "v_pk_fma_f32 v[122:123], v[122:123], s[58:59], v[34:35] op_sel_hi:[1,0,1]\n\t" \
  "v_pk_fma_f32 v[124:125], v[124:125], s[58:59], v[36:37] op_sel_hi:[1,0,1]\n\t" \
  "v_pk_fma_f32 v[126:127], v[126:127], s[58:59], v[38:39] op_sel_hi:[1,0,1]\n\t" \
  "v_pk_fma_f32 v[128:129], v[128:129], s[58:59], v[40:41] op_sel_hi:[1,0,1]\n\t" \
  "v_pk_fma_f32 v[130:131], v[130:131], s[58:59], v[42:43] op_sel_hi:[1,0,1]\n\t" \
  "v_pk_fma_f32 v[132:133], v[132:133], s[58:59], v[44:45] op_sel_hi:[1,0,1]\n\t" \
  "v_pk_fma_f32 v[134:135], v[134:135], s[58:59], v[46:47] op_sel_hi:[1,0,1]\n\t" \
  "v_cvt_pk_bf16_f32 v204, v120, v121\n\t" \
  "v_cvt_pk_bf16_f32 v205, v122, v123\n\t" \
  "v_cvt_pk_bf16_f32 v206, v124, v125\n\t" \
  "v_cvt_pk_bf16_f32 v207, v126, v127\n\t" \
  "v_cvt_pk_bf16_f32 v208, v128, v129\n\t" \
  "v_cvt_pk_bf16_f32 v209, v130, v131\n\t" \
  "v_cvt_pk_bf16_f32 v210, v132, v133\n\t" \
  "v_cvt_pk_bf16_f32 v211, v134, v135\n\t" \
  "v_permlane32_swap_b32 v204, v206\n\t" \
  "v_permlane32_swap_b32 v205, v207\n\t" \
  "v_permlane32_swap_b32 v208, v210\n\t" \
  "v_permlane32_swap_b32 v209, v211\n\t" \
  "s_nop 0\n\t" \
  "v_permlane16_swap_b32 v204, v208\n\t" \
  "v_permlane16_swap_b32 v205, v209\n\t" \
  "v_permlane16_swap_b32 v206, v210\n\t" \
  "v_permlane16_swap_b32 v207, v211\n\t" \
  "global_store_dwordx4 v14, v[204:207], s[80:81] sc1\n\t" \
  "global_store_dwordx4 v61, v[208:211], s[80:81] sc1\n\t" \
  "s_branch FIN%=\n\t" \
  "ATT%=:\n\t" \
  "s_or_b32 s58, s87, s86\n\t" \
  "s_cmp_eq_u32 s58, 0\n\t" \
  "s_cselect_b32 s58, 32768, 0\n\t" \
  "s_lshl_b32 s54, s87, 17\n\t" \
  "s_mul_i32 s55, s86, 40960\n\t" \
  "s_add_u32 s54, s54, s55\n\t" \
  "s_add_u32 s54, s54, s58\n\t" \
  "s_sub_u32 s54, s54, 28672\n\t" \
  "s_and_b32 s55, s86, 1\n\t" \
  "s_lshl_b32 s55, s55, 2\n\t" \
  "v_xor_b32 v90, s55, v90\n\t" \
  "v_lshl_add_u32 v91, v82, 10, s54\n\t" \
  "v_lshl_add_u32 v64, v90, 4, v91\n\t" \
  "s_sub_u32 s58, 32768, s58\n\t" \
  "v_xor_b32 v65, 64, v64\n\t" \
  "v_add_u32 v66, 14336, v64\n\t" \
  "v_add_u32 v65, 7168, v65\n\t" \
  "v_add_u32 v68, s58, v64\n\t" \
  "s_mul_i32 s55, s86, 49152\n\t" \
  "v_add_u32 v67, 14336, v65\n\t" \
  "v_lshrrev_b32 v88, 2, v80\n\t" \
  "v_and_b32 v88, 4, v88\n\t" \
  "v_xor_b32 v88, v88, v83\n\t" \
  "v_lshlrev_b32 v88, 4, v88\n\t" \
  "s_lshl_b32 s56, s88, 7\n\t" \
  "v_mul_u32_u24 v92, 3072, v82\n\t" \
  "s_add_u32 s55, s55, s56\n\t" \
  "s_add_u32 s55, s55, 4096\n\t" \
  "v_add3_u32 v69, v92, v88, s55\n\t" \
  "s_mul_i32 s47, s86, 5120\n\t" \
  "v_add_u32 v70, 23552, v69\n\t" \
  "v_add_u32 v71, 1024, v69\n\t" \
  "s_lshl_b32 s48, s86, 11\n\t" \
  "v_add_u32 v72, 1024, v70\n\t" \
  "v_add_u32 v73, 2048, v69\n\t" \
  "v_add_u32 v74, 2048, v70\n\t" \
  "s_add_u32 s48, s48, 20480\n\t" \
  "s_waitcnt lgkmcnt(0)\n\t" \
  "s_mov_b32 s92, s42\n\t" \
  "s_mov_b32 s93, s43\n\t" \
  "s_sub_u32 s40, s40, 4096\n\t" \
  "s_subb_u32 s41, s41, 0\n\t" \
  "s_sub_u32 s42, s42, 4096\n\t" \
  "s_subb_u32 s43, s43, 0\n\t" \
  "s_lshr_b32 s85, s84, 6\n\t" \
  "s_add_u32 s85, s85, s87\n\t" \
  "s_and_b32 s85, s85, 1\n\t" \
  "s_lshl_b32 s85, s85, 2\n\t" \
  "s_xor_b32 s57, s85, 0\n\t" \
  "s_lshl_b32 s58, s57, 7\n\t" \
  "s_add_u32 s4, s40, s58\n\t" \
  "s_addc_u32 s5, s41, 0\n\t" \
  "s_mul_i32 s58, s57, 196608\n\t" \
  "s_add_u32 s6, s42, s58\n\t" \
  "s_addc_u32 s7, s43, 0\n\t" \
  "s_add_u32 m0, s47, 0\n\t" \
  "s_nop 0\n\t" \
  "global_load_lds_dwordx4 v64, s[4:5] offset:0\n\t" \
  "global_load_lds_dwordx4 v65, s[4:5] offset:1024\n\t" \
  "global_load_lds_dwordx4 v66, s[4:5] offset:2048\n\t" \
  "global_load_lds_dwordx4 v67, s[4:5] offset:3072\n\t" \
  "s_add_u32 m0, s47, 4096\n\t" \
  "s_nop 0\n\t" \
  "global_load_lds_dwordx4 v68, s[4:5] offset:0\n\t" \
  "s_add_u32 m0, s48, 0\n\t" \
  "s_nop 0\n\t" \
  "global_load_lds_dwordx4 v69, s[6:7] offset:0\n\t" \
  "global_load_lds_dwordx4 v70, s[6:7] offset:1024\n\t" \
  "s_add_u32 m0, s48, 8192\n\t" \
  "s_nop 0\n\t" \
  "global_load_lds_dwordx4 v71, s[6:7] offset:0\n\t" \
  "global_load_lds_dwordx4 v72, s[6:7] offset:1024\n\t" \
  "s_add_u32 m0, s48, 16384\n\t" \
  "s_nop 0\n\t" \
  "global_load_lds_dwordx4 v73, s[6:7] offset:0\n\t" \
  "global_load_lds_dwordx4 v74, s[6:7] offset:1024\n\t" \
  "s_lshl2_add_u32 s54, s89, s86\n\t" \
  "s_lshl_b32 s54, s54, 9\n\t" \
  "v_and_b32 v96, 3, v80\n\t" \
  "v_lshl_add_u32 v96, v96, 7, s54\n\t" \
  "v_min_u32 v96, END%=-BEG%=, v96\n\t" \
  "global_load_dword v141, v96, s[10:11]\n\t" \
  "s_lshl2_add_u32 s54, s89, s86\n\t" \
  "v_min_u32 v96, 0, v80\n\t" \
  "s_mul_i32 s55, s54, 1\n\t" \
  "v_add_u32 v96, s55, v96\n\t" \
  "v_lshlrev_b32 v96, 16, v96\n\t" \
  "v_min_u32 v96, 1048448, v96\n\t" \
  "s_lshl_b32 s55, s50, 20\n\t" \
  "s_add_u32 s55, s55, 4096\n\t" \
  "v_add_u32 v96, s55, v96\n\t" \
  "global_load_dword v141, v96, s[40:41]\n\t" \
  "v_min_u32 v96, 0, v80\n\t" \
  "s_mul_i32 s55, s54, 1\n\t" \
  "v_add_u32 v96, s55, v96\n\t" \
  "v_lshlrev_b32 v96, 16, v96\n\t" \
  "v_min_u32 v96, 1572736, v96\n\t" \
  "global_load_dword v141, v96, s[92:93]\n\t" \
  "v_min_u32 v96, 0, v80\n\t" \
  "s_mul_i32 s55, s54, 1\n\t" \
  "v_add_u32 v96, s55, v96\n\t" \
  "v_lshlrev_b32 v96, 16, v96\n\t" \
  "v_min_u32 v96, 1834880, v96\n\t" \
  "global_load_dword v141, v96, s[64:65]\n\t" \
  "v_min_u32 v96, 0, v80\n\t" \
  "s_mul_i32 s55, s54, 1\n\t" \
  "v_add_u32 v96, s55, v96\n\t" \
  "v_lshlrev_b32 v96, 16, v96\n\t" \
  "v_min_u32 v96, 1834880, v96\n\t" \
  "global_load_dword v141, v96, s[66:67]\n\t" \
  "v_min_u32 v96, 0, v80\n\t" \
  "s_mul_i32 s55, s54, 1\n\t" \
  "v_add_u32 v96, s55, v96\n\t" \
  "v_lshlrev_b32 v96, 16, v96\n\t" \
  "v_min_u32 v96, 524160, v96\n\t" \
  "global_load_dword v141, v96, s[62:63]\n\t" \
  "v_min_u32 v96, 0, v80\n\t" \
  "s_mul_i32 s55, s54, 1\n\t" \
  "v_add_u32 v96, s55, v96\n\t" \
  "v_lshlrev_b32 v96, 16, v96\n\t" \
  "v_min_u32 v96, 2097024, v96\n\t" \
  "global_load_dword v141, v96, s[80:81]\n\t" \
  "v_min_u32 v96, 2, v80\n\t" \
  "s_mul_i32 s55, s54, 3\n\t" \
  "v_add_u32 v96, s55, v96\n\t" \
  "v_lshlrev_b32 v96, 16, v96\n\t" \
  "v_min_u32 v96, 17825664, v96\n\t" \
  "global_load_dword v141, v96, s[44:45]\n\t" \
  "s_xor_b32 s57, s85, 1\n\t" \
  "s_lshl_b32 s58, s57, 7\n\t" \
  "s_add_u32 s4, s40, s58\n\t" \
  "s_addc_u32 s5, s41, 0\n\t" \
  "s_mul_i32 s58, s57, 196608\n\t" \
  "s_add_u32 s6, s42, s58\n\t" \
  "s_addc_u32 s7, s43, 0\n\t" \
  "s_lshr_b32 s90, s86, 1\n\t" \
  "s_and_b32 s91, s86, 1\n\t" \
  "v_and_b32 v84, 31, v80\n\t" \
  "s_add_u32 m0, s47, 45056\n\t" \
  "s_nop 0\n\t" \
  "global_load_lds_dwordx4 v64, s[4:5] offset:0\n\t" \
  "v_lshrrev_b32 v85, 5, v80\n\t" \
  "v_bfe_u32 v86, v80, 2, 2\n\t" \
  "v_bfe_u32 v111, v80, 1, 3\n\t" \
  "global_load_lds_dwordx4 v65, s[4:5] offset:1024\n\t" \
  "v_xor_b32 v111, v111, v85\n\t" \
  "v_lshlrev_b32 v111, 4, v111\n\t" \
  "v_lshl_add_u32 v111, v84, 7, v111\n\t" \
  "global_load_lds_dwordx4 v66, s[4:5] offset:2048\n\t" \
  "v_and_b32 v87, 3, v80\n\t" \
  "v_lshlrev_b32 v87, 3, v87\n\t" \
  "v_and_b32 v112, 16, v80\n\t" \
  "global_load_lds_dwordx4 v67, s[4:5] offset:3072\n\t" \
  "v_lshl_add_u32 v87, v112, 1, v87\n\t" \
  "v_lshl_add_u32 v112, v85, 3, v86\n\t" \
  "s_lshl_b32 s54, s90, 13\n\t" \
  "s_add_u32 m0, s47, 49152\n\t" \
  "s_nop 0\n\t" \
  "global_load_lds_dwordx4 v68, s[4:5] offset:0\n\t" \
  "s_add_u32 s55, s54, 4096\n\t" \
  "v_add_u32 v48, s55, v111\n\t" \
  "s_add_u32 m0, s48, 45056\n\t" \
  "s_nop 0\n\t" \
  "global_load_lds_dwordx4 v69, s[6:7] offset:0\n\t" \
  "v_mov_b32 v52, v111\n\t" \
  "v_and_b32 v113, 8, v80\n\t" \
  "v_lshl_add_u32 v87, v113, 3, v87\n\t" \
  "global_load_lds_dwordx4 v70, s[6:7] offset:1024\n\t" \
  "s_lshl_b32 s55, s91, 6\n\t" \
  "v_xor_b32 v56, s55, v87\n\t" \
  "v_lshl_add_u32 v56, v112, 7, v56\n\t" \
  "s_add_u32 m0, s48, 53248\n\t" \
  "s_nop 0\n\t" \
  "global_load_lds_dwordx4 v71, s[6:7] offset:0\n\t" \
  "v_add_u32 v56, 20480, v56\n\t" \
  "s_add_u32 s54, s54, 8192\n\t" \
  "v_add_u32 v57, s54, v56\n\t" \
  "global_load_lds_dwordx4 v72, s[6:7] offset:1024\n\t" \
  "v_xor_b32 v49, 32, v48\n\t" \
  "v_xor_b32 v50, 64, v48\n\t" \
  "v_xor_b32 v51, 96, v48\n\t" \
  "s_add_u32 m0, s48, 61440\n\t" \
  "s_nop 0\n\t" \
  "global_load_lds_dwordx4 v73, s[6:7] offset:0\n\t" \
  "v_xor_b32 v53, 32, v52\n\t" \
  "v_xor_b32 v54, 64, v52\n\t" \
  "v_xor_b32 v55, 96, v52\n\t" \
  "global_load_lds_dwordx4 v74, s[6:7] offset:1024\n\t" \
  "s_lshl2_add_u32 s94, s88, s86\n\t" \
  "s_lshl_b32 s94, s94, 2\n\t" \
  "s_lshl_b32 s33, s94, 7\n\t" \
  "s_add_u32 s33, s33, 4096\n\t" \
  "v_lshrrev_b32 v24, 1, v0\n\t" \
  "v_add_u32 v24, s33, v24\n\t" \
  "v_add_u32 v25, 90112, v24\n\t" \
  "s_lshr_b32 s95, s87, 1\n\t" \
  "s_cmp_ge_u32 s95, 6\n\t" \
  "s_cselect_b32 s33, 6, 0\n\t" \
  "s_sub_u32 s95, s95, s33\n\t" \
  "s_and_b32 s33, s95, 1\n\t" \
  "s_xor_b32 s33, s33, s91\n\t" \
  "s_lshl_b32 s33, s33, 6\n\t" \
  "v_xor_b32 v30, s33, v56\n\t" \
  "s_lshr_b32 s33, s95, 1\n\t" \
  "s_lshl2_add_u32 s33, s33, s86\n\t" \
  "s_lshl_b32 s33, s33, 11\n\t" \
  "v_add_u32 v30, s33, v30\n\t" \
  "v_add_u32 v31, 90112, v30\n\t" \
  "v_bfe_u32 v40, v0, 4, 4\n\t" \
  "v_bfe_u32 v41, v0, 8, 1\n\t" \
  "v_cmp_eq_u32 vcc, v40, v41\n\t" \
  "v_mov_b32 v41, 0x3f803f80\n\t" \
  "s_nop 0\n\t" \
  "v_cndmask_b32 v40, 0, v41, vcc\n\t" \
  "v_mov_b32 v41, v40\n\t" \
  "v_mov_b32 v42, v40\n\t" \
  "v_mov_b32 v43, v40\n\t" \
  "s_mov_b32 s61, 0x3f803f80\n\t" \
  "s_waitcnt vmcnt(19)\n\t" \
  "s_barrier\n\t" \
  "ds_read_b128 v[80:83], v48\n\t" \
  "ds_read_b64_tr_b16 v[92:93], v56 offset:0\n\t" \
  "ds_read_b64_tr_b16 v[94:95], v56 offset:512\n\t" \
  "ds_read_b128 v[84:87], v48 offset:4096\n\t" \
  "ds_read_b64_tr_b16 v[96:97], v56 offset:8192\n\t" \
  "ds_read_b64_tr_b16 v[98:99], v56 offset:8704\n\t" \
  "ds_read_b64_tr_b16 v[100:101], v56 offset:16384\n\t" \
  "ds_read_b64_tr_b16 v[102:103], v56 offset:16896\n\t" \
  "ds_read_b128 v[104:107], v52\n\t" \
  "ds_read_b64_tr_b16 v[88:89], v57 offset:0\n\t" \
  "ds_read_b64_tr_b16 v[90:91], v57 offset:512\n\t" \
  "s_waitcnt lgkmcnt(8)\n\t" \
  "v_mfma_f32_32x32x16_bf16 v[144:159], v[92:95], v[80:83], 0\n\t" \
  "ds_read_b128 v[108:111], v49\n\t" \
  "ds_read_b64_tr_b16 v[120:121], v56 offset:2048\n\t" \
  "ds_read_b64_tr_b16 v[122:123], v56 offset:2560\n\t" \
  "s_xor_b32 s57, s85, 2\n\t" \
  "s_lshl_b32 s58, s57, 7\n\t" \
  "s_add_u32 s4, s40, s58\n\t" \
  "s_addc_u32 s5, s41, 0\n\t" \
  "s_waitcnt lgkmcnt(10)\n\t" \
  "v_mfma_f32_32x32x16_bf16 v[192:207], v[92:95], v[84:87], 0\n\t" \
  "ds_read_b128 v[112:115], v49 offset:4096\n\t" \
  "ds_read_b64_tr_b16 v[124:125], v56 offset:10240\n\t" \
  "ds_read_b64_tr_b16 v[126:127], v56 offset:10752\n\t" \
  "s_mul_i32 s58, s57, 196608\n\t" \
  "s_add_u32 s6, s42, s58\n\t" \
  "s_addc_u32 s7, s43, 0\n\t" \
  "s_waitcnt lgkmcnt(11)\n\t" \
  "v_mfma_f32_32x32x16_bf16 v[160:175], v[96:99], v[80:83], 0\n\t" \
  "ds_read_b64_tr_b16 v[128:129], v56 offset:18432\n\t" \
  "ds_read_b64_tr_b16 v[130:131], v56 offset:18944\n\t" \
  "v_mfma_f32_32x32x16_bf16 v[208:223], v[96:99], v[84:87], 0\n\t" \
  "ds_read_b128 v[132:135], v53\n\t" \
  "ds_read_b64_tr_b16 v[116:117], v57 offset:2048\n\t" \
  "ds_read_b64_tr_b16 v[118:119], v57 offset:2560\n\t" \
  "v_mov_b32 v58, 0\n\t" \
  "global_load_dwordx2 v[138:139], v58, s[44:45] sc1\n\t" \
  "s_add_u32 m0, s47, 90112\n\t" \
  "s_waitcnt lgkmcnt(14)\n\t" \
  "v_mfma_f32_32x32x16_bf16 v[176:191], v[100:103], v[80:83], 0\n\t" \
  "global_load_lds_dwordx4 v64, s[4:5] offset:0\n\t" \
  "s_lshl2_add_u32 s33, s84, s86\n\t" \
  "s_mul_i32 s33, s33, 2304\n\t" \
  "v_min_u32 v75, 272, v0\n\t" \
  "v_lshl_add_u32 v75, v75, 3, s33\n\t" \
  "v_mfma_f32_32x32x16_bf16 v[224:239], v[100:103], v[84:87], 0\n\t" \
  "global_load_lds_dwordx4 v65, s[4:5] offset:1024\n\t" \
  "v_cmp_gt_u32 vcc, 1835008, v75\n\t" \
  "v_mov_b32 v136, s62\n\t" \
  "v_mov_b32 v137, s63\n\t" \
  "s_waitcnt lgkmcnt(11)\n\t" \
  "v_mfma_f32_32x32x16_bf16 v[240:255], v[88:91], v[104:107], 0\n\t" \
  "global_load_lds_dwordx4 v66, s[4:5] offset:2048\n\t" \
  "v_mov_b32 v78, s66\n\t" \
  "v_mov_b32 v79, s67\n\t" \
  "v_subrev_u32 v58, 1835008, v75\n\t" \
  "s_waitcnt lgkmcnt(0)\n\t" \
  "v_mfma_f32_32x32x16_bf16 v[144:159], v[120:123], v[108:111], v[144:159]\n\t" \
  "ds_read_b128 v[80:83], v50\n\t" \
  "ds_read_b64_tr_b16 v[92:93], v56 offset:4096\n\t" \
  "ds_read_b64_tr_b16 v[94:95], v56 offset:4608\n\t" \
  "s_mov_b32 s55, 0xb000\n\t" \
  "v_mfma_f32_32x32x16_bf16 v[192:207], v[120:123], v[112:115], v[192:207]\n\t" \
  "ds_read_b128 v[84:87], v50 offset:4096\n\t" \
  "ds_read_b64_tr_b16 v[96:97], v56 offset:12288\n\t" \
  "ds_read_b64_tr_b16 v[98:99], v56 offset:12800\n\t" \
  "v_mfma_f32_32x32x16_bf16 v[160:175], v[124:127], v[108:111], v[160:175]\n\t" \
  "ds_read_b64_tr_b16 v[100:101], v56 offset:20480\n\t" \
  "ds_read_b64_tr_b16 v[102:103], v56 offset:20992\n\t" \
  "v_mfma_f32_32x32x16_bf16 v[208:223], v[124:127], v[112:115], v[208:223]\n\t" \
  "ds_read_b128 v[104:107], v54\n\t" \
  "ds_read_b64_tr_b16 v[88:89], v57 offset:4096\n\t" \
  "ds_read_b64_tr_b16 v[90:91], v57 offset:4608\n\t" \
  "v_mfma_f32_32x32x16_bf16 v[176:191], v[128:131], v[108:111], v[176:191]\n\t" \
  "global_load_lds_dwordx4 v67, s[4:5] offset:3072\n\t" \
  "ds_read_b64 v[26:27], v24 offset:0\n\t" \
  "ds_read_b64_tr_b16 v[32:33], v30 offset:0\n\t" \
  "ds_read_b64_tr_b16 v[34:35], v30 offset:512\n\t" \
  "v_cndmask_b32 v136, v136, v78, vcc\n\t" \
  "v_cndmask_b32 v137, v137, v79, vcc\n\t" \
  "v_cndmask_b32 v75, v58, v75, vcc\n\t" \
  "s_add_u32 m0, s47, 94208\n\t" \
  "v_mfma_f32_32x32x16_bf16 v[224:239], v[128:131], v[112:115], v[224:239]\n\t" \
  "global_load_lds_dwordx4 v68, s[4:5] offset:0\n\t" \
  "v_add_co_u32 v136, vcc, v136, v75\n\t" \
  "s_nop 1\n\t" \
  "v_addc_co_u32 v137, vcc, 0, v137, vcc\n\t" \
  "s_lshl_b32 s33, s87, 7\n\t" \
  "s_add_u32 m0, s48, 90112\n\t" \
  "v_mfma_f32_32x32x16_bf16 v[240:255], v[116:119], v[132:135], v[240:255]\n\t" \
  "global_load_lds_dwordx4 v69, s[6:7] offset:0\n\t" \
  "s_add_u32 s33, s33, s94\n\t" \
  "s_lshl_b32 s33, s33, 5\n\t" \
  "s_and_b32 s34, s85, 4\n\t" \
  "s_waitcnt lgkmcnt(3)\n\t" \
  "v_mfma_f32_32x32x16_bf16 v[144:159], v[92:95], v[80:83], v[144:159]\n\t" \
  "ds_read_b128 v[108:111], v51\n\t" \
  "ds_read_b64_tr_b16 v[120:121], v56 offset:6144\n\t" \
  "ds_read_b64_tr_b16 v[122:123], v56 offset:6656\n\t" \
  "v_mfma_f32_32x32x16_bf16 v[192:207], v[92:95], v[84:87], v[192:207]\n\t" \
  "ds_read_b128 v[112:115], v51 offset:4096\n\t" \
  "ds_read_b64_tr_b16 v[124:125], v56 offset:14336\n\t" \
  "ds_read_b64_tr_b16 v[126:127], v56 offset:14848\n\t" \
  "v_mfma_f32_32x32x16_bf16 v[160:175], v[96:99], v[80:83], v[160:175]\n\t" \
  "ds_read_b64_tr_b16 v[128:129], v56 offset:22528\n\t" \
  "ds_read_b64_tr_b16 v[130:131], v56 offset:23040\n\t" \
  "v_mfma_f32_32x32x16_bf16 v[208:223], v[96:99], v[84:87], v[208:223]\n\t" \
  "ds_read_b128 v[132:135], v55\n\t" \
  "ds_read_b64_tr_b16 v[116:117], v57 offset:6144\n\t" \
  "ds_read_b64_tr_b16 v[118:119], v57 offset:6656\n\t" \
  "s_waitcnt lgkmcnt(11)\n\t" \
  "v_mfma_f32_16x16x32_bf16 v[36:39], v[40:43], v[32:35], 0\n\t" \
  "v_mfma_f32_32x32x16_bf16 v[176:191], v[100:103], v[80:83], v[176:191]\n\t" \
  "global_load_lds_dwordx4 v70, s[6:7] offset:1024\n\t" \
  "v_dot2_f32_bf16 v28, v26, s61, 0\n\t" \
  "v_dot2_f32_bf16 v29, v26, v26, 0\n\t" \
  "s_lshl2_add_u32 s33, s34, s33\n\t" \
  "s_add_u32 s33, s33, 8192\n\t" \
  "v_lshrrev_b32 v75, 8, v0\n\t" \
  "s_add_u32 m0, s48, 98304\n\t" \
  "v_mfma_f32_32x32x16_bf16 v[224:239], v[100:103], v[84:87], v[224:239]\n\t" \
  "global_load_lds_dwordx4 v71, s[6:7] offset:0\n\t" \
  "v_lshl_add_u32 v142, v75, 5, s33\n\t" \
  "v_bfe_u32 v75, v0, 4, 1\n\t" \
  "v_lshl_add_u32 v142, v75, 3, v142\n\t" \
  "v_mfma_f32_32x32x16_bf16 v[240:255], v[88:91], v[104:107], v[240:255]\n\t" \
  "global_load_lds_dwordx4 v72, s[6:7] offset:1024\n\t" \
  "v_dot2c_f32_bf16 v28, s61, v27\n\t" \
  "v_dot2c_f32_bf16 v29, v27, v27\n\t" \
  "s_lshl_b32 s33, s86, 7\n\t" \
  "s_add_u32 s33, s33, 158720\n\t" \
  "v_min_u32 v143, 2048, v0\n\t" \
  "v_cmp_gt_u32 vcc, 256, v0\n\t" \
  "s_waitcnt lgkmcnt(0)\n\t" \
  "s_add_u32 m0, s48, 106496\n\t" \
  "v_mfma_f32_32x32x16_bf16 v[144:159], v[120:123], v[108:111], v[144:159]\n\t" \
  "global_load_lds_dwordx4 v73, s[6:7] offset:0\n\t" \
  "v_add_u32 v48, s55, v48\n\t" \
  "v_add_u32 v49, s55, v49\n\t" \
  "v_add_u32 v50, s55, v50\n\t" \
  "v_add_u32 v51, s55, v51\n\t" \
  "v_add_u32 v52, s55, v52\n\t" \
  "v_mfma_f32_32x32x16_bf16 v[192:207], v[120:123], v[112:115], v[192:207]\n\t" \
  "global_load_lds_dwordx4 v74, s[6:7] offset:1024\n\t" \
  "v_add_u32 v53, s55, v53\n\t" \
  "v_add_u32 v54, s55, v54\n\t" \
  "v_add_u32 v55, s55, v55\n\t" \
  "v_add_u32 v56, s55, v56\n\t" \
  "v_add_u32 v57, s55, v57\n\t" \
  "v_mfma_f32_32x32x16_bf16 v[160:175], v[124:127], v[108:111], v[160:175]\n\t" \
  "s_waitcnt vmcnt(12)\n\t" \
  "s_waitcnt lgkmcnt(0)\n\t" \
  "s_barrier\n\t" \
  "ds_read_b128 v[80:83], v48\n\t" \
  "ds_read_b64_tr_b16 v[92:93], v56 offset:0\n\t" \
  "ds_read_b64_tr_b16 v[94:95], v56 offset:512\n\t" \
  "v_mfma_f32_32x32x16_bf16 v[208:223], v[124:127], v[112:115], v[208:223]\n\t" \
  "ds_read_b128 v[84:87], v48 offset:4096\n\t" \
  "ds_read_b64_tr_b16 v[96:97], v56 offset:8192\n\t" \
  "ds_read_b64_tr_b16 v[98:99], v56 offset:8704\n\t" \
  "v_mfma_f32_32x32x16_bf16 v[176:191], v[128:131], v[108:111], v[176:191]\n\t" \
  "ds_read_b64_tr_b16 v[100:101], v56 offset:16384\n\t" \
  "ds_read_b64_tr_b16 v[102:103], v56 offset:16896\n\t" \
  "v_mfma_f32_32x32x16_bf16 v[224:239], v[128:131], v[112:115], v[224:239]\n\t" \
  "ds_read_b128 v[104:107], v52\n\t" \
  "ds_read_b64_tr_b16 v[88:89], v57 offset:0\n\t" \
  "ds_read_b64_tr_b16 v[90:91], v57 offset:512\n\t" \
  "v_mfma_f32_32x32x16_bf16 v[240:255], v[116:119], v[132:135], v[240:255]\n\t" \
  "v_lshrrev_b32 v75, 2, v0\n\t" \
  "v_mov_b32 v143, 1024\n\t" \
  "v_cndmask_b32 v143, v143, v75, vcc\n\t" \
  "s_waitcnt lgkmcnt(8)\n\t" \
  "v_mfma_f32_32x32x16_bf16 v[144:159], v[92:95], v[80:83], v[144:159]\n\t" \
  "ds_read_b128 v[108:111], v49\n\t" \
  "ds_read_b64_tr_b16 v[120:121], v56 offset:2048\n\t" \
  "ds_read_b64_tr_b16 v[122:123], v56 offset:2560\n\t" \
  "s_xor_b32 s57, s85, 3\n\t" \
  "s_lshl_b32 s58, s57, 7\n\t" \
  "s_add_u32 s4, s40, s58\n\t" \
  "s_addc_u32 s5, s41, 0\n\t" \
  "s_waitcnt lgkmcnt(10)\n\t" \
  "v_mfma_f32_32x32x16_bf16 v[192:207], v[92:95], v[84:87], v[192:207]\n\t" \
  "ds_read_b128 v[112:115], v49 offset:4096\n\t" \
  "ds_read_b64_tr_b16 v[124:125], v56 offset:10240\n\t" \
  "ds_read_b64_tr_b16 v[126:127], v56 offset:10752\n\t" \
  "s_mul_i32 s58, s57, 196608\n\t" \
  "s_add_u32 s6, s42, s58\n\t" \
  "s_addc_u32 s7, s43, 0\n\t" \
  "s_waitcnt lgkmcnt(11)\n\t" \
  "v_mfma_f32_32x32x16_bf16 v[160:175], v[96:99], v[80:83], v[160:175]\n\t" \
  "ds_read_b64_tr_b16 v[128:129], v56 offset:18432\n\t" \
  "ds_read_b64_tr_b16 v[130:131], v56 offset:18944\n\t" \
  "v_mfma_f32_32x32x16_bf16 v[208:223], v[96:99], v[84:87], v[208:223]\n\t" \
  "ds_read_b128 v[132:135], v53\n\t" \
  "ds_read_b64_tr_b16 v[116:117], v57 offset:2048\n\t" \
  "ds_read_b64_tr_b16 v[118:119], v57 offset:2560\n\t" \
  "s_add_u32 m0, s47, 0\n\t" \
  "s_waitcnt lgkmcnt(14)\n\t" \
  "v_mfma_f32_32x32x16_bf16 v[176:191], v[100:103], v[80:83], v[176:191]\n\t" \
  "global_load_lds_dwordx4 v64, s[4:5] offset:0\n\t" \
  "v_add_u32 v143, s33, v143\n\t" \
  "s_lshl_b32 s33, s86, 5\n\t" \
  "s_add_u32 s33, s33, 158720\n\t" \
  "v_mfma_f32_32x32x16_bf16 v[224:239], v[100:103], v[84:87], v[224:239]\n\t" \
  "global_load_lds_dwordx4 v65, s[4:5] offset:1024\n\t" \
  "v_and_b32 v75, 28, v75\n\t" \
  "v_add_u32 v23, s33, v75\n\t" \
  "s_lshr_b32 s33, s95, 1\n\t" \
  "s_waitcnt lgkmcnt(11)\n\t" \
  "v_mfma_f32_32x32x16_bf16 v[240:255], v[88:91], v[104:107], v[240:255]\n\t" \
  "global_load_lds_dwordx4 v66, s[4:5] offset:2048\n\t" \
  "s_lshl_b32 s33, s33, 9\n\t" \
  "s_lshl_b32 s35, s88, 6\n\t" \
  "s_add_u32 s33, s33, s35\n\t" \
  "s_waitcnt lgkmcnt(0)\n\t" \
  "v_mfma_f32_32x32x16_bf16 v[144:159], v[120:123], v[108:111], v[144:159]\n\t" \
  "ds_read_b128 v[80:83], v50\n\t" \
  "ds_read_b64_tr_b16 v[92:93], v56 offset:4096\n\t" \
  "ds_read_b64_tr_b16 v[94:95], v56 offset:4608\n\t" \
  "s_mov_b32 s55, 0xb000\n\t" \
  "v_mfma_f32_32x32x16_bf16 v[192:207], v[120:123], v[112:115], v[192:207]\n\t" \
  "ds_read_b128 v[84:87], v50 offset:4096\n\t" \
  "ds_read_b64_tr_b16 v[96:97], v56 offset:12288\n\t" \
  "ds_read_b64_tr_b16 v[98:99], v56 offset:12800\n\t" \
  "v_mfma_f32_32x32x16_bf16 v[160:175], v[124:127], v[108:111], v[160:175]\n\t" \
  "ds_read_b64_tr_b16 v[100:101], v56 offset:20480\n\t" \
  "ds_read_b64_tr_b16 v[102:103], v56 offset:20992\n\t" \
  "v_mfma_f32_32x32x16_bf16 v[208:223], v[124:127], v[112:115], v[208:223]\n\t" \
  "ds_read_b128 v[104:107], v54\n\t" \
  "ds_read_b64_tr_b16 v[88:89], v57 offset:4096\n\t" \
  "ds_read_b64_tr_b16 v[90:91], v57 offset:4608\n\t" \
  "v_mfma_f32_32x32x16_bf16 v[176:191], v[128:131], v[108:111], v[176:191]\n\t" \
  "global_load_lds_dwordx4 v67, s[4:5] offset:3072\n\t" \
  "ds_read_b64 v[26:27], v24 offset:45056\n\t" \
  "ds_read_b64_tr_b16 v[32:33], v30 offset:45056\n\t" \
  "ds_read_b64_tr_b16 v[34:35], v30 offset:45568\n\t" \
  "s_and_b32 s35, s95, 1\n\t" \
  "s_lshl_b32 s35, s35, 5\n\t" \
  "s_add_u32 s33, s33, s35\n\t" \
  "s_add_u32 m0, s47, 4096\n\t" \
  "v_mfma_f32_32x32x16_bf16 v[224:239], v[128:131], v[112:115], v[224:239]\n\t" \
  "global_load_lds_dwordx4 v68, s[4:5] offset:0\n\t" \
  "s_lshl3_add_u32 s33, s86, s33\n\t" \
  "s_lshl_b32 s33, s33, 4\n\t" \
  "s_lshl1_add_u32 s33, s34, s33\n\t" \
  "s_add_u32 m0, s48, 0\n\t" \
  "v_mfma_f32_32x32x16_bf16 v[240:255], v[116:119], v[132:135], v[240:255]\n\t" \
  "global_load_lds_dwordx4 v69, s[6:7] offset:0\n\t" \
  "s_add_u32 s33, s33, 131072\n\t" \
  "v_add_u32 v22, s33, v0\n\t" \
  "s_lshr_b32 s97, s52, 1\n\t" \
  "s_waitcnt lgkmcnt(3)\n\t" \
  "v_mfma_f32_32x32x16_bf16 v[144:159], v[92:95], v[80:83], v[144:159]\n\t" \
  "ds_read_b128 v[108:111], v51\n\t" \
  "ds_read_b64_tr_b16 v[120:121], v56 offset:6144\n\t" \
  "ds_read_b64_tr_b16 v[122:123], v56 offset:6656\n\t" \
  "v_mfma_f32_32x32x16_bf16 v[192:207], v[92:95], v[84:87], v[192:207]\n\t" \
  "ds_read_b128 v[112:115], v51 offset:4096\n\t" \
  "ds_read_b64_tr_b16 v[124:125], v56 offset:14336\n\t" \
  "ds_read_b64_tr_b16 v[126:127], v56 offset:14848\n\t" \
  "v_mfma_f32_32x32x16_bf16 v[160:175], v[96:99], v[80:83], v[160:175]\n\t" \
  "ds_read_b64_tr_b16 v[128:129], v56 offset:22528\n\t" \
  "ds_read_b64_tr_b16 v[130:131], v56 offset:23040\n\t" \
  "v_mfma_f32_32x32x16_bf16 v[208:223], v[96:99], v[84:87], v[208:223]\n\t" \
  "ds_read_b128 v[132:135], v55\n\t" \
  "ds_read_b64_tr_b16 v[116:117], v57 offset:6144\n\t" \
  "ds_read_b64_tr_b16 v[118:119], v57 offset:6656\n\t" \
  "s_waitcnt lgkmcnt(11)\n\t" \
  "v_mfma_f32_16x16x32_bf16 v[36:39], v[40:43], v[32:35], v[36:39]\n\t" \
  "v_mfma_f32_32x32x16_bf16 v[176:191], v[100:103], v[80:83], v[176:191]\n\t" \
  "global_load_lds_dwordx4 v70, s[6:7] offset:1024\n\t" \
  "v_dot2c_f32_bf16 v28, s61, v26\n\t" \
  "v_dot2c_f32_bf16 v29, v26, v26\n\t" \
  "s_lshr_b32 s98, s89, 3\n\t" \
  "s_lshl1_add_u32 s96, s87, s90\n\t" \
  "s_lshl2_add_u32 s96, s96, s97\n\t" \
  "s_and_b32 s33, s88, 1\n\t" \
  "s_add_u32 m0, s48, 8192\n\t" \
  "v_mfma_f32_32x32x16_bf16 v[224:239], v[100:103], v[84:87], v[224:239]\n\t" \
  "global_load_lds_dwordx4 v71, s[6:7] offset:0\n\t" \
  "s_lshl1_add_u32 s99, s33, s91\n\t" \
  "s_lshl_b32 s33, s96, 7\n\t" \
  "s_add_u32 s33, s33, 196608\n\t" \
  "v_mfma_f32_32x32x16_bf16 v[240:255], v[88:91], v[104:107], v[240:255]\n\t" \
  "global_load_lds_dwordx4 v72, s[6:7] offset:1024\n\t" \
  "v_dot2c_f32_bf16 v28, s61, v27\n\t" \
  "v_dot2c_f32_bf16 v29, v27, v27\n\t" \
  "v_bfe_u32 v75, v0, 4, 3\n\t" \
  "v_lshl_add_u32 v13, v75, 3, s33\n\t" \
  "s_lshl3_add_u32 s33, s99, s33\n\t" \
  "s_waitcnt lgkmcnt(0)\n\t" \
  "s_add_u32 m0, s48, 16384\n\t" \
  "v_mfma_f32_32x32x16_bf16 v[144:159], v[120:123], v[108:111], v[144:159]\n\t" \
  "global_load_lds_dwordx4 v73, s[6:7] offset:0\n\t" \
  "v_add_u32 v48, s55, v48\n\t" \
  "v_add_u32 v49, s55, v49\n\t" \
  "v_add_u32 v50, s55, v50\n\t" \
  "v_add_u32 v51, s55, v51\n\t" \
  "v_add_u32 v52, s55, v52\n\t" \
  "v_mfma_f32_32x32x16_bf16 v[192:207], v[120:123], v[112:115], v[192:207]\n\t" \
  "global_load_lds_dwordx4 v74, s[6:7] offset:1024\n\t" \
  "v_add_u32 v53, s55, v53\n\t" \
  "v_add_u32 v54, s55, v54\n\t" \
  "v_add_u32 v55, s55, v55\n\t" \
  "v_add_u32 v56, s55, v56\n\t" \
  "v_add_u32 v57, s55, v57\n\t" \
  "v_mfma_f32_32x32x16_bf16 v[160:175], v[124:127], v[108:111], v[160:175]\n\t" \
  "s_waitcnt vmcnt(11)\n\t" \
  "s_waitcnt lgkmcnt(0)\n\t" \
  "s_barrier\n\t" \
  "ds_read_b128 v[80:83], v48\n\t" \
  "ds_read_b64_tr_b16 v[92:93], v56 offset:0\n\t" \
  "ds_read_b64_tr_b16 v[94:95], v56 offset:512\n\t" \
  "v_mfma_f32_32x32x16_bf16 v[208:223], v[124:127], v[112:115], v[208:223]\n\t" \
  "ds_read_b128 v[84:87], v48 offset:4096\n\t" \
  "ds_read_b64_tr_b16 v[96:97], v56 offset:8192\n\t" \
  "ds_read_b64_tr_b16 v[98:99], v56 offset:8704\n\t" \
  "v_mfma_f32_32x32x16_bf16 v[176:191], v[128:131], v[108:111], v[176:191]\n\t" \
  "ds_read_b64_tr_b16 v[100:101], v56 offset:16384\n\t" \
  "ds_read_b64_tr_b16 v[102:103], v56 offset:16896\n\t" \
  "v_mfma_f32_32x32x16_bf16 v[224:239], v[128:131], v[112:115], v[224:239]\n\t" \
  "ds_read_b128 v[104:107], v52\n\t" \
  "ds_read_b64_tr_b16 v[88:89], v57 offset:0\n\t" \
  "ds_read_b64_tr_b16 v[90:91], v57 offset:512\n\t" \
  "v_mfma_f32_32x32x16_bf16 v[240:255], v[116:119], v[132:135], v[240:255]\n\t" \
  "s_add_u32 s33, s33, 0\n\t" \
  "v_mov_b32 v12, s33\n\t" \
  "s_lshl2_add_u32 s26, s87, s98\n\t" \
  "s_waitcnt lgkmcnt(8)\n\t" \
  "v_mfma_f32_32x32x16_bf16 v[144:159], v[92:95], v[80:83], v[144:159]\n\t" \
  "ds_read_b128 v[108:111], v49\n\t" \
  "ds_read_b64_tr_b16 v[120:121], v56 offset:2048\n\t" \
  "ds_read_b64_tr_b16 v[122:123], v56 offset:2560\n\t" \
  "s_xor_b32 s57, s85, 4\n\t" \
  "s_lshl_b32 s58, s57, 7\n\t" \
  "s_add_u32 s4, s40, s58\n\t" \
  "s_addc_u32 s5, s41, 0\n\t" \
  "s_waitcnt lgkmcnt(10)\n\t" \
  "v_mfma_f32_32x32x16_bf16 v[192:207], v[92:95], v[84:87], v[192:207]\n\t" \
  "ds_read_b128 v[112:115], v49 offset:4096\n\t" \
  "ds_read_b64_tr_b16 v[124:125], v56 offset:10240\n\t" \
  "ds_read_b64_tr_b16 v[126:127], v56 offset:10752\n\t" \
  "s_mul_i32 s58, s57, 196608\n\t" \
  "s_add_u32 s6, s42, s58\n\t" \
  "s_addc_u32 s7, s43, 0\n\t" \
  "s_waitcnt lgkmcnt(11)\n\t" \
  "v_mfma_f32_32x32x16_bf16 v[160:175], v[96:99], v[80:83], v[160:175]\n\t" \
  "ds_read_b64_tr_b16 v[128:129], v56 offset:18432\n\t" \
  "ds_read_b64_tr_b16 v[130:131], v56 offset:18944\n\t" \
  "v_mfma_f32_32x32x16_bf16 v[208:223], v[96:99], v[84:87], v[208:223]\n\t" \
  "ds_read_b128 v[132:135], v53\n\t" \
  "ds_read_b64_tr_b16 v[116:117], v57 offset:2048\n\t" \
  "ds_read_b64_tr_b16 v[118:119], v57 offset:2560\n\t" \
  "s_add_u32 m0, s47, 45056\n\t" \
  "s_waitcnt lgkmcnt(14)\n\t" \
  "v_mfma_f32_32x32x16_bf16 v[176:191], v[100:103], v[80:83], v[176:191]\n\t" \
  "global_load_lds_dwordx4 v64, s[4:5] offset:0\n\t" \
  "s_lshl4_add_u32 s26, s26, s86\n\t" \
  "s_lshl2_add_u32 s27, s97, s26\n\t" \
  "s_lshl_b32 s27, s27, 6\n\t" \
  "v_mfma_f32_32x32x16_bf16 v[224:239], v[100:103], v[84:87], v[224:239]\n\t" \
  "global_load_lds_dwordx4 v65, s[4:5] offset:1024\n\t" \
  "s_add_u32 s27, s27, 10485760\n\t" \
  "s_lshl2_add_u32 s33, s84, s86\n\t" \
  "s_lshl_b32 s34, s33, 7\n\t" \
  "s_waitcnt lgkmcnt(11)\n\t" \
  "v_mfma_f32_32x32x16_bf16 v[240:255], v[88:91], v[104:107], v[240:255]\n\t" \
  "global_load_lds_dwordx4 v66, s[4:5] offset:2048\n\t" \
  "s_add_u32 s35, s34, 131072\n\t" \
  "s_min_u32 s35, s35, 184192\n\t" \
  "s_and_b32 s34, s34, 0xffff\n\t" \
  "s_waitcnt lgkmcnt(0)\n\t" \
  "v_mfma_f32_32x32x16_bf16 v[144:159], v[120:123], v[108:111], v[144:159]\n\t" \
  "ds_read_b128 v[80:83], v50\n\t" \
  "ds_read_b64_tr_b16 v[92:93], v56 offset:4096\n\t" \
  "ds_read_b64_tr_b16 v[94:95], v56 offset:4608\n\t" \
  "s_mov_b32 s55, 0xfffea000\n\t" \
  "v_mfma_f32_32x32x16_bf16 v[192:207], v[120:123], v[112:115], v[192:207]\n\t" \
  "ds_read_b128 v[84:87], v50 offset:4096\n\t" \
  "ds_read_b64_tr_b16 v[96:97], v56 offset:12288\n\t" \
  "ds_read_b64_tr_b16 v[98:99], v56 offset:12800\n\t" \
  "v_mfma_f32_32x32x16_bf16 v[160:175], v[124:127], v[108:111], v[160:175]\n\t" \
  "ds_read_b64_tr_b16 v[100:101], v56 offset:20480\n\t" \
  "ds_read_b64_tr_b16 v[102:103], v56 offset:20992\n\t" \
  "v_mfma_f32_32x32x16_bf16 v[208:223], v[124:127], v[112:115], v[208:223]\n\t" \
  "ds_read_b128 v[104:107], v54\n\t" \
  "ds_read_b64_tr_b16 v[88:89], v57 offset:4096\n\t" \
  "ds_read_b64_tr_b16 v[90:91], v57 offset:4608\n\t" \
  "v_mfma_f32_32x32x16_bf16 v[176:191], v[128:131], v[108:111], v[176:191]\n\t" \
  "global_load_lds_dwordx4 v67, s[4:5] offset:3072\n\t" \
  "ds_read_b64 v[26:27], v25 offset:0\n\t" \
  "ds_read_b64_tr_b16 v[32:33], v31 offset:0\n\t" \
  "ds_read_b64_tr_b16 v[34:35], v31 offset:512\n\t" \
  "s_add_u32 s34, s34, 8192\n\t" \
  "v_mov_b32 v58, s27\n\t" \
  "v_writelane_b32 v58, s34, 1\n\t" \
  "s_add_u32 m0, s47, 49152\n\t" \
  "v_mfma_f32_32x32x16_bf16 v[224:239], v[128:131], v[112:115], v[224:239]\n\t" \
  "global_load_lds_dwordx4 v68, s[4:5] offset:0\n\t" \
  "v_writelane_b32 v58, s35, 2\n\t" \
  "v_cmp_eq_u32 vcc, 48, v0\n\t" \
  "s_nop 1\n\t" \
  "v_cndmask_b32 v58, v58, v13, vcc\n\t" \
  "global_load_dword v141, v58, s[44:45] sc1\n\t" \
  "s_add_u32 m0, s48, 45056\n\t" \
  "v_mfma_f32_32x32x16_bf16 v[240:255], v[116:119], v[132:135], v[240:255]\n\t" \
  "global_load_lds_dwordx4 v69, s[6:7] offset:0\n\t" \
  "s_mul_i32 s33, s97, 458752\n\t" \
  "s_add_u32 s66, s66, s33\n\t" \
  "s_addc_u32 s67, s67, 0\n\t" \
  "s_mov_b32 s19, 0\n\t" \
  "s_waitcnt lgkmcnt(3)\n\t" \
  "v_mfma_f32_32x32x16_bf16 v[144:159], v[92:95], v[80:83], v[144:159]\n\t" \
  "ds_read_b128 v[108:111], v51\n\t" \
  "ds_read_b64_tr_b16 v[120:121], v56 offset:6144\n\t" \
  "ds_read_b64_tr_b16 v[122:123], v56 offset:6656\n\t" \
  "v_mfma_f32_32x32x16_bf16 v[192:207], v[92:95], v[84:87], v[192:207]\n\t" \
  "ds_read_b128 v[112:115], v51 offset:4096\n\t" \
  "ds_read_b64_tr_b16 v[124:125], v56 offset:14336\n\t" \
  "ds_read_b64_tr_b16 v[126:127], v56 offset:14848\n\t" \
  "v_mfma_f32_32x32x16_bf16 v[160:175], v[96:99], v[80:83], v[160:175]\n\t" \
  "ds_read_b64_tr_b16 v[128:129], v56 offset:22528\n\t" \
  "ds_read_b64_tr_b16 v[130:131], v56 offset:23040\n\t" \
  "v_mfma_f32_32x32x16_bf16 v[208:223], v[96:99], v[84:87], v[208:223]\n\t" \
  "ds_read_b128 v[132:135], v55\n\t" \
  "ds_read_b64_tr_b16 v[116:117], v57 offset:6144\n\t" \
  "ds_read_b64_tr_b16 v[118:119], v57 offset:6656\n\t" \
  "s_waitcnt lgkmcnt(11)\n\t" \
  "v_mfma_f32_16x16x32_bf16 v[36:39], v[40:43], v[32:35], v[36:39]\n\t" \
  "v_mfma_f32_32x32x16_bf16 v[176:191], v[100:103], v[80:83], v[176:191]\n\t" \
  "global_load_lds_dwordx4 v70, s[6:7] offset:1024\n\t" \
  "v_dot2c_f32_bf16 v28, s61, v26\n\t" \
  "v_dot2c_f32_bf16 v29, v26, v26\n\t" \
  "s_xor_b32 s33, s19, 1\n\t" \
  "s_mul_i32 s33, s33, 229376\n\t" \
  "s_add_u32 s20, s66, s33\n\t" \
  "s_addc_u32 s21, s67, 0\n\t" \
  "s_add_u32 m0, s48, 53248\n\t" \
  "v_mfma_f32_32x32x16_bf16 v[224:239], v[100:103], v[84:87], v[224:239]\n\t" \
  "global_load_lds_dwordx4 v71, s[6:7] offset:0\n\t" \
  "s_mul_i32 s33, s19, 229376\n\t" \
  "s_add_u32 s66, s66, s33\n\t" \
  "s_addc_u32 s67, s67, 0\n\t" \
  "s_lshl_b32 s33, s97, 17\n\t" \
  "v_mfma_f32_32x32x16_bf16 v[240:255], v[88:91], v[104:107], v[240:255]\n\t" \
  "global_load_lds_dwordx4 v72, s[6:7] offset:1024\n\t" \
  "v_dot2c_f32_bf16 v28, s61, v27\n\t" \
  "v_dot2c_f32_bf16 v29, v27, v27\n\t" \
  "s_add_u32 s62, s62, s33\n\t" \
  "s_addc_u32 s63, s63, 0\n\t" \
  "v_lshrrev_b32 v75, 8, v0\n\t" \
  "v_and_b32 v78, 0xf0, v0\n\t" \
  "s_waitcnt lgkmcnt(0)\n\t" \
  "s_add_u32 m0, s48, 61440\n\t" \
  "v_mfma_f32_32x32x16_bf16 v[144:159], v[120:123], v[108:111], v[144:159]\n\t" \
  "global_load_lds_dwordx4 v73, s[6:7] offset:0\n\t" \
  "v_add_u32 v48, s55, v48\n\t" \
  "v_add_u32 v49, s55, v49\n\t" \
  "v_add_u32 v50, s55, v50\n\t" \
  "v_add_u32 v51, s55, v51\n\t" \
  "v_add_u32 v52, s55, v52\n\t" \
  "v_mfma_f32_32x32x16_bf16 v[192:207], v[120:123], v[112:115], v[192:207]\n\t" \
  "global_load_lds_dwordx4 v74, s[6:7] offset:1024\n\t" \
  "v_add_u32 v53, s55, v53\n\t" \
  "v_add_u32 v54, s55, v54\n\t" \
  "v_add_u32 v55, s55, v55\n\t" \
  "v_add_u32 v56, s55, v56\n\t" \
  "v_add_u32 v57, s55, v57\n\t" \
  "v_mfma_f32_32x32x16_bf16 v[160:175], v[124:127], v[108:111], v[160:175]\n\t" \
  "s_waitcnt vmcnt(12)\n\t" \
  "s_waitcnt lgkmcnt(0)\n\t" \
  "s_barrier\n\t" \
  "ds_read_b128 v[80:83], v48\n\t" \
  "ds_read_b64_tr_b16 v[92:93], v56 offset:0\n\t" \
  "ds_read_b64_tr_b16 v[94:95], v56 offset:512\n\t" \
  "v_mfma_f32_32x32x16_bf16 v[208:223], v[124:127], v[112:115], v[208:223]\n\t" \
  "ds_read_b128 v[84:87], v48 offset:4096\n\t" \
  "ds_read_b64_tr_b16 v[96:97], v56 offset:8192\n\t" \
  "ds_read_b64_tr_b16 v[98:99], v56 offset:8704\n\t" \
  "v_mfma_f32_32x32x16_bf16 v[176:191], v[128:131], v[108:111], v[176:191]\n\t" \
  "ds_read_b64_tr_b16 v[100:101], v56 offset:16384\n\t" \
  "ds_read_b64_tr_b16 v[102:103], v56 offset:16896\n\t" \
  "v_mfma_f32_32x32x16_bf16 v[224:239], v[128:131], v[112:115], v[224:239]\n\t" \
  "ds_read_b128 v[104:107], v52\n\t" \
  "ds_read_b64_tr_b16 v[88:89], v57 offset:0\n\t" \
  "ds_read_b64_tr_b16 v[90:91], v57 offset:512\n\t" \
  "v_mfma_f32_32x32x16_bf16 v[240:255], v[116:119], v[132:135], v[240:255]\n\t" \
  "v_lshlrev_b32 v79, 6, v75\n\t" \
  "v_xor_b32 v78, v78, v79\n\t" \
  "v_lshl_add_u32 v78, v75, 10, v78\n\t" \
  "s_waitcnt lgkmcnt(8)\n\t" \
  "v_mfma_f32_32x32x16_bf16 v[144:159], v[92:95], v[80:83], v[144:159]\n\t" \
  "ds_read_b128 v[108:111], v49\n\t" \
  "ds_read_b64_tr_b16 v[120:121], v56 offset:2048\n\t" \
  "ds_read_b64_tr_b16 v[122:123], v56 offset:2560\n\t" \
  "s_xor_b32 s57, s85, 5\n\t" \
  "s_lshl_b32 s58, s57, 7\n\t" \
  "s_add_u32 s4, s40, s58\n\t" \
  "s_addc_u32 s5, s41, 0\n\t" \
  "s_waitcnt lgkmcnt(10)\n\t" \
  "v_mfma_f32_32x32x16_bf16 v[192:207], v[92:95], v[84:87], v[192:207]\n\t" \
  "ds_read_b128 v[112:115], v49 offset:4096\n\t" \
  "ds_read_b64_tr_b16 v[124:125], v56 offset:10240\n\t" \
  "ds_read_b64_tr_b16 v[126:127], v56 offset:10752\n\t" \
  "s_mul_i32 s58, s57, 196608\n\t" \
  "s_add_u32 s6, s42, s58\n\t" \
  "s_addc_u32 s7, s43, 0\n\t" \
  "s_waitcnt lgkmcnt(11)\n\t" \
  "v_mfma_f32_32x32x16_bf16 v[160:175], v[96:99], v[80:83], v[160:175]\n\t" \
  "ds_read_b64_tr_b16 v[128:129], v56 offset:18432\n\t" \
  "ds_read_b64_tr_b16 v[130:131], v56 offset:18944\n\t" \
  "v_mfma_f32_32x32x16_bf16 v[208:223], v[96:99], v[84:87], v[208:223]\n\t" \
  "ds_read_b128 v[132:135], v53\n\t" \
  "ds_read_b64_tr_b16 v[116:117], v57 offset:2048\n\t" \
  "ds_read_b64_tr_b16 v[118:119], v57 offset:2560\n\t" \
  "s_add_u32 m0, s47, 90112\n\t" \
  "s_waitcnt lgkmcnt(14)\n\t" \
  "v_mfma_f32_32x32x16_bf16 v[176:191], v[100:103], v[80:83], v[176:191]\n\t" \
  "global_load_lds_dwordx4 v64, s[4:5] offset:0\n\t" \
  "s_lshl_b32 s33, s86, 13\n\t" \
  "s_lshl_b32 s34, s98, 8\n\t" \
  "s_add_u32 s33, s33, s34\n\t" \
  "v_mfma_f32_32x32x16_bf16 v[224:239], v[100:103], v[84:87], v[224:239]\n\t" \
  "global_load_lds_dwordx4 v65, s[4:5] offset:1024\n\t" \
  "v_add_u32 v76, s33, v78\n\t" \
  "v_add_u32 v77, 3072, v76\n\t" \
  "s_lshl_b32 s83, s86, 11\n\t" \
  "s_waitcnt lgkmcnt(11)\n\t" \
  "v_mfma_f32_32x32x16_bf16 v[240:255], v[88:91], v[104:107], v[240:255]\n\t" \
  "global_load_lds_dwordx4 v66, s[4:5] offset:2048\n\t" \
  "s_lshl_b32 s33, s86, 8\n\t" \
  "s_add_u32 s33, s33, 157696\n\t" \
  "v_lshrrev_b32 v75, 2, v0\n\t" \
  "v_add_u32 v63, s33, v75\n\t" \
  "s_waitcnt lgkmcnt(0)\n\t" \
  "v_mfma_f32_32x32x16_bf16 v[144:159], v[120:123], v[108:111], v[144:159]\n\t" \
  "ds_read_b128 v[80:83], v50\n\t" \
  "ds_read_b64_tr_b16 v[92:93], v56 offset:4096\n\t" \
  "ds_read_b64_tr_b16 v[94:95], v56 offset:4608\n\t" \
  "s_mov_b32 s55, 0xb000\n\t" \
  "v_mfma_f32_32x32x16_bf16 v[192:207], v[120:123], v[112:115], v[192:207]\n\t" \
  "ds_read_b128 v[84:87], v50 offset:4096\n\t" \
  "ds_read_b64_tr_b16 v[96:97], v56 offset:12288\n\t" \
  "ds_read_b64_tr_b16 v[98:99], v56 offset:12800\n\t" \
  "v_mfma_f32_32x32x16_bf16 v[160:175], v[124:127], v[108:111], v[160:175]\n\t" \
  "ds_read_b64_tr_b16 v[100:101], v56 offset:20480\n\t" \
  "ds_read_b64_tr_b16 v[102:103], v56 offset:20992\n\t" \
  "v_mfma_f32_32x32x16_bf16 v[208:223], v[124:127], v[112:115], v[208:223]\n\t" \
  "ds_read_b128 v[104:107], v54\n\t" \
  "ds_read_b64_tr_b16 v[88:89], v57 offset:4096\n\t" \
  "ds_read_b64_tr_b16 v[90:91], v57 offset:4608\n\t" \
  "v_mfma_f32_32x32x16_bf16 v[176:191], v[128:131], v[108:111], v[176:191]\n\t" \
  "global_load_lds_dwordx4 v67, s[4:5] offset:3072\n\t" \
  "ds_read_b64 v[26:27], v24 offset:0\n\t" \
  "ds_read_b64_tr_b16 v[32:33], v30 offset:0\n\t" \
  "ds_read_b64_tr_b16 v[34:35], v30 offset:512\n\t" \
  "s_lshl1_add_u32 s33, s87, s90\n\t" \
  "s_lshl_b32 s33, s33, 11\n\t" \
  "s_add_u32 s33, s33, 8192\n\t" \
  "s_add_u32 m0, s47, 94208\n\t" \
  "v_mfma_f32_32x32x16_bf16 v[224:239], v[128:131], v[112:115], v[224:239]\n\t" \
  "global_load_lds_dwordx4 v68, s[4:5] offset:0\n\t" \
  "v_and_b32 v75, 0x1f0, v0\n\t" \
  "v_lshl_add_u32 v60, v75, 1, s33\n\t" \
  "v_add_u32 v61, 1024, v60\n\t" \
  "s_add_u32 m0, s48, 90112\n\t" \
  "v_mfma_f32_32x32x16_bf16 v[240:255], v[116:119], v[132:135], v[240:255]\n\t" \
  "global_load_lds_dwordx4 v69, s[6:7] offset:0\n\t" \
  "s_lshl_b32 s33, s87, 12\n\t" \
  "s_sub_u32 s33, s33, 1024\n\t" \
  "v_lshl_add_u32 v59, v75, 1, s33\n\t" \
  "s_waitcnt lgkmcnt(3)\n\t" \
  "v_mfma_f32_32x32x16_bf16 v[144:159], v[92:95], v[80:83], v[144:159]\n\t" \
  "ds_read_b128 v[108:111], v51\n\t" \
  "ds_read_b64_tr_b16 v[120:121], v56 offset:6144\n\t" \
  "ds_read_b64_tr_b16 v[122:123], v56 offset:6656\n\t" \
  "v_mfma_f32_32x32x16_bf16 v[192:207], v[92:95], v[84:87], v[192:207]\n\t" \
  "ds_read_b128 v[112:115], v51 offset:4096\n\t" \
  "ds_read_b64_tr_b16 v[124:125], v56 offset:14336\n\t" \
  "ds_read_b64_tr_b16 v[126:127], v56 offset:14848\n\t" \
  "v_mfma_f32_32x32x16_bf16 v[160:175], v[96:99], v[80:83], v[160:175]\n\t" \
  "ds_read_b64_tr_b16 v[128:129], v56 offset:22528\n\t" \
  "ds_read_b64_tr_b16 v[130:131], v56 offset:23040\n\t" \
  "v_mfma_f32_32x32x16_bf16 v[208:223], v[96:99], v[84:87], v[208:223]\n\t" \
  "ds_read_b128 v[132:135], v55\n\t" \
  "ds_read_b64_tr_b16 v[116:117], v57 offset:6144\n\t" \
  "ds_read_b64_tr_b16 v[118:119], v57 offset:6656\n\t" \
  "s_waitcnt lgkmcnt(11)\n\t" \
  "v_mfma_f32_16x16x32_bf16 v[36:39], v[40:43], v[32:35], v[36:39]\n\t" \
  "v_mfma_f32_32x32x16_bf16 v[176:191], v[100:103], v[80:83], v[176:191]\n\t" \
  "global_load_lds_dwordx4 v70, s[6:7] offset:1024\n\t" \
  "v_dot2c_f32_bf16 v28, s61, v26\n\t" \
  "v_dot2c_f32_bf16 v29, v26, v26\n\t" \
  "v_max_i32 v59, 0, v59\n\t" \
  "v_add_u32 v59, 8192, v59\n\t" \
  "s_min_u32 s33, s86, 2\n\t" \
  "s_add_u32 m0, s48, 98304\n\t" \
  "v_mfma_f32_32x32x16_bf16 v[224:239], v[100:103], v[84:87], v[224:239]\n\t" \
  "global_load_lds_dwordx4 v71, s[6:7] offset:0\n\t" \
  "s_lshl_b32 s33, s33, 3\n\t" \
  "s_add_u32 s33, s33, s88\n\t" \
  "s_lshl_b32 s33, s33, 10\n\t" \
  "v_mfma_f32_32x32x16_bf16 v[240:255], v[88:91], v[104:107], v[240:255]\n\t" \
  "global_load_lds_dwordx4 v72, s[6:7] offset:1024\n\t" \
  "v_dot2c_f32_bf16 v28, s61, v27\n\t" \
  "v_dot2c_f32_bf16 v29, v27, v27\n\t" \
  "s_add_u32 s33, s33, 131072\n\t" \
  "v_add_u32 v62, s33, v0\n\t" \
  "v_readfirstlane_b32 s68, v138\n\t" \
  "v_readfirstlane_b32 s69, v139\n\t" \
  "s_waitcnt lgkmcnt(0)\n\t" \
  "s_add_u32 m0, s48, 106496\n\t" \
  "v_mfma_f32_32x32x16_bf16 v[144:159], v[120:123], v[108:111], v[144:159]\n\t" \
  "global_load_lds_dwordx4 v73, s[6:7] offset:0\n\t" \
  "v_add_u32 v48, s55, v48\n\t" \
  "v_add_u32 v49, s55, v49\n\t" \
  "v_add_u32 v50, s55, v50\n\t" \
  "v_add_u32 v51, s55, v51\n\t" \
  "v_add_u32 v52, s55, v52\n\t" \
  "v_mfma_f32_32x32x16_bf16 v[192:207], v[120:123], v[112:115], v[192:207]\n\t" \
  "global_load_lds_dwordx4 v74, s[6:7] offset:1024\n\t" \
  "v_add_u32 v53, s55, v53\n\t" \
  "v_add_u32 v54, s55, v54\n\t" \
  "v_add_u32 v55, s55, v55\n\t" \
  "v_add_u32 v56, s55, v56\n\t" \
  "v_add_u32 v57, s55, v57\n\t" \
  "ds_write2_b32 v143, v36, v37 offset1:16\n\t" \
  "v_mfma_f32_32x32x16_bf16 v[160:175], v[124:127], v[108:111], v[160:175]\n\t" \
  "s_waitcnt vmcnt(11)\n\t" \
  "s_waitcnt lgkmcnt(0)\n\t" \
  "s_barrier\n\t" \
  "ds_read_b128 v[80:83], v48\n\t" \
  "ds_read_b64_tr_b16 v[92:93], v56 offset:0\n\t" \
  "ds_read_b64_tr_b16 v[94:95], v56 offset:512\n\t" \
  "v_mfma_f32_32x32x16_bf16 v[208:223], v[124:127], v[112:115], v[208:223]\n\t" \
  "ds_read_b128 v[84:87], v48 offset:4096\n\t" \
  "ds_read_b64_tr_b16 v[96:97], v56 offset:8192\n\t" \
  "ds_read_b64_tr_b16 v[98:99], v56 offset:8704\n\t" \
  "v_mfma_f32_32x32x16_bf16 v[176:191], v[128:131], v[108:111], v[176:191]\n\t" \
  "ds_read_b64_tr_b16 v[100:101], v56 offset:16384\n\t" \
  "ds_read_b64_tr_b16 v[102:103], v56 offset:16896\n\t" \
  "v_mfma_f32_32x32x16_bf16 v[224:239], v[128:131], v[112:115], v[224:239]\n\t" \
  "ds_read_b128 v[104:107], v52\n\t" \
  "ds_read_b64_tr_b16 v[88:89], v57 offset:0\n\t" \
  "ds_read_b64_tr_b16 v[90:91], v57 offset:512\n\t" \
  "v_mfma_f32_32x32x16_bf16 v[240:255], v[116:119], v[132:135], v[240:255]\n\t" \
  "s_add_u32 s14, s68, 1\n\t" \
  "s_addc_u32 s15, s69, 0\n\t" \
  "s_mov_b32 s49, s14\n\t" \
  "s_lshl_b64 s[12:13], s[14:15], 3\n\t" \
  "s_or_b32 s68, s12, 4\n\t" \
  "s_mov_b32 s69, s13\n\t" \
  "v_mov_b32 v138, s12\n\t" \
  "v_mov_b32 v139, s13\n\t" \
  "global_load_dword v141, v[136:137], off\n\t" \
  "s_waitcnt lgkmcnt(8)\n\t" \
  "v_mfma_f32_32x32x16_bf16 v[144:159], v[92:95], v[80:83], v[144:159]\n\t" \
  "ds_read_b128 v[108:111], v49\n\t" \
  "ds_read_b64_tr_b16 v[120:121], v56 offset:2048\n\t" \
  "ds_read_b64_tr_b16 v[122:123], v56 offset:2560\n\t" \
  "s_xor_b32 s57, s85, 6\n\t" \
  "s_lshl_b32 s58, s57, 7\n\t" \
  "s_add_u32 s4, s40, s58\n\t" \
  "s_addc_u32 s5, s41, 0\n\t" \
  "ds_read_b32 v32, v23\n\t" \
  "ds_read_b32 v33, v23 offset:128\n\t" \
  "ds_read_b32 v34, v23 offset:256\n\t" \
  "ds_read_b32 v35, v23 offset:384\n\t" \
  "s_waitcnt lgkmcnt(14)\n\t" \
  "v_mfma_f32_32x32x16_bf16 v[192:207], v[92:95], v[84:87], v[192:207]\n\t" \
  "ds_read_b128 v[112:115], v49 offset:4096\n\t" \
  "ds_read_b64_tr_b16 v[124:125], v56 offset:10240\n\t" \
  "ds_read_b64_tr_b16 v[126:127], v56 offset:10752\n\t" \
  "s_mul_i32 s58, s57, 196608\n\t" \
  "s_add_u32 s6, s42, s58\n\t" \
  "s_addc_u32 s7, s43, 0\n\t" \
  "v_mov_b32 v26, s49\n\t" \
  "s_mov_b32 s60, 0xaaaaaaaa\n\t" \
  "s_mov_b32 s61, 0xaaaaaaaa\n\t" \
  "s_waitcnt lgkmcnt(14)\n\t" \
  "v_mfma_f32_32x32x16_bf16 v[160:175], v[96:99], v[80:83], v[160:175]\n\t" \
  "ds_read_b64_tr_b16 v[128:129], v56 offset:18432\n\t" \
  "ds_read_b64_tr_b16 v[130:131], v56 offset:18944\n\t" \
  "v_add_f32_dpp v28, v28, v28 quad_perm:[1,0,3,2] row_mask:0xf bank_mask:0xf\n\t" \
  "v_add_f32_dpp v29, v29, v29 quad_perm:[1,0,3,2] row_mask:0xf bank_mask:0xf\n\t" \
  "v_mfma_f32_32x32x16_bf16 v[208:223], v[96:99], v[84:87], v[208:223]\n\t" \
  "ds_read_b128 v[132:135], v53\n\t" \
  "ds_read_b64_tr_b16 v[116:117], v57 offset:2048\n\t" \
  "ds_read_b64_tr_b16 v[118:119], v57 offset:2560\n\t" \
  "v_add_f32_dpp v28, v28, v28 quad_perm:[2,3,0,1] row_mask:0xf bank_mask:0xf\n\t" \
  "v_add_f32_dpp v29, v29, v29 quad_perm:[2,3,0,1] row_mask:0xf bank_mask:0xf\n\t" \
  "s_add_u32 m0, s47, 0\n\t" \
  "s_waitcnt lgkmcnt(14)\n\t" \
  "v_mfma_f32_32x32x16_bf16 v[176:191], v[100:103], v[80:83], v[176:191]\n\t" \
  "global_load_lds_dwordx4 v64, s[4:5] offset:0\n\t" \
  "s_waitcnt lgkmcnt(10)\n\t" \
  "v_add_f32 v32, v32, v33\n\t" \
  "s_waitcnt lgkmcnt(8)\n\t" \
  "v_add_f32 v34, v34, v35\n\t" \
  "v_mov_b32 v33, s49\n\t" \
  "v_add_f32 v32, v32, v34\n\t" \
  "v_mfma_f32_32x32x16_bf16 v[224:239], v[100:103], v[84:87], v[224:239]\n\t" \
  "global_load_lds_dwordx4 v65, s[4:5] offset:1024\n\t" \
  "v_mfma_f32_32x32x16_bf16 v[240:255], v[88:91], v[104:107], v[240:255]\n\t" \
  "global_load_lds_dwordx4 v66, s[4:5] offset:2048\n\t" \
  "s_mov_b64 exec, 0xff\n\t" \
  "global_store_dwordx2 v22, v[32:33], s[44:45] sc1\n\t" \
  "s_mov_b64 exec, -1\n\t" \
  "s_waitcnt lgkmcnt(0)\n\t" \
  "v_mfma_f32_32x32x16_bf16 v[144:159], v[120:123], v[108:111], v[144:159]\n\t" \
  "ds_read_b128 v[80:83], v50\n\t" \
  "ds_read_b64_tr_b16 v[92:93], v56 offset:4096\n\t" \
  "ds_read_b64_tr_b16 v[94:95], v56 offset:4608\n\t" \
  "s_mov_b32 s55, 0xb000\n\t" \
  "v_mfma_f32_32x32x16_bf16 v[192:207], v[120:123], v[112:115], v[192:207]\n\t" \
  "ds_read_b128 v[84:87], v50 offset:4096\n\t" \
  "ds_read_b64_tr_b16 v[96:97], v56 offset:12288\n\t" \
  "ds_read_b64_tr_b16 v[98:99], v56 offset:12800\n\t" \
  "v_mfma_f32_32x32x16_bf16 v[160:175], v[124:127], v[108:111], v[160:175]\n\t" \
  "ds_read_b64_tr_b16 v[100:101], v56 offset:20480\n\t" \
  "ds_read_b64_tr_b16 v[102:103], v56 offset:20992\n\t" \
  "v_add_f32_dpp v28, v28, v28 row_half_mirror row_mask:0xf bank_mask:0xf\n\t" \
  "v_add_f32_dpp v29, v29, v29 row_half_mirror row_mask:0xf bank_mask:0xf\n\t" \
  "v_mfma_f32_32x32x16_bf16 v[208:223], v[124:127], v[112:115], v[208:223]\n\t" \
  "ds_read_b128 v[104:107], v54\n\t" \
  "ds_read_b64_tr_b16 v[88:89], v57 offset:4096\n\t" \
  "ds_read_b64_tr_b16 v[90:91], v57 offset:4608\n\t" \
  "v_add_f32_dpp v28, v28, v28 row_mirror row_mask:0xf bank_mask:0xf\n\t" \
  "v_add_f32_dpp v29, v29, v29 row_mirror row_mask:0xf bank_mask:0xf\n\t" \
  "v_mfma_f32_32x32x16_bf16 v[176:191], v[128:131], v[108:111], v[176:191]\n\t" \
  "global_load_lds_dwordx4 v67, s[4:5] offset:3072\n\t" \
  "v_cndmask_b32 v27, v28, v29, s[60:61]\n\t" \
  "s_mov_b32 s60, 0x00030003\n\t" \
  "s_mov_b32 s61, 0x00030003\n\t" \
  "s_add_u32 m0, s47, 4096\n\t" \
  "v_mfma_f32_32x32x16_bf16 v[224:239], v[128:131], v[112:115], v[224:239]\n\t" \
  "global_load_lds_dwordx4 v68, s[4:5] offset:0\n\t" \
  "s_mov_b64 exec, s[60:61]\n\t" \
  "global_store_dwordx2 v142, v[26:27], s[44:45] sc1\n\t" \
  "s_mov_b64 exec, -1\n\t" \
  "s_add_u32 m0, s48, 0\n\t" \
  "v_mfma_f32_32x32x16_bf16 v[240:255], v[116:119], v[132:135], v[240:255]\n\t" \
  "global_load_lds_dwordx4 v69, s[6:7] offset:0\n\t" \
  "s_waitcnt lgkmcnt(0)\n\t" \
  "v_mfma_f32_32x32x16_bf16 v[144:159], v[92:95], v[80:83], v[144:159]\n\t" \
  "ds_read_b128 v[108:111], v51\n\t" \
  "ds_read_b64_tr_b16 v[120:121], v56 offset:6144\n\t" \
  "ds_read_b64_tr_b16 v[122:123], v56 offset:6656\n\t" \
  "v_mfma_f32_32x32x16_bf16 v[192:207], v[92:95], v[84:87], v[192:207]\n\t" \
  "ds_read_b128 v[112:115], v51 offset:4096\n\t" \
  "ds_read_b64_tr_b16 v[124:125], v56 offset:14336\n\t" \
  "ds_read_b64_tr_b16 v[126:127], v56 offset:14848\n\t" \
  "v_mfma_f32_32x32x16_bf16 v[160:175], v[96:99], v[80:83], v[160:175]\n\t" \
  "ds_read_b64_tr_b16 v[128:129], v56 offset:22528\n\t" \
  "ds_read_b64_tr_b16 v[130:131], v56 offset:23040\n\t" \
  "v_mfma_f32_32x32x16_bf16 v[208:223], v[96:99], v[84:87], v[208:223]\n\t" \
  "ds_read_b128 v[132:135], v55\n\t" \
  "ds_read_b64_tr_b16 v[116:117], v57 offset:6144\n\t" \
  "ds_read_b64_tr_b16 v[118:119], v57 offset:6656\n\t" \
  "v_mfma_f32_32x32x16_bf16 v[176:191], v[100:103], v[80:83], v[176:191]\n\t" \
  "global_load_lds_dwordx4 v70, s[6:7] offset:1024\n\t" \
  "v_bfe_u32 v78, v0, 6, 2\n\t" \
  "v_lshrrev_b32 v79, 9, v0\n\t" \
  "v_lshl_add_u32 v75, v79, 2, v78\n\t" \
  "v_bfe_u32 v58, v0, 4, 2\n\t" \
  "v_lshlrev_b32 v58, 3, v58\n\t" \
  "s_add_u32 m0, s48, 8192\n\t" \
  "v_mfma_f32_32x32x16_bf16 v[224:239], v[100:103], v[84:87], v[224:239]\n\t" \
  "global_load_lds_dwordx4 v71, s[6:7] offset:0\n\t" \
  "v_lshl_add_u32 v75, v75, 8, v58\n\t" \
  "v_bfe_u32 v58, v0, 8, 1\n\t" \
  "v_lshl_add_u32 v75, v58, 5, v75\n\t" \
  "s_add_u32 s33, s97, 1\n\t" \
  "s_and_b32 s33, s33, 3\n\t" \
  "v_mfma_f32_32x32x16_bf16 v[240:255], v[88:91], v[104:107], v[240:255]\n\t" \
  "global_load_lds_dwordx4 v72, s[6:7] offset:1024\n\t" \
  "v_xor_b32 v58, s33, v78\n\t" \
  "v_lshl_add_u32 v10, v58, 6, v75\n\t" \
  "v_add_u32 v1, 90112, v10\n\t" \
  "v_add_u32 v5, 24576, v10\n\t" \
  "s_add_u32 s33, s97, 2\n\t" \
  "s_waitcnt lgkmcnt(0)\n\t" \
  "s_add_u32 m0, s48, 16384\n\t" \
  "v_mfma_f32_32x32x16_bf16 v[144:159], v[120:123], v[108:111], v[144:159]\n\t" \
  "global_load_lds_dwordx4 v73, s[6:7] offset:0\n\t" \
  "v_add_u32 v48, s55, v48\n\t" \
  "v_add_u32 v49, s55, v49\n\t" \
  "v_add_u32 v50, s55, v50\n\t" \
  "v_add_u32 v51, s55, v51\n\t" \
  "v_add_u32 v52, s55, v52\n\t" \
  "v_mfma_f32_32x32x16_bf16 v[192:207], v[120:123], v[112:115], v[192:207]\n\t" \
  "global_load_lds_dwordx4 v74, s[6:7] offset:1024\n\t" \
  "v_add_u32 v53, s55, v53\n\t" \
  "v_add_u32 v54, s55, v54\n\t" \
  "v_add_u32 v55, s55, v55\n\t" \
  "v_add_u32 v56, s55, v56\n\t" \
  "v_add_u32 v57, s55, v57\n\t" \
  "v_mfma_f32_32x32x16_bf16 v[160:175], v[124:127], v[108:111], v[160:175]\n\t" \
  "s_waitcnt vmcnt(14)\n\t" \
  "s_waitcnt lgkmcnt(0)\n\t" \
  "s_barrier\n\t" \
  "ds_read_b128 v[80:83], v48\n\t" \
  "ds_read_b64_tr_b16 v[92:93], v56 offset:0\n\t" \
  "ds_read_b64_tr_b16 v[94:95], v56 offset:512\n\t" \
  "v_mfma_f32_32x32x16_bf16 v[208:223], v[124:127], v[112:115], v[208:223]\n\t" \
  "ds_read_b128 v[84:87], v48 offset:4096\n\t" \
  "ds_read_b64_tr_b16 v[96:97], v56 offset:8192\n\t" \
  "ds_read_b64_tr_b16 v[98:99], v56 offset:8704\n\t" \
  "v_mfma_f32_32x32x16_bf16 v[176:191], v[128:131], v[108:111], v[176:191]\n\t" \
  "ds_read_b64_tr_b16 v[100:101], v56 offset:16384\n\t" \
  "ds_read_b64_tr_b16 v[102:103], v56 offset:16896\n\t" \
  "v_mfma_f32_32x32x16_bf16 v[224:239], v[128:131], v[112:115], v[224:239]\n\t" \
  "ds_read_b128 v[104:107], v52\n\t" \
  "ds_read_b64_tr_b16 v[88:89], v57 offset:0\n\t" \
  "ds_read_b64_tr_b16 v[90:91], v57 offset:512\n\t" \
  "v_mfma_f32_32x32x16_bf16 v[240:255], v[116:119], v[132:135], v[240:255]\n\t" \
  "s_and_b32 s33, s33, 3\n\t" \
  "v_xor_b32 v58, s33, v78\n\t" \
  "v_lshl_add_u32 v11, v58, 6, v75\n\t" \
  "v_add_u32 v2, 90112, v11\n\t" \
  "v_add_u32 v6, 24576, v11\n\t" \
  "s_waitcnt lgkmcnt(8)\n\t" \
  "v_mfma_f32_32x32x16_bf16 v[144:159], v[92:95], v[80:83], v[144:159]\n\t" \
  "ds_read_b128 v[108:111], v49\n\t" \
  "ds_read_b64_tr_b16 v[120:121], v56 offset:2048\n\t" \
  "ds_read_b64_tr_b16 v[122:123], v56 offset:2560\n\t" \
  "s_xor_b32 s57, s85, 7\n\t" \
  "s_lshl_b32 s58, s57, 7\n\t" \
  "s_add_u32 s4, s40, s58\n\t" \
  "s_addc_u32 s5, s41, 0\n\t" \
  "s_waitcnt lgkmcnt(10)\n\t" \
  "v_mfma_f32_32x32x16_bf16 v[192:207], v[92:95], v[84:87], v[192:207]\n\t" \
  "ds_read_b128 v[112:115], v49 offset:4096\n\t" \
  "ds_read_b64_tr_b16 v[124:125], v56 offset:10240\n\t" \
  "ds_read_b64_tr_b16 v[126:127], v56 offset:10752\n\t" \
  "s_mul_i32 s58, s57, 196608\n\t" \
  "s_add_u32 s6, s42, s58\n\t" \
  "s_addc_u32 s7, s43, 0\n\t" \
  "s_waitcnt lgkmcnt(11)\n\t" \
  "v_mfma_f32_32x32x16_bf16 v[160:175], v[96:99], v[80:83], v[160:175]\n\t" \
  "ds_read_b64_tr_b16 v[128:129], v56 offset:18432\n\t" \
  "ds_read_b64_tr_b16 v[130:131], v56 offset:18944\n\t" \
  "v_mfma_f32_32x32x16_bf16 v[208:223], v[96:99], v[84:87], v[208:223]\n\t" \
  "ds_read_b128 v[132:135], v53\n\t" \
  "ds_read_b64_tr_b16 v[116:117], v57 offset:2048\n\t" \
  "ds_read_b64_tr_b16 v[118:119], v57 offset:2560\n\t" \
  "s_add_u32 m0, s47, 45056\n\t" \
  "s_waitcnt lgkmcnt(14)\n\t" \
  "v_mfma_f32_32x32x16_bf16 v[176:191], v[100:103], v[80:83], v[176:191]\n\t" \
  "global_load_lds_dwordx4 v64, s[4:5] offset:0\n\t" \
  "s_add_u32 s33, s97, 3\n\t" \
  "s_and_b32 s33, s33, 3\n\t" \
  "v_xor_b32 v58, s33, v78\n\t" \
  "v_lshl_add_u32 v15, v58, 6, v75\n\t" \
  "v_mfma_f32_32x32x16_bf16 v[224:239], v[100:103], v[84:87], v[224:239]\n\t" \
  "global_load_lds_dwordx4 v65, s[4:5] offset:1024\n\t" \
  "v_add_u32 v3, 90112, v15\n\t" \
  "v_add_u32 v7, 24576, v15\n\t" \
  "s_add_u32 s33, s97, 4\n\t" \
  "s_and_b32 s33, s33, 3\n\t" \
  "v_xor_b32 v58, s33, v78\n\t" \
  "s_waitcnt lgkmcnt(11)\n\t" \
  "v_mfma_f32_32x32x16_bf16 v[240:255], v[88:91], v[104:107], v[240:255]\n\t" \
  "global_load_lds_dwordx4 v66, s[4:5] offset:2048\n\t" \
  "v_lshl_add_u32 v16, v58, 6, v75\n\t" \
  "v_add_u32 v4, 90112, v16\n\t" \
  "v_add_u32 v8, 24576, v16\n\t" \
  "v_lshrrev_b32 v79, 9, v0\n\t" \
  "s_lshl2_add_u32 s34, s87, s86\n\t" \
  "s_waitcnt lgkmcnt(0)\n\t" \
  "v_mfma_f32_32x32x16_bf16 v[144:159], v[120:123], v[108:111], v[144:159]\n\t" \
  "ds_read_b128 v[80:83], v50\n\t" \
  "ds_read_b64_tr_b16 v[92:93], v56 offset:4096\n\t" \
  "ds_read_b64_tr_b16 v[94:95], v56 offset:4608\n\t" \
  "s_mov_b32 s55, 0xfffea000\n\t" \
  "v_mfma_f32_32x32x16_bf16 v[192:207], v[120:123], v[112:115], v[192:207]\n\t" \
  "ds_read_b128 v[84:87], v50 offset:4096\n\t" \
  "ds_read_b64_tr_b16 v[96:97], v56 offset:12288\n\t" \
  "ds_read_b64_tr_b16 v[98:99], v56 offset:12800\n\t" \
  "v_mfma_f32_32x32x16_bf16 v[160:175], v[124:127], v[108:111], v[160:175]\n\t" \
  "ds_read_b64_tr_b16 v[100:101], v56 offset:20480\n\t" \
  "ds_read_b64_tr_b16 v[102:103], v56 offset:20992\n\t" \
  "v_mfma_f32_32x32x16_bf16 v[208:223], v[124:127], v[112:115], v[208:223]\n\t" \
  "ds_read_b128 v[104:107], v54\n\t" \
  "ds_read_b64_tr_b16 v[88:89], v57 offset:4096\n\t" \
  "ds_read_b64_tr_b16 v[90:91], v57 offset:4608\n\t" \
  "v_mfma_f32_32x32x16_bf16 v[176:191], v[128:131], v[108:111], v[176:191]\n\t" \
  "global_load_lds_dwordx4 v67, s[4:5] offset:3072\n\t" \
  "s_lshl2_add_u32 s33, s34, s97\n\t" \
  "s_mul_i32 s33, s33, 36864\n\t" \
  "s_add_u32 s33, s33, 262144\n\t" \
  "s_add_u32 s74, s44, s33\n\t" \
  "s_addc_u32 s75, s45, 0\n\t" \
  "s_lshl_b32 s33, s96, 1\n\t" \
  "s_add_u32 m0, s47, 49152\n\t" \
  "v_mfma_f32_32x32x16_bf16 v[224:239], v[128:131], v[112:115], v[224:239]\n\t" \
  "global_load_lds_dwordx4 v68, s[4:5] offset:0\n\t" \
  "s_sub_u32 s33, s33, s97\n\t" \
  "s_mul_i32 s33, s33, 36864\n\t" \
  "s_add_u32 s33, s33, 262144\n\t" \
  "s_lshl_b32 s34, s99, 11\n\t" \
  "s_add_u32 s33, s33, s34\n\t" \
  "s_add_u32 m0, s48, 45056\n\t" \
  "v_mfma_f32_32x32x16_bf16 v[240:255], v[116:119], v[132:135], v[240:255]\n\t" \
  "global_load_lds_dwordx4 v69, s[6:7] offset:0\n\t" \
  "s_add_u32 s33, s33, 28672\n\t" \
  "s_add_u32 s70, s44, s33\n\t" \
  "s_addc_u32 s71, s45, 0\n\t" \
  "s_add_u32 s72, s70, 147456\n\t" \
  "s_addc_u32 s73, s71, 0\n\t" \
  "s_lshl_b32 s33, s91, 7\n\t" \
  "s_waitcnt lgkmcnt(0)\n\t" \
  "v_mfma_f32_32x32x16_bf16 v[144:159], v[92:95], v[80:83], v[144:159]\n\t" \
  "ds_read_b128 v[108:111], v51\n\t" \
  "ds_read_b64_tr_b16 v[120:121], v56 offset:6144\n\t" \
  "ds_read_b64_tr_b16 v[122:123], v56 offset:6656\n\t" \
  "v_mfma_f32_32x32x16_bf16 v[192:207], v[92:95], v[84:87], v[192:207]\n\t" \
  "ds_read_b128 v[112:115], v51 offset:4096\n\t" \
  "ds_read_b64_tr_b16 v[124:125], v56 offset:14336\n\t" \
  "ds_read_b64_tr_b16 v[126:127], v56 offset:14848\n\t" \
  "v_mfma_f32_32x32x16_bf16 v[160:175], v[96:99], v[80:83], v[160:175]\n\t" \
  "ds_read_b64_tr_b16 v[128:129], v56 offset:22528\n\t" \
  "ds_read_b64_tr_b16 v[130:131], v56 offset:23040\n\t" \
  "v_mfma_f32_32x32x16_bf16 v[208:223], v[96:99], v[84:87], v[208:223]\n\t" \
  "ds_read_b128 v[132:135], v55\n\t" \
  "ds_read_b64_tr_b16 v[116:117], v57 offset:6144\n\t" \
  "ds_read_b64_tr_b16 v[118:119], v57 offset:6656\n\t" \
  "v_mfma_f32_32x32x16_bf16 v[176:191], v[100:103], v[80:83], v[176:191]\n\t" \
  "global_load_lds_dwordx4 v70, s[6:7] offset:1024\n\t" \
  "s_add_u32 s33, s33, 157696\n\t" \
  "v_lshl_add_u32 v17, v79, 4, s33\n\t" \
  "v_mul_u32_u24 v79, 2304, v79\n\t" \
  "v_and_b32 v75, 0x1f0, v0\n\t" \
  "v_add_u32 v75, v75, v79\n\t" \
  "s_add_u32 m0, s48, 53248\n\t" \
  "v_mfma_f32_32x32x16_bf16 v[224:239], v[100:103], v[84:87], v[224:239]\n\t" \
  "global_load_lds_dwordx4 v71, s[6:7] offset:0\n\t" \
  "s_lshl_b32 s33, s90, 10\n\t" \
  "s_mul_i32 s34, s91, 9216\n\t" \
  "s_add_u32 s35, s33, s34\n\t" \
  "s_add_u32 s36, s35, 37120\n\t" \
  "v_add_u32 v18, s36, v75\n\t" \
  "v_mfma_f32_32x32x16_bf16 v[240:255], v[88:91], v[104:107], v[240:255]\n\t" \
  "global_load_lds_dwordx4 v72, s[6:7] offset:1024\n\t" \
  "v_add_u32 v75, v75, v79\n\t" \
  "s_add_u32 s36, s35, s34\n\t" \
  "s_add_u32 s36, s36, 256\n\t" \
  "v_add_u32 v19, s36, v75\n\t" \
  "s_add_u32 s35, s35, 36896\n\t" \
  "s_waitcnt lgkmcnt(0)\n\t" \
  "s_add_u32 m0, s48, 61440\n\t" \
  "v_mfma_f32_32x32x16_bf16 v[144:159], v[120:123], v[108:111], v[144:159]\n\t" \
  "global_load_lds_dwordx4 v73, s[6:7] offset:0\n\t" \
  "v_add_u32 v48, s55, v48\n\t" \
  "v_add_u32 v49, s55, v49\n\t" \
  "v_add_u32 v50, s55, v50\n\t" \
  "v_add_u32 v51, s55, v51\n\t" \
  "v_add_u32 v52, s55, v52\n\t" \
  "v_mfma_f32_32x32x16_bf16 v[192:207], v[120:123], v[112:115], v[192:207]\n\t" \
  "global_load_lds_dwordx4 v74, s[6:7] offset:1024\n\t" \
  "v_add_u32 v53, s55, v53\n\t" \
  "v_add_u32 v54, s55, v54\n\t" \
  "v_add_u32 v55, s55, v55\n\t" \
  "v_add_u32 v56, s55, v56\n\t" \
  "v_add_u32 v57, s55, v57\n\t" \
  "v_mfma_f32_32x32x16_bf16 v[160:175], v[124:127], v[108:111], v[160:175]\n\t" \
  "s_waitcnt vmcnt(11)\n\t" \
  "s_waitcnt lgkmcnt(0)\n\t" \
  "s_barrier\n\t" \
  "ds_read_b128 v[80:83], v48\n\t" \
  "ds_read_b64_tr_b16 v[92:93], v56 offset:0\n\t" \
  "ds_read_b64_tr_b16 v[94:95], v56 offset:512\n\t" \
  "v_mfma_f32_32x32x16_bf16 v[208:223], v[124:127], v[112:115], v[208:223]\n\t" \
  "ds_read_b128 v[84:87], v48 offset:4096\n\t" \
  "ds_read_b64_tr_b16 v[96:97], v56 offset:8192\n\t" \
  "ds_read_b64_tr_b16 v[98:99], v56 offset:8704\n\t" \
  "v_mfma_f32_32x32x16_bf16 v[176:191], v[128:131], v[108:111], v[176:191]\n\t" \
  "ds_read_b64_tr_b16 v[100:101], v56 offset:16384\n\t" \
  "ds_read_b64_tr_b16 v[102:103], v56 offset:16896\n\t" \
  "v_mfma_f32_32x32x16_bf16 v[224:239], v[128:131], v[112:115], v[224:239]\n\t" \
  "ds_read_b128 v[104:107], v52\n\t" \
  "ds_read_b64_tr_b16 v[88:89], v57 offset:0\n\t" \
  "ds_read_b64_tr_b16 v[90:91], v57 offset:512\n\t" \
  "s_mov_b32 s56, 0x10000\n\t" \
  "v_mov_b32 v64, v76\n\t" \
  "v_mov_b32 v65, v77\n\t" \
  "v_mfma_f32_32x32x16_bf16 v[240:255], v[116:119], v[132:135], v[240:255]\n\t" \
  "v_add_u32 v20, s35, v75\n\t" \
  "v_lshl_add_u32 v75, v79, 1, v75\n\t" \
  "s_sub_u32 s36, s36, 224\n\t" \
  "v_add_u32 v21, s36, v75\n\t" \
  "global_load_dwordx4 v[24:27], v60, s[44:45] sc1\n\t" \
  "global_load_dwordx4 v[28:31], v60, s[44:45] offset:16 sc1\n\t" \
  "global_load_dwordx4 v[32:35], v61, s[44:45] sc1\n\t" \
  "global_load_dwordx4 v[36:39], v61, s[44:45] offset:16 sc1\n\t" \
  "global_load_dwordx4 v[40:43], v59, s[44:45] sc1\n\t" \
  "global_load_dwordx4 v[44:47], v59, s[44:45] offset:16 sc1\n\t" \
  "global_load_dwordx4 v[76:79], v62, s[44:45] sc1\n\t" \
  "s_waitcnt lgkmcnt(8)\n\t" \
  "v_mfma_f32_32x32x16_bf16 v[144:159], v[92:95], v[80:83], v[144:159]\n\t" \
  "ds_read_b128 v[108:111], v49\n\t" \
  "ds_read_b64_tr_b16 v[120:121], v56 offset:2048\n\t" \
  "ds_read_b64_tr_b16 v[122:123], v56 offset:2560\n\t" \
  "s_waitcnt lgkmcnt(10)\n\t" \
  "v_mfma_f32_32x32x16_bf16 v[192:207], v[92:95], v[84:87], v[192:207]\n\t" \
  "ds_read_b128 v[112:115], v49 offset:4096\n\t" \
  "ds_read_b64_tr_b16 v[124:125], v56 offset:10240\n\t" \
  "ds_read_b64_tr_b16 v[126:127], v56 offset:10752\n\t" \
  "s_waitcnt lgkmcnt(11)\n\t" \
  "v_mfma_f32_32x32x16_bf16 v[160:175], v[96:99], v[80:83], v[160:175]\n\t" \
  "ds_read_b64_tr_b16 v[128:129], v56 offset:18432\n\t" \
  "ds_read_b64_tr_b16 v[130:131], v56 offset:18944\n\t" \
  "v_mfma_f32_32x32x16_bf16 v[208:223], v[96:99], v[84:87], v[208:223]\n\t" \
  "ds_read_b128 v[132:135], v53\n\t" \
  "ds_read_b64_tr_b16 v[116:117], v57 offset:2048\n\t" \
  "ds_read_b64_tr_b16 v[118:119], v57 offset:2560\n\t" \
  "s_waitcnt lgkmcnt(14)\n\t" \
  "v_mfma_f32_32x32x16_bf16 v[176:191], v[100:103], v[80:83], v[176:191]\n\t" \
  "s_add_u32 s92, s66, 0\n\t" \
  "s_addc_u32 s93, s67, 0\n\t" \
  "s_add_u32 m0, s83, 90112\n\t" \
  "s_nop 0\n\t" \
  "global_load_lds_dwordx4 v64, s[92:93]\n\t" \
  "v_mfma_f32_32x32x16_bf16 v[224:239], v[100:103], v[84:87], v[224:239]\n\t" \
  "global_load_lds_dwordx4 v65, s[92:93] offset:1024\n\t" \
  "s_waitcnt lgkmcnt(11)\n\t" \
  "v_mfma_f32_32x32x16_bf16 v[240:255], v[88:91], v[104:107], v[240:255]\n\t" \
  "s_add_u32 s94, s66, 98304\n\t" \
  "s_addc_u32 s95, s67, 0\n\t" \
  "s_add_u32 m0, s83, 98304\n\t" \
  "s_nop 0\n\t" \
  "global_load_lds_dwordx4 v64, s[94:95]\n\t" \
  "s_waitcnt lgkmcnt(0)\n\t" \
  "v_mfma_f32_32x32x16_bf16 v[144:159], v[120:123], v[108:111], v[144:159]\n\t" \
  "ds_read_b128 v[80:83], v50\n\t" \
  "ds_read_b64_tr_b16 v[92:93], v56 offset:4096\n\t" \
  "ds_read_b64_tr_b16 v[94:95], v56 offset:4608\n\t" \
  "s_mov_b32 s55, 0xb000\n\t" \
  "v_mfma_f32_32x32x16_bf16 v[192:207], v[120:123], v[112:115], v[192:207]\n\t" \
  "ds_read_b128 v[84:87], v50 offset:4096\n\t" \
  "ds_read_b64_tr_b16 v[96:97], v56 offset:12288\n\t" \
  "ds_read_b64_tr_b16 v[98:99], v56 offset:12800\n\t" \
  "v_mfma_f32_32x32x16_bf16 v[160:175], v[124:127], v[108:111], v[160:175]\n\t" \
  "ds_read_b64_tr_b16 v[100:101], v56 offset:20480\n\t" \
  "ds_read_b64_tr_b16 v[102:103], v56 offset:20992\n\t" \
  "v_mfma_f32_32x32x16_bf16 v[208:223], v[124:127], v[112:115], v[208:223]\n\t" \
  "ds_read_b128 v[104:107], v54\n\t" \
  "ds_read_b64_tr_b16 v[88:89], v57 offset:4096\n\t" \
  "ds_read_b64_tr_b16 v[90:91], v57 offset:4608\n\t" \
  "v_mfma_f32_32x32x16_bf16 v[176:191], v[128:131], v[108:111], v[176:191]\n\t" \
  "global_load_lds_dwordx4 v65, s[94:95] offset:1024\n\t" \
  "v_mfma_f32_32x32x16_bf16 v[224:239], v[128:131], v[112:115], v[224:239]\n\t" \
  "s_add_u32 s92, s66, 32768\n\t" \
  "s_addc_u32 s93, s67, 0\n\t" \
  "s_add_u32 m0, s83, 106496\n\t" \
  "s_nop 0\n\t" \
  "global_load_lds_dwordx4 v64, s[92:93]\n\t" \
  "v_mfma_f32_32x32x16_bf16 v[240:255], v[116:119], v[132:135], v[240:255]\n\t" \
  "global_load_lds_dwordx4 v65, s[92:93] offset:1024\n\t" \
  "s_waitcnt lgkmcnt(0)\n\t" \
  "v_mfma_f32_32x32x16_bf16 v[144:159], v[92:95], v[80:83], v[144:159]\n\t" \
  "ds_read_b128 v[108:111], v51\n\t" \
  "ds_read_b64_tr_b16 v[120:121], v56 offset:6144\n\t" \
  "ds_read_b64_tr_b16 v[122:123], v56 offset:6656\n\t" \
  "v_mfma_f32_32x32x16_bf16 v[192:207], v[92:95], v[84:87], v[192:207]\n\t" \
  "ds_read_b128 v[112:115], v51 offset:4096\n\t" \
  "ds_read_b64_tr_b16 v[124:125], v56 offset:14336\n\t" \
  "ds_read_b64_tr_b16 v[126:127], v56 offset:14848\n\t" \
  "v_mfma_f32_32x32x16_bf16 v[160:175], v[96:99], v[80:83], v[160:175]\n\t" \
  "ds_read_b64_tr_b16 v[128:129], v56 offset:22528\n\t" \
  "ds_read_b64_tr_b16 v[130:131], v56 offset:23040\n\t" \
  "v_mfma_f32_32x32x16_bf16 v[208:223], v[96:99], v[84:87], v[208:223]\n\t" \
  "ds_read_b128 v[132:135], v55\n\t" \
  "ds_read_b64_tr_b16 v[116:117], v57 offset:6144\n\t" \
  "ds_read_b64_tr_b16 v[118:119], v57 offset:6656\n\t" \
  "v_mfma_f32_32x32x16_bf16 v[176:191], v[100:103], v[80:83], v[176:191]\n\t" \
  "v_mfma_f32_32x32x16_bf16 v[224:239], v[100:103], v[84:87], v[224:239]\n\t" \
  "s_add_u32 s94, s66, 131072\n\t" \
  "s_addc_u32 s95, s67, 0\n\t" \
  "s_add_u32 m0, s83, 114688\n\t" \
  "s_nop 0\n\t" \
  "global_load_lds_dwordx4 v64, s[94:95]\n\t" \
  "v_mfma_f32_32x32x16_bf16 v[240:255], v[88:91], v[104:107], v[240:255]\n\t" \
  "global_load_lds_dwordx4 v65, s[94:95] offset:1024\n\t" \
  "s_waitcnt lgkmcnt(0)\n\t" \
  "v_mfma_f32_32x32x16_bf16 v[144:159], v[120:123], v[108:111], v[144:159]\n\t" \
  "v_add_u32 v48, s55, v48\n\t" \
  "v_add_u32 v49, s55, v49\n\t" \
  "v_add_u32 v50, s55, v50\n\t" \
  "v_add_u32 v51, s55, v51\n\t" \
  "v_add_u32 v52, s55, v52\n\t" \
  "s_add_u32 s92, s20, 0\n\t" \
  "s_addc_u32 s93, s21, 0\n\t" \
  "s_add_u32 m0, s83, 122880\n\t" \
  "s_nop 0\n\t" \
  "global_load_lds_dwordx4 v64, s[92:93]\n\t" \
  "v_mfma_f32_32x32x16_bf16 v[192:207], v[120:123], v[112:115], v[192:207]\n\t" \
  "v_add_u32 v53, s55, v53\n\t" \
  "v_add_u32 v54, s55, v54\n\t" \
  "v_add_u32 v55, s55, v55\n\t" \
  "v_add_u32 v56, s55, v56\n\t" \
  "v_add_u32 v57, s55, v57\n\t" \
  "global_load_lds_dwordx4 v65, s[92:93] offset:1024\n\t" \
  "v_mfma_f32_32x32x16_bf16 v[160:175], v[124:127], v[108:111], v[160:175]\n\t" \
  "s_waitcnt vmcnt(17)\n\t" \
  "s_waitcnt lgkmcnt(0)\n\t" \
  "s_barrier\n\t" \
  "ds_read_b128 v[80:83], v48\n\t" \
  "ds_read_b64_tr_b16 v[92:93], v56 offset:0\n\t" \
  "ds_read_b64_tr_b16 v[94:95], v56 offset:512\n\t" \
  "v_mfma_f32_32x32x16_bf16 v[208:223], v[124:127], v[112:115], v[208:223]\n\t" \
  "ds_read_b128 v[84:87], v48 offset:4096\n\t" \
  "ds_read_b64_tr_b16 v[96:97], v56 offset:8192\n\t" \
  "ds_read_b64_tr_b16 v[98:99], v56 offset:8704\n\t" \
  "v_mfma_f32_32x32x16_bf16 v[176:191], v[128:131], v[108:111], v[176:191]\n\t" \
  "ds_read_b64_tr_b16 v[100:101], v56 offset:16384\n\t" \
  "ds_read_b64_tr_b16 v[102:103], v56 offset:16896\n\t" \
  "v_mfma_f32_32x32x16_bf16 v[224:239], v[128:131], v[112:115], v[224:239]\n\t" \
  "ds_read_b128 v[104:107], v52\n\t" \
  "ds_read_b64_tr_b16 v[88:89], v57 offset:0\n\t" \
  "ds_read_b64_tr_b16 v[90:91], v57 offset:512\n\t" \
  "v_mfma_f32_32x32x16_bf16 v[240:255], v[116:119], v[132:135], v[240:255]\n\t" \
  "s_waitcnt lgkmcnt(8)\n\t" \
  "v_mfma_f32_32x32x16_bf16 v[144:159], v[92:95], v[80:83], v[144:159]\n\t" \
  "ds_read_b128 v[108:111], v49\n\t" \
  "ds_read_b64_tr_b16 v[120:121], v56 offset:2048\n\t" \
  "ds_read_b64_tr_b16 v[122:123], v56 offset:2560\n\t" \
  "s_waitcnt lgkmcnt(10)\n\t" \
  "v_mfma_f32_32x32x16_bf16 v[192:207], v[92:95], v[84:87], v[192:207]\n\t" \
  "ds_read_b128 v[112:115], v49 offset:4096\n\t" \
  "ds_read_b64_tr_b16 v[124:125], v56 offset:10240\n\t" \
  "ds_read_b64_tr_b16 v[126:127], v56 offset:10752\n\t" \
  "s_waitcnt lgkmcnt(11)\n\t" \
  "v_mfma_f32_32x32x16_bf16 v[160:175], v[96:99], v[80:83], v[160:175]\n\t" \
  "ds_read_b64_tr_b16 v[128:129], v56 offset:18432\n\t" \
  "ds_read_b64_tr_b16 v[130:131], v56 offset:18944\n\t" \
  "v_mfma_f32_32x32x16_bf16 v[208:223], v[96:99], v[84:87], v[208:223]\n\t" \
  "ds_read_b128 v[132:135], v53\n\t" \
  "ds_read_b64_tr_b16 v[116:117], v57 offset:2048\n\t" \
  "ds_read_b64_tr_b16 v[118:119], v57 offset:2560\n\t" \
  "s_waitcnt lgkmcnt(14)\n\t" \
  "v_mfma_f32_32x32x16_bf16 v[176:191], v[100:103], v[80:83], v[176:191]\n\t" \
  "s_waitcnt vmcnt(10)\n\t" \
  "KCA%=:\n\t" \
  "v_and_b32 v58, v24, v26\n\t" \
  "v_or_b32 v75, v24, v26\n\t" \
  "v_bitop3_b32 v58, v58, v28, v30 bitop3:0x80\n\t" \
  "v_or3_b32 v75, v75, v28, v30\n\t" \
  "v_bitop3_b32 v58, v58, v32, v34 bitop3:0x80\n\t" \
  "v_or3_b32 v75, v75, v32, v34\n\t" \
  "v_bitop3_b32 v58, v58, v36, v38 bitop3:0x80\n\t" \
  "v_or3_b32 v75, v75, v36, v38\n\t" \
  "v_bitop3_b32 v58, v58, v40, v42 bitop3:0x80\n\t" \
  "v_or3_b32 v75, v75, v40, v42\n\t" \
  "v_bitop3_b32 v58, v58, v44, v46 bitop3:0x80\n\t" \
  "v_or3_b32 v75, v75, v44, v46\n\t" \
  "v_bitop3_b32 v58, v58, v77, v79 bitop3:0x80\n\t" \
  "v_or3_b32 v75, v75, v77, v79\n\t" \
  "v_cmp_ne_u32 vcc, s49, v75\n\t" \
  "v_cmp_ne_u32 s[58:59], s49, v58\n\t" \
  "s_nop 1\n\t" \
  "s_or_b64 vcc, vcc, s[58:59]\n\t" \
  "s_cbranch_scc1 KRA%=\n\t" \
  "KOA%=:\n\t" \
  "v_add_f32 v76, v76, v78\n\t" \
  "v_mov_b32 v136, 0x3727c5ac\n\t" \
  "ds_write_b32 v63, v76\n\t" \
  "v_mfma_f32_32x32x16_bf16 v[224:239], v[100:103], v[84:87], v[224:239]\n\t" \
  "s_add_u32 s94, s20, 98304\n\t" \
  "s_addc_u32 s95, s21, 0\n\t" \
  "s_add_u32 m0, s83, 131072\n\t" \
  "s_nop 0\n\t" \
  "global_load_lds_dwordx4 v64, s[94:95]\n\t" \
  "s_waitcnt lgkmcnt(12)\n\t" \
  "v_mfma_f32_32x32x16_bf16 v[240:255], v[88:91], v[104:107], v[240:255]\n\t" \
  "v_add_f32 v58, v25, v29\n\t" \
  "v_add_f32 v59, v27, v31\n\t" \
  "v_mul_f32 v30, 0xbb000000, v58\n\t" \
  "v_fmamk_f32 v59, v59, 0x3b000000, v136\n\t" \
  "s_waitcnt lgkmcnt(1)\n\t" \
  "v_mfma_f32_32x32x16_bf16 v[144:159], v[120:123], v[108:111], v[144:159]\n\t" \
  "ds_read_b128 v[80:83], v50\n\t" \
  "ds_read_b64_tr_b16 v[92:93], v56 offset:4096\n\t" \
  "ds_read_b64_tr_b16 v[94:95], v56 offset:4608\n\t" \
  "s_mov_b32 s55, 0xb000\n\t" \
  "v_mfma_f32_32x32x16_bf16 v[192:207], v[120:123], v[112:115], v[192:207]\n\t" \
  "ds_read_b128 v[84:87], v50 offset:4096\n\t" \
  "ds_read_b64_tr_b16 v[96:97], v56 offset:12288\n\t" \
  "ds_read_b64_tr_b16 v[98:99], v56 offset:12800\n\t" \
  "v_mfma_f32_32x32x16_bf16 v[160:175], v[124:127], v[108:111], v[160:175]\n\t" \
  "ds_read_b64_tr_b16 v[100:101], v56 offset:20480\n\t" \
  "ds_read_b64_tr_b16 v[102:103], v56 offset:20992\n\t" \
  "v_mfma_f32_32x32x16_bf16 v[208:223], v[124:127], v[112:115], v[208:223]\n\t" \
  "ds_read_b128 v[104:107], v54\n\t" \
  "ds_read_b64_tr_b16 v[88:89], v57 offset:4096\n\t" \
  "ds_read_b64_tr_b16 v[90:91], v57 offset:4608\n\t" \
  "v_mfma_f32_32x32x16_bf16 v[176:191], v[128:131], v[108:111], v[176:191]\n\t" \
  "v_fma_f32 v59, -v30, v30, v59\n\t" \
  "v_max_f32 v59, v59, v136\n\t" \
  "global_load_lds_dwordx4 v65, s[94:95] offset:1024\n\t" \
  "v_mfma_f32_32x32x16_bf16 v[224:239], v[128:131], v[112:115], v[224:239]\n\t" \
  "v_rsq_f32 v29, v59\n\t" \
  "s_nop 0\n\t" \
  "v_mul_f32 v31, v30, v29\n\t" \
  "s_add_u32 s92, s20, 32768\n\t" \
  "s_addc_u32 s93, s21, 0\n\t" \
  "s_add_u32 m0, s83, 139264\n\t" \
  "s_nop 0\n\t" \
  "global_load_lds_dwordx4 v64, s[92:93]\n\t" \
  "v_mfma_f32_32x32x16_bf16 v[240:255], v[116:119], v[132:135], v[240:255]\n\t" \
  "v_add_f32 v58, v33, v37\n\t" \
  "v_add_f32 v59, v35, v39\n\t" \
  "global_load_lds_dwordx4 v65, s[92:93] offset:1024\n\t" \
  "s_waitcnt lgkmcnt(0)\n\t" \
  "v_mfma_f32_32x32x16_bf16 v[144:159], v[92:95], v[80:83], v[144:159]\n\t" \
  "ds_read_b128 v[108:111], v51\n\t" \
  "ds_read_b64_tr_b16 v[120:121], v56 offset:6144\n\t" \
  "ds_read_b64_tr_b16 v[122:123], v56 offset:6656\n\t" \
  "v_mfma_f32_32x32x16_bf16 v[192:207], v[92:95], v[84:87], v[192:207]\n\t" \
  "ds_read_b128 v[112:115], v51 offset:4096\n\t" \
  "ds_read_b64_tr_b16 v[124:125], v56 offset:14336\n\t" \
  "ds_read_b64_tr_b16 v[126:127], v56 offset:14848\n\t" \
  "v_mfma_f32_32x32x16_bf16 v[160:175], v[96:99], v[80:83], v[160:175]\n\t" \
  "ds_read_b64_tr_b16 v[128:129], v56 offset:22528\n\t" \
  "ds_read_b64_tr_b16 v[130:131], v56 offset:23040\n\t" \
  "v_mfma_f32_32x32x16_bf16 v[208:223], v[96:99], v[84:87], v[208:223]\n\t" \
  "ds_read_b128 v[132:135], v55\n\t" \
  "ds_read_b64_tr_b16 v[116:117], v57 offset:6144\n\t" \
  "ds_read_b64_tr_b16 v[118:119], v57 offset:6656\n\t" \
  "v_mfma_f32_32x32x16_bf16 v[176:191], v[100:103], v[80:83], v[176:191]\n\t" \
  "v_mul_f32 v34, 0xbb000000, v58\n\t" \
  "v_fmamk_f32 v59, v59, 0x3b000000, v136\n\t" \
  "v_fma_f32 v59, -v34, v34, v59\n\t" \
  "v_max_f32 v59, v59, v136\n\t" \
  "s_add_u32 s94, s20, 131072\n\t" \
  "s_addc_u32 s95, s21, 0\n\t" \
  "s_add_u32 m0, s83, 147456\n\t" \
  "s_nop 0\n\t" \
  "global_load_lds_dwordx4 v64, s[94:95]\n\t" \
  "v_mfma_f32_32x32x16_bf16 v[224:239], v[100:103], v[84:87], v[224:239]\n\t" \
  "v_rsq_f32 v33, v59\n\t" \
  "global_load_lds_dwordx4 v65, s[94:95] offset:1024\n\t" \
  "v_mfma_f32_32x32x16_bf16 v[240:255], v[88:91], v[104:107], v[240:255]\n\t" \
  "v_mul_f32 v35, v34, v33\n\t" \
  "v_add_f32 v58, v41, v45\n\t" \
  "v_add_f32 v59, v43, v47\n\t" \
  "s_waitcnt lgkmcnt(0)\n\t" \
  "v_mfma_f32_32x32x16_bf16 v[144:159], v[120:123], v[108:111], v[144:159]\n\t" \
  "s_waitcnt lgkmcnt(0)\n\t" \
  "s_barrier\n\t" \
  "v_mul_f32 v38, 0xbb000000, v58\n\t" \
  "v_fmamk_f32 v59, v59, 0x3b000000, v136\n\t" \
  "v_mfma_f32_32x32x16_bf16 v[192:207], v[120:123], v[112:115], v[192:207]\n\t" \
  "ds_read_b128 v[84:87], v17 offset:256\n\t" \
  "ds_read_b128 v[88:91], v17 offset:288\n\t" \
  "ds_read_b128 v[92:95], v17 offset:320\n\t" \
  "ds_read_b128 v[96:99], v17 offset:352\n\t" \
  "v_mfma_f32_32x32x16_bf16 v[160:175], v[124:127], v[108:111], v[160:175]\n\t" \
  "ds_read_b128 v[68:71], v17 offset:512\n\t" \
  "ds_read_b128 v[72:75], v17 offset:544\n\t" \
  "ds_read_b128 v[76:79], v17 offset:576\n\t" \
  "ds_read_b128 v[80:83], v17 offset:608\n\t" \
  "v_mfma_f32_32x32x16_bf16 v[208:223], v[124:127], v[112:115], v[208:223]\n\t" \
  "v_fma_f32 v59, -v38, v38, v59\n\t" \
  "v_max_f32 v59, v59, v136\n\t" \
  "v_rsq_f32 v37, v59\n\t" \
  "v_mfma_f32_32x32x16_bf16 v[176:191], v[128:131], v[108:111], v[176:191]\n\t" \
  "v_mul_f32 v39, v38, v37\n\t" \
  "v_mfma_f32_32x32x16_bf16 v[224:239], v[128:131], v[112:115], v[224:239]\n\t" \
  "v_mfma_f32_32x32x16_bf16 v[240:255], v[116:119], v[132:135], v[240:255]\n\t" \
  "ds_read_b128 v[100:103], v17 offset:0\n\t" \
  "ds_read_b128 v[104:107], v17 offset:32\n\t" \
  "ds_read_b128 v[108:111], v17 offset:64\n\t" \
  "ds_read_b128 v[112:115], v17 offset:96\n\t" \
  "s_waitcnt lgkmcnt(11)\n\t" \
  "v_pk_mul_f32 v[56:57], v[84:85], v[30:31] op_sel:[0,1] op_sel_hi:[1,1]\n\t" \
  "v_pk_mul_f32 v[58:59], v[86:87], v[30:31] op_sel:[0,1] op_sel_hi:[1,1]\n\t" \
  "s_waitcnt lgkmcnt(10)\n\t" \
  "v_pk_mul_f32 v[60:61], v[88:89], v[30:31] op_sel:[0,1] op_sel_hi:[1,1]\n\t" \
  "v_pk_mul_f32 v[62:63], v[90:91], v[30:31] op_sel:[0,1] op_sel_hi:[1,1]\n\t" \
  "v_pk_fma_f32 v[160:161], v[160:161], v[28:29], v[56:57] op_sel:[0,1,0] op_sel_hi:[1,1,1]\n\t" \
  "v_pk_fma_f32 v[162:163], v[162:163], v[28:29], v[58:59] op_sel:[0,1,0] op_sel_hi:[1,1,1]\n\t" \
  "v_pk_fma_f32 v[164:165], v[164:165], v[28:29], v[60:61] op_sel:[0,1,0] op_sel_hi:[1,1,1]\n\t" \
  "v_pk_fma_f32 v[166:167], v[166:167], v[28:29], v[62:63] op_sel:[0,1,0] op_sel_hi:[1,1,1]\n\t" \
  "s_waitcnt lgkmcnt(9)\n\t" \
  "v_pk_mul_f32 v[56:57], v[92:93], v[30:31] op_sel:[0,1] op_sel_hi:[1,1]\n\t" \
  "v_pk_mul_f32 v[58:59], v[94:95], v[30:31] op_sel:[0,1] op_sel_hi:[1,1]\n\t" \
  "s_waitcnt lgkmcnt(8)\n\t" \
  "v_pk_mul_f32 v[60:61], v[96:97], v[30:31] op_sel:[0,1] op_sel_hi:[1,1]\n\t" \
  "v_pk_mul_f32 v[62:63], v[98:99], v[30:31] op_sel:[0,1] op_sel_hi:[1,1]\n\t" \
  "v_pk_fma_f32 v[168:169], v[168:169], v[28:29], v[56:57] op_sel:[0,1,0] op_sel_hi:[1,1,1]\n\t" \
  "v_pk_fma_f32 v[170:171], v[170:171], v[28:29], v[58:59] op_sel:[0,1,0] op_sel_hi:[1,1,1]\n\t" \
  "v_pk_fma_f32 v[172:173], v[172:173], v[28:29], v[60:61] op_sel:[0,1,0] op_sel_hi:[1,1,1]\n\t" \
  "v_pk_fma_f32 v[174:175], v[174:175], v[28:29], v[62:63] op_sel:[0,1,0] op_sel_hi:[1,1,1]\n\t" \
  "v_cvt_pk_bf16_f32 v40, v160, v161\n\t" \
  "v_cvt_pk_bf16_f32 v41, v162, v163\n\t" \
  "v_cvt_pk_bf16_f32 v42, v164, v165\n\t" \
  "v_cvt_pk_bf16_f32 v43, v166, v167\n\t" \
  "v_cvt_pk_bf16_f32 v44, v168, v169\n\t" \
  "v_cvt_pk_bf16_f32 v45, v170, v171\n\t" \
  "v_cvt_pk_bf16_f32 v46, v172, v173\n\t" \
  "v_cvt_pk_bf16_f32 v47, v174, v175\n\t" \
  "ds_write_b128 v18, v[40:43] offset:0\n\t" \
  "ds_write_b128 v18, v[44:47] offset:4608\n\t" \
  "s_waitcnt lgkmcnt(9)\n\t" \
  "v_pk_mul_f32 v[56:57], v[68:69], v[30:31] op_sel:[0,1] op_sel_hi:[1,1]\n\t" \
  "v_pk_mul_f32 v[58:59], v[70:71], v[30:31] op_sel:[0,1] op_sel_hi:[1,1]\n\t" \
  "s_waitcnt lgkmcnt(8)\n\t" \
  "v_pk_mul_f32 v[60:61], v[72:73], v[30:31] op_sel:[0,1] op_sel_hi:[1,1]\n\t" \
  "v_pk_mul_f32 v[62:63], v[74:75], v[30:31] op_sel:[0,1] op_sel_hi:[1,1]\n\t" \
  "v_pk_fma_f32 v[176:177], v[176:177], v[28:29], v[56:57] op_sel:[0,1,0] op_sel_hi:[1,1,1]\n\t" \
  "v_pk_fma_f32 v[178:179], v[178:179], v[28:29], v[58:59] op_sel:[0,1,0] op_sel_hi:[1,1,1]\n\t" \
  "v_pk_fma_f32 v[180:181], v[180:181], v[28:29], v[60:61] op_sel:[0,1,0] op_sel_hi:[1,1,1]\n\t" \
  "v_pk_fma_f32 v[182:183], v[182:183], v[28:29], v[62:63] op_sel:[0,1,0] op_sel_hi:[1,1,1]\n\t" \
  "s_waitcnt lgkmcnt(7)\n\t" \
  "v_pk_mul_f32 v[56:57], v[76:77], v[30:31] op_sel:[0,1] op_sel_hi:[1,1]\n\t" \
  "v_pk_mul_f32 v[58:59], v[78:79], v[30:31] op_sel:[0,1] op_sel_hi:[1,1]\n\t" \
  "s_waitcnt lgkmcnt(6)\n\t" \
  "v_pk_mul_f32 v[60:61], v[80:81], v[30:31] op_sel:[0,1] op_sel_hi:[1,1]\n\t" \
  "v_pk_mul_f32 v[62:63], v[82:83], v[30:31] op_sel:[0,1] op_sel_hi:[1,1]\n\t" \
  "v_pk_fma_f32 v[184:185], v[184:185], v[28:29], v[56:57] op_sel:[0,1,0] op_sel_hi:[1,1,1]\n\t" \
  "v_pk_fma_f32 v[186:187], v[186:187], v[28:29], v[58:59] op_sel:[0,1,0] op_sel_hi:[1,1,1]\n\t" \
  "v_pk_fma_f32 v[188:189], v[188:189], v[28:29], v[60:61] op_sel:[0,1,0] op_sel_hi:[1,1,1]\n\t" \
  "v_pk_fma_f32 v[190:191], v[190:191], v[28:29], v[62:63] op_sel:[0,1,0] op_sel_hi:[1,1,1]\n\t" \
  "ds_write_b128 v19, v[176:179] offset:0\n\t" \
  "ds_write_b128 v19, v[180:183] offset:2304\n\t" \
  "ds_write_b128 v19, v[184:187] offset:9216\n\t" \
  "ds_write_b128 v19, v[188:191] offset:11520\n\t" \
  "v_pk_mul_f32 v[56:57], v[84:85], v[34:35] op_sel:[0,1] op_sel_hi:[1,1]\n\t" \
  "v_pk_mul_f32 v[58:59], v[86:87], v[34:35] op_sel:[0,1] op_sel_hi:[1,1]\n\t" \
  "v_pk_mul_f32 v[60:61], v[88:89], v[34:35] op_sel:[0,1] op_sel_hi:[1,1]\n\t" \
  "v_pk_mul_f32 v[62:63], v[90:91], v[34:35] op_sel:[0,1] op_sel_hi:[1,1]\n\t" \
  "v_pk_fma_f32 v[208:209], v[208:209], v[32:33], v[56:57] op_sel:[0,1,0] op_sel_hi:[1,1,1]\n\t" \
  "v_pk_fma_f32 v[210:211], v[210:211], v[32:33], v[58:59] op_sel:[0,1,0] op_sel_hi:[1,1,1]\n\t" \
  "v_pk_fma_f32 v[212:213], v[212:213], v[32:33], v[60:61] op_sel:[0,1,0] op_sel_hi:[1,1,1]\n\t" \
  "v_pk_fma_f32 v[214:215], v[214:215], v[32:33], v[62:63] op_sel:[0,1,0] op_sel_hi:[1,1,1]\n\t" \
  "v_pk_mul_f32 v[56:57], v[92:93], v[34:35] op_sel:[0,1] op_sel_hi:[1,1]\n\t" \
  "v_pk_mul_f32 v[58:59], v[94:95], v[34:35] op_sel:[0,1] op_sel_hi:[1,1]\n\t" \
  "v_pk_mul_f32 v[60:61], v[96:97], v[34:35] op_sel:[0,1] op_sel_hi:[1,1]\n\t" \
  "v_pk_mul_f32 v[62:63], v[98:99], v[34:35] op_sel:[0,1] op_sel_hi:[1,1]\n\t" \
  "v_pk_fma_f32 v[216:217], v[216:217], v[32:33], v[56:57] op_sel:[0,1,0] op_sel_hi:[1,1,1]\n\t" \
  "v_pk_fma_f32 v[218:219], v[218:219], v[32:33], v[58:59] op_sel:[0,1,0] op_sel_hi:[1,1,1]\n\t" \
  "v_pk_fma_f32 v[220:221], v[220:221], v[32:33], v[60:61] op_sel:[0,1,0] op_sel_hi:[1,1,1]\n\t" \
  "v_pk_fma_f32 v[222:223], v[222:223], v[32:33], v[62:63] op_sel:[0,1,0] op_sel_hi:[1,1,1]\n\t" \
  "v_cvt_pk_bf16_f32 v48, v208, v209\n\t" \
  "v_cvt_pk_bf16_f32 v49, v210, v211\n\t" \
  "v_cvt_pk_bf16_f32 v50, v212, v213\n\t" \
  "v_cvt_pk_bf16_f32 v51, v214, v215\n\t" \
  "v_cvt_pk_bf16_f32 v52, v216, v217\n\t" \
  "v_cvt_pk_bf16_f32 v53, v218, v219\n\t" \
  "v_cvt_pk_bf16_f32 v54, v220, v221\n\t" \
  "v_cvt_pk_bf16_f32 v55, v222, v223\n\t" \
  "ds_write_b128 v18, v[48:51] offset:512\n\t" \
  "ds_write_b128 v18, v[52:55] offset:5120\n\t" \
  "v_pk_mul_f32 v[56:57], v[68:69], v[34:35] op_sel:[0,1] op_sel_hi:[1,1]\n\t" \
  "v_pk_mul_f32 v[58:59], v[70:71], v[34:35] op_sel:[0,1] op_sel_hi:[1,1]\n\t" \
  "v_pk_mul_f32 v[60:61], v[72:73], v[34:35] op_sel:[0,1] op_sel_hi:[1,1]\n\t" \
  "v_pk_mul_f32 v[62:63], v[74:75], v[34:35] op_sel:[0,1] op_sel_hi:[1,1]\n\t" \
  "v_pk_fma_f32 v[224:225], v[224:225], v[32:33], v[56:57] op_sel:[0,1,0] op_sel_hi:[1,1,1]\n\t" \
  "v_pk_fma_f32 v[226:227], v[226:227], v[32:33], v[58:59] op_sel:[0,1,0] op_sel_hi:[1,1,1]\n\t" \
  "v_pk_fma_f32 v[228:229], v[228:229], v[32:33], v[60:61] op_sel:[0,1,0] op_sel_hi:[1,1,1]\n\t" \
  "v_pk_fma_f32 v[230:231], v[230:231], v[32:33], v[62:63] op_sel:[0,1,0] op_sel_hi:[1,1,1]\n\t" \
  "v_pk_mul_f32 v[56:57], v[76:77], v[34:35] op_sel:[0,1] op_sel_hi:[1,1]\n\t" \
  "v_pk_mul_f32 v[58:59], v[78:79], v[34:35] op_sel:[0,1] op_sel_hi:[1,1]\n\t" \
  "v_pk_mul_f32 v[60:61], v[80:81], v[34:35] op_sel:[0,1] op_sel_hi:[1,1]\n\t" \
  "v_pk_mul_f32 v[62:63], v[82:83], v[34:35] op_sel:[0,1] op_sel_hi:[1,1]\n\t" \
  "v_pk_fma_f32 v[232:233], v[232:233], v[32:33], v[56:57] op_sel:[0,1,0] op_sel_hi:[1,1,1]\n\t" \
  "v_pk_fma_f32 v[234:235], v[234:235], v[32:33], v[58:59] op_sel:[0,1,0] op_sel_hi:[1,1,1]\n\t" \
  "v_pk_fma_f32 v[236:237], v[236:237], v[32:33], v[60:61] op_sel:[0,1,0] op_sel_hi:[1,1,1]\n\t" \
  "v_pk_fma_f32 v[238:239], v[238:239], v[32:33], v[62:63] op_sel:[0,1,0] op_sel_hi:[1,1,1]\n\t" \
  "ds_write_b128 v19, v[224:227] offset:512\n\t" \
  "ds_write_b128 v19, v[228:231] offset:2816\n\t" \
  "ds_write_b128 v19, v[232:235] offset:9728\n\t" \
  "ds_write_b128 v19, v[236:239] offset:12032\n\t" \
  "s_waitcnt lgkmcnt(0)\n\t" \
  "s_mov_b32 s60, 0xffff0000\n\t" \
  "s_mov_b32 s61, 0xffff0000\n\t" \
  "s_cmp_eq_u32 s90, 1\n\t" \
  "s_cbranch_scc1 HV%=\n\t" \
  "v_pk_mul_f32 v[56:57], v[84:85], v[38:39] op_sel:[0,1] op_sel_hi:[1,1]\n\t" \
  "v_pk_mul_f32 v[58:59], v[86:87], v[38:39] op_sel:[0,1] op_sel_hi:[1,1]\n\t" \
  "v_pk_mul_f32 v[60:61], v[88:89], v[38:39] op_sel:[0,1] op_sel_hi:[1,1]\n\t" \
  "v_pk_mul_f32 v[62:63], v[90:91], v[38:39] op_sel:[0,1] op_sel_hi:[1,1]\n\t" \
  "v_pk_fma_f32 v[240:241], v[240:241], v[36:37], v[56:57] op_sel:[0,1,0] op_sel_hi:[1,1,1]\n\t" \
  "v_pk_fma_f32 v[242:243], v[242:243], v[36:37], v[58:59] op_sel:[0,1,0] op_sel_hi:[1,1,1]\n\t" \
  "v_pk_fma_f32 v[244:245], v[244:245], v[36:37], v[60:61] op_sel:[0,1,0] op_sel_hi:[1,1,1]\n\t" \
  "v_pk_fma_f32 v[246:247], v[246:247], v[36:37], v[62:63] op_sel:[0,1,0] op_sel_hi:[1,1,1]\n\t" \
  "v_pk_mul_f32 v[56:57], v[92:93], v[38:39] op_sel:[0,1] op_sel_hi:[1,1]\n\t" \
  "v_pk_mul_f32 v[58:59], v[94:95], v[38:39] op_sel:[0,1] op_sel_hi:[1,1]\n\t" \
  "v_pk_mul_f32 v[60:61], v[96:97], v[38:39] op_sel:[0,1] op_sel_hi:[1,1]\n\t" \
  "v_pk_mul_f32 v[62:63], v[98:99], v[38:39] op_sel:[0,1] op_sel_hi:[1,1]\n\t" \
  "v_pk_fma_f32 v[248:249], v[248:249], v[36:37], v[56:57] op_sel:[0,1,0] op_sel_hi:[1,1,1]\n\t" \
  "v_pk_fma_f32 v[250:251], v[250:251], v[36:37], v[58:59] op_sel:[0,1,0] op_sel_hi:[1,1,1]\n\t" \
  "v_pk_fma_f32 v[252:253], v[252:253], v[36:37], v[60:61] op_sel:[0,1,0] op_sel_hi:[1,1,1]\n\t" \
  "v_pk_fma_f32 v[254:255], v[254:255], v[36:37], v[62:63] op_sel:[0,1,0] op_sel_hi:[1,1,1]\n\t" \
  "v_add_u32 v24, 0xfffffe00, v18\n\t" \
  "v_cvt_pk_bf16_f32 v124, v240, v241\n\t" \
  "v_cvt_pk_bf16_f32 v125, v242, v243\n\t" \
  "v_cvt_pk_bf16_f32 v126, v244, v245\n\t" \
  "v_cvt_pk_bf16_f32 v127, v246, v247\n\t" \
  "v_cvt_pk_bf16_f32 v128, v248, v249\n\t" \
  "v_cvt_pk_bf16_f32 v129, v250, v251\n\t" \
  "v_cvt_pk_bf16_f32 v130, v252, v253\n\t" \
  "v_cvt_pk_bf16_f32 v131, v254, v255\n\t" \
  "s_mov_b64 exec, s[60:61]\n\t" \
  "ds_write_b128 v24, v[124:127] offset:0\n\t" \
  "ds_write_b128 v24, v[128:131] offset:4608\n\t" \
  "s_branch HE%=\n\t" \
  "HV%=:\n\t" \
  "v_pk_mul_f32 v[56:57], v[68:69], v[38:39] op_sel:[0,1] op_sel_hi:[1,1]\n\t" \
  "v_pk_mul_f32 v[58:59], v[70:71], v[38:39] op_sel:[0,1] op_sel_hi:[1,1]\n\t" \
  "v_pk_mul_f32 v[60:61], v[72:73], v[38:39] op_sel:[0,1] op_sel_hi:[1,1]\n\t" \
  "v_pk_mul_f32 v[62:63], v[74:75], v[38:39] op_sel:[0,1] op_sel_hi:[1,1]\n\t" \
  "v_pk_fma_f32 v[240:241], v[240:241], v[36:37], v[56:57] op_sel:[0,1,0] op_sel_hi:[1,1,1]\n\t" \
  "v_pk_fma_f32 v[242:243], v[242:243], v[36:37], v[58:59] op_sel:[0,1,0] op_sel_hi:[1,1,1]\n\t" \
  "v_pk_fma_f32 v[244:245], v[244:245], v[36:37], v[60:61] op_sel:[0,1,0] op_sel_hi:[1,1,1]\n\t" \
  "v_pk_fma_f32 v[246:247], v[246:247], v[36:37], v[62:63] op_sel:[0,1,0] op_sel_hi:[1,1,1]\n\t" \
  "v_pk_mul_f32 v[56:57], v[76:77], v[38:39] op_sel:[0,1] op_sel_hi:[1,1]\n\t" \
  "v_pk_mul_f32 v[58:59], v[78:79], v[38:39] op_sel:[0,1] op_sel_hi:[1,1]\n\t" \
  "v_pk_mul_f32 v[60:61], v[80:81], v[38:39] op_sel:[0,1] op_sel_hi:[1,1]\n\t" \
  "v_pk_mul_f32 v[62:63], v[82:83], v[38:39] op_sel:[0,1] op_sel_hi:[1,1]\n\t" \
  "v_pk_fma_f32 v[248:249], v[248:249], v[36:37], v[56:57] op_sel:[0,1,0] op_sel_hi:[1,1,1]\n\t" \
  "v_pk_fma_f32 v[250:251], v[250:251], v[36:37], v[58:59] op_sel:[0,1,0] op_sel_hi:[1,1,1]\n\t" \
  "v_pk_fma_f32 v[252:253], v[252:253], v[36:37], v[60:61] op_sel:[0,1,0] op_sel_hi:[1,1,1]\n\t" \
  "v_pk_fma_f32 v[254:255], v[254:255], v[36:37], v[62:63] op_sel:[0,1,0] op_sel_hi:[1,1,1]\n\t" \
  "v_add_u32 v24, 0xfffffa00, v19\n\t" \
  "s_mov_b64 exec, s[60:61]\n\t" \
  "ds_write_b128 v24, v[240:243] offset:0\n\t" \
  "ds_write_b128 v24, v[244:247] offset:2304\n\t" \
  "ds_write_b128 v24, v[248:251] offset:9216\n\t" \
  "ds_write_b128 v24, v[252:255] offset:11520\n\t" \
  "HE%=:\n\t" \
  "s_mov_b64 exec, -1\n\t" \
  "v_mul_f32 v28, 0x3eb8aa3b, v29\n\t" \
  "v_pk_fma_f32 v[144:145], v[100:101], v[30:31], v[144:145] op_sel_hi:[1,0,1]\n\t" \
  "v_pk_fma_f32 v[146:147], v[102:103], v[30:31], v[146:147] op_sel_hi:[1,0,1]\n\t" \
  "v_pk_fma_f32 v[148:149], v[104:105], v[30:31], v[148:149] op_sel_hi:[1,0,1]\n\t" \
  "v_pk_fma_f32 v[150:151], v[106:107], v[30:31], v[150:151] op_sel_hi:[1,0,1]\n\t" \
  "v_pk_fma_f32 v[152:153], v[108:109], v[30:31], v[152:153] op_sel_hi:[1,0,1]\n\t" \
  "v_pk_fma_f32 v[154:155], v[110:111], v[30:31], v[154:155] op_sel_hi:[1,0,1]\n\t" \
  "v_pk_fma_f32 v[156:157], v[112:113], v[30:31], v[156:157] op_sel_hi:[1,0,1]\n\t" \
  "v_pk_fma_f32 v[158:159], v[114:115], v[30:31], v[158:159] op_sel_hi:[1,0,1]\n\t" \
  "v_cvt_pk_bf16_f32 v68, v144, v145\n\t" \
  "v_cvt_pk_bf16_f32 v69, v146, v147\n\t" \
  "v_cvt_pk_bf16_f32 v70, v148, v149\n\t" \
  "v_cvt_pk_bf16_f32 v71, v150, v151\n\t" \
  "v_cvt_pk_bf16_f32 v72, v152, v153\n\t" \
  "v_cvt_pk_bf16_f32 v73, v154, v155\n\t" \
  "v_cvt_pk_bf16_f32 v74, v156, v157\n\t" \
  "v_cvt_pk_bf16_f32 v75, v158, v159\n\t" \
  "v_permlane32_swap_b32 v68, v72\n\t" \
  "v_permlane32_swap_b32 v69, v73\n\t" \
  "v_permlane32_swap_b32 v70, v74\n\t" \
  "v_permlane32_swap_b32 v71, v75\n\t" \
  "v_mul_f32 v32, 0x3eb8aa3b, v33\n\t" \
  "v_pk_fma_f32 v[192:193], v[100:101], v[34:35], v[192:193] op_sel_hi:[1,0,1]\n\t" \
  "v_pk_fma_f32 v[194:195], v[102:103], v[34:35], v[194:195] op_sel_hi:[1,0,1]\n\t" \
  "v_pk_fma_f32 v[196:197], v[104:105], v[34:35], v[196:197] op_sel_hi:[1,0,1]\n\t" \
  "v_pk_fma_f32 v[198:199], v[106:107], v[34:35], v[198:199] op_sel_hi:[1,0,1]\n\t" \
  "v_pk_fma_f32 v[200:201], v[108:109], v[34:35], v[200:201] op_sel_hi:[1,0,1]\n\t" \
  "v_pk_fma_f32 v[202:203], v[110:111], v[34:35], v[202:203] op_sel_hi:[1,0,1]\n\t" \
  "v_pk_fma_f32 v[204:205], v[112:113], v[34:35], v[204:205] op_sel_hi:[1,0,1]\n\t" \
  "v_pk_fma_f32 v[206:207], v[114:115], v[34:35], v[206:207] op_sel_hi:[1,0,1]\n\t" \
  "v_cvt_pk_bf16_f32 v76, v192, v193\n\t" \
  "v_cvt_pk_bf16_f32 v77, v194, v195\n\t" \
  "v_cvt_pk_bf16_f32 v78, v196, v197\n\t" \
  "v_cvt_pk_bf16_f32 v79, v198, v199\n\t" \
  "v_cvt_pk_bf16_f32 v80, v200, v201\n\t" \
  "v_cvt_pk_bf16_f32 v81, v202, v203\n\t" \
  "v_cvt_pk_bf16_f32 v82, v204, v205\n\t" \
  "v_cvt_pk_bf16_f32 v83, v206, v207\n\t" \
  "v_permlane32_swap_b32 v76, v80\n\t" \
  "v_permlane32_swap_b32 v77, v81\n\t" \
  "v_permlane32_swap_b32 v78, v82\n\t" \
  "v_permlane32_swap_b32 v79, v83\n\t" \
  "s_waitcnt lgkmcnt(0)\n\t" \
  "s_barrier\n\t" \
  "ds_read_b128 v[144:147], v20 offset:224\n\t" \
  "ds_read_b128 v[148:151], v20 offset:2528\n\t" \
  "ds_read_b128 v[152:155], v20 offset:192\n\t" \
  "ds_read_b128 v[156:159], v20 offset:2496\n\t" \
  "ds_read_b128 v[160:163], v20 offset:160\n\t" \
  "ds_read_b128 v[164:167], v20 offset:2464\n\t" \
  "ds_read_b128 v[168:171], v20 offset:128\n\t" \
  "ds_read_b128 v[172:175], v20 offset:2432\n\t" \
  "ds_read_b128 v[176:179], v20 offset:96\n\t" \
  "ds_read_b128 v[180:183], v20 offset:2400\n\t" \
  "ds_read_b128 v[184:187], v20 offset:64\n\t" \
  "ds_read_b128 v[188:191], v20 offset:2368\n\t" \
  "ds_read_b128 v[192:195], v20 offset:32\n\t" \
  "ds_read_b128 v[196:199], v20 offset:2336\n\t" \
  "ds_read_b128 v[200:203], v20 offset:0\n\t" \
  "ds_read_b128 v[204:207], v20 offset:2304\n\t" \
  "s_waitcnt lgkmcnt(14)\n\t" \
  "v_dot2_f32_bf16 v84, v68, v144, 0\n\t" \
  "s_waitcnt lgkmcnt(13)\n\t" \
  "v_dot2_f32_bf16 v85, v68, v152, 0\n\t" \
  "s_waitcnt lgkmcnt(11)\n\t" \
  "v_dot2_f32_bf16 v86, v68, v160, 0\n\t" \
  "s_waitcnt lgkmcnt(9)\n\t" \
  "v_dot2_f32_bf16 v87, v68, v168, 0\n\t" \
  "v_dot2c_f32_bf16 v84, v69, v145\n\t" \
  "v_dot2c_f32_bf16 v85, v69, v153\n\t" \
  "v_dot2c_f32_bf16 v86, v69, v161\n\t" \
  "v_dot2c_f32_bf16 v87, v69, v169\n\t" \
  "v_dot2c_f32_bf16 v84, v70, v146\n\t" \
  "v_dot2c_f32_bf16 v85, v70, v154\n\t" \
  "v_dot2c_f32_bf16 v86, v70, v162\n\t" \
  "ds_read_b128 v[208:211], v21 offset:224\n\t" \
  "v_dot2c_f32_bf16 v87, v70, v170\n\t" \
  "v_dot2c_f32_bf16 v84, v71, v147\n\t" \
  "v_dot2c_f32_bf16 v85, v71, v155\n\t" \
  "ds_read_b128 v[212:215], v21 offset:192\n\t" \
  "v_dot2c_f32_bf16 v86, v71, v163\n\t" \
  "v_dot2c_f32_bf16 v87, v71, v171\n\t" \
  "v_dot2c_f32_bf16 v84, v72, v148\n\t" \
  "ds_read_b128 v[216:219], v21 offset:160\n\t" \
  "v_dot2c_f32_bf16 v85, v72, v156\n\t" \
  "v_dot2c_f32_bf16 v86, v72, v164\n\t" \
  "s_waitcnt lgkmcnt(11)\n\t" \
  "v_dot2c_f32_bf16 v87, v72, v172\n\t" \
  "ds_read_b128 v[220:223], v21 offset:128\n\t" \
  "v_dot2c_f32_bf16 v84, v73, v149\n\t" \
  "v_dot2c_f32_bf16 v85, v73, v157\n\t" \
  "v_dot2c_f32_bf16 v86, v73, v165\n\t" \
  "ds_read_b128 v[224:227], v21 offset:96\n\t" \
  "v_dot2c_f32_bf16 v87, v73, v173\n\t" \
  "v_dot2c_f32_bf16 v84, v74, v150\n\t" \
  "v_dot2c_f32_bf16 v85, v74, v158\n\t" \
  "ds_read_b128 v[228:231], v21 offset:64\n\t" \
  "v_dot2c_f32_bf16 v86, v74, v166\n\t" \
  "v_dot2c_f32_bf16 v87, v74, v174\n\t" \
  "v_dot2c_f32_bf16 v84, v75, v151\n\t" \
  "ds_read_b128 v[232:235], v21 offset:32\n\t" \
  "v_dot2c_f32_bf16 v85, v75, v159\n\t" \
  "v_dot2c_f32_bf16 v86, v75, v167\n\t" \
  "v_dot2c_f32_bf16 v87, v75, v175\n\t" \
  "ds_read_b128 v[236:239], v21 offset:0\n\t" \
  "s_waitcnt lgkmcnt(8)\n\t" \
  "v_dot2_f32_bf16 v88, v68, v176, 0\n\t" \
  "v_dot2_f32_bf16 v89, v68, v184, 0\n\t" \
  "v_dot2_f32_bf16 v90, v68, v192, 0\n\t" \
  "v_dot2_f32_bf16 v91, v68, v200, 0\n\t" \
  "v_dot2c_f32_bf16 v88, v69, v177\n\t" \
  "v_dot2c_f32_bf16 v89, v69, v185\n\t" \
  "v_dot2c_f32_bf16 v90, v69, v193\n\t" \
  "v_dot2c_f32_bf16 v91, v69, v201\n\t" \
  "v_dot2c_f32_bf16 v88, v70, v178\n\t" \
  "v_dot2c_f32_bf16 v89, v70, v186\n\t" \
  "v_dot2c_f32_bf16 v90, v70, v194\n\t" \
  "v_dot2c_f32_bf16 v91, v70, v202\n\t" \
  "v_dot2c_f32_bf16 v88, v71, v179\n\t" \
  "v_dot2c_f32_bf16 v89, v71, v187\n\t" \
  "v_dot2c_f32_bf16 v90, v71, v195\n\t" \
  "v_dot2c_f32_bf16 v91, v71, v203\n\t" \
  "v_dot2c_f32_bf16 v88, v72, v180\n\t" \
  "v_dot2c_f32_bf16 v89, v72, v188\n\t" \
  "v_dot2c_f32_bf16 v90, v72, v196\n\t" \
  "v_dot2c_f32_bf16 v91, v72, v204\n\t" \
  "v_dot2c_f32_bf16 v88, v73, v181\n\t" \
  "v_dot2c_f32_bf16 v89, v73, v189\n\t" \
  "v_dot2c_f32_bf16 v90, v73, v197\n\t" \
  "v_dot2c_f32_bf16 v91, v73, v205\n\t" \
  "v_dot2c_f32_bf16 v88, v74, v182\n\t" \
  "v_dot2c_f32_bf16 v89, v74, v190\n\t" \
  "v_dot2c_f32_bf16 v90, v74, v198\n\t" \
  "v_dot2c_f32_bf16 v91, v74, v206\n\t" \
  "v_dot2c_f32_bf16 v88, v75, v183\n\t" \
  "v_dot2c_f32_bf16 v89, v75, v191\n\t" \
  "v_dot2c_f32_bf16 v90, v75, v199\n\t" \
  "v_dot2c_f32_bf16 v91, v75, v207\n\t" \
  "s_add_u32 s92, s66, 65536\n\t" \
  "s_addc_u32 s93, s67, 0\n\t" \
  "s_add_u32 m0, s83, 57344\n\t" \
  "s_nop 0\n\t" \
  "global_load_lds_dwordx4 v64, s[92:93]\n\t" \
  "s_or_b32 s58, s87, s90\n\t" \
  "s_cbranch_scc1 MK%=\n\t" \
  "v_and_b32 v124, 0x1f0, v0\n\t" \
  "v_mov_b32 v125, 0xff7fffff\n\t" \
  "s_nop 1\n\t" \
  "v_cmp_le_u32 vcc, 32, v124\n\t" \
  "s_nop 1\n\t" \
  "v_cndmask_b32 v85, v125, v85, vcc\n\t" \
  "v_cmp_le_u32 vcc, 64, v124\n\t" \
  "s_nop 1\n\t" \
  "v_cndmask_b32 v86, v125, v86, vcc\n\t" \
  "v_cmp_le_u32 vcc, 96, v124\n\t" \
  "s_nop 1\n\t" \
  "v_cndmask_b32 v87, v125, v87, vcc\n\t" \
  "v_cmp_le_u32 vcc, 128, v124\n\t" \
  "s_nop 1\n\t" \
  "v_cndmask_b32 v88, v125, v88, vcc\n\t" \
  "v_cmp_le_u32 vcc, 160, v124\n\t" \
  "s_nop 1\n\t" \
  "v_cndmask_b32 v89, v125, v89, vcc\n\t" \
  "v_cmp_le_u32 vcc, 192, v124\n\t" \
  "s_nop 1\n\t" \
  "v_cndmask_b32 v90, v125, v90, vcc\n\t" \
  "v_cmp_le_u32 vcc, 224, v124\n\t" \
  "s_nop 1\n\t" \
  "v_cndmask_b32 v91, v125, v91, vcc\n\t" \
  "MK%=:\n\t" \
  "s_nop 2\n\t" \
  "v_max3_f32 v124, v84, v85, v86\n\t" \
  "v_max3_f32 v125, v87, v88, v89\n\t" \
  "v_max3_f32 v124, v124, v90, v91\n\t" \
  "ds_read_b128 v[144:147], v21 offset:2528\n\t" \
  "v_max_f32 v124, v124, v125\n\t" \
  "v_mul_f32 v124, -v124, v28\n\t" \
  "v_pk_fma_f32 v[84:85], v[84:85], v[28:29], v[124:125] op_sel_hi:[1,0,0]\n\t" \
  "ds_read_b128 v[148:151], v21 offset:2496\n\t" \
  "v_pk_fma_f32 v[86:87], v[86:87], v[28:29], v[124:125] op_sel_hi:[1,0,0]\n\t" \
  "v_pk_fma_f32 v[88:89], v[88:89], v[28:29], v[124:125] op_sel_hi:[1,0,0]\n\t" \
  "v_pk_fma_f32 v[90:91], v[90:91], v[28:29], v[124:125] op_sel_hi:[1,0,0]\n\t" \
  "ds_read_b128 v[152:155], v21 offset:2464\n\t" \
  "v_exp_f32 v84, v84\n\t" \
  "v_exp_f32 v85, v85\n\t" \
  "v_exp_f32 v86, v86\n\t" \
  "ds_read_b128 v[156:159], v21 offset:2432\n\t" \
  "v_exp_f32 v87, v87\n\t" \
  "v_exp_f32 v88, v88\n\t" \
  "v_exp_f32 v89, v89\n\t" \
  "ds_read_b128 v[160:163], v21 offset:2400\n\t" \
  "v_exp_f32 v90, v90\n\t" \
  "v_exp_f32 v91, v91\n\t" \
  "v_pk_add_f32 v[124:125], v[84:85], v[86:87]\n\t" \
  "ds_read_b128 v[164:167], v21 offset:2368\n\t" \
  "v_pk_add_f32 v[126:127], v[88:89], v[90:91]\n\t" \
  "v_pk_add_f32 v[124:125], v[124:125], v[126:127]\n\t" \
  "v_add_f32 v124, v124, v125\n\t" \
  "ds_read_b128 v[168:171], v21 offset:2336\n\t" \
  "v_rcp_f32 v124, v124\n\t" \
  "s_nop 0\n\t" \
  "v_mul_f32 v124, 0xbfb8aa3b, v124\n\t" \
  "v_pk_mul_f32 v[84:85], v[84:85], v[124:125] op_sel_hi:[1,0]\n\t" \
  "ds_read_b128 v[172:175], v21 offset:2304\n\t" \
  "v_pk_mul_f32 v[86:87], v[86:87], v[124:125] op_sel_hi:[1,0]\n\t" \
  "v_pk_mul_f32 v[88:89], v[88:89], v[124:125] op_sel_hi:[1,0]\n\t" \
  "v_pk_mul_f32 v[90:91], v[90:91], v[124:125] op_sel_hi:[1,0]\n\t" \
  "s_waitcnt lgkmcnt(8)\n\t" \
  "v_pk_mul_f32 v[92:93], v[208:209], v[84:85] op_sel_hi:[1,0]\n\t" \
  "ds_read_b128 v[176:179], v21 offset:4832\n\t" \
  "v_pk_mul_f32 v[94:95], v[210:211], v[84:85] op_sel_hi:[1,0]\n\t" \
  "v_pk_fma_f32 v[92:93], v[212:213], v[84:85], v[92:93] op_sel:[0,1,0] op_sel_hi:[1,1,1]\n\t" \
  "v_pk_fma_f32 v[94:95], v[214:215], v[84:85], v[94:95] op_sel:[0,1,0] op_sel_hi:[1,1,1]\n\t" \
  "ds_read_b128 v[180:183], v21 offset:4800\n\t" \
  "v_pk_fma_f32 v[92:93], v[216:217], v[86:87], v[92:93] op_sel:[0,0,0] op_sel_hi:[1,0,1]\n\t" \
  "v_pk_fma_f32 v[94:95], v[218:219], v[86:87], v[94:95] op_sel:[0,0,0] op_sel_hi:[1,0,1]\n\t" \
  "ds_read_b128 v[184:187], v21 offset:4768\n\t" \
  "v_pk_fma_f32 v[92:93], v[220:221], v[86:87], v[92:93] op_sel:[0,1,0] op_sel_hi:[1,1,1]\n\t" \
  "v_pk_fma_f32 v[94:95], v[222:223], v[86:87], v[94:95] op_sel:[0,1,0] op_sel_hi:[1,1,1]\n\t" \
  "ds_read_b128 v[188:191], v21 offset:4736\n\t" \
  "v_pk_fma_f32 v[92:93], v[224:225], v[88:89], v[92:93] op_sel:[0,0,0] op_sel_hi:[1,0,1]\n\t" \
  "v_pk_fma_f32 v[94:95], v[226:227], v[88:89], v[94:95] op_sel:[0,0,0] op_sel_hi:[1,0,1]\n\t" \
  "v_pk_fma_f32 v[92:93], v[228:229], v[88:89], v[92:93] op_sel:[0,1,0] op_sel_hi:[1,1,1]\n\t" \
  "global_load_lds_dwordx4 v65, s[92:93] offset:1024\n\t" \
  "ds_read_b128 v[192:195], v21 offset:4704\n\t" \
  "v_pk_fma_f32 v[94:95], v[230:231], v[88:89], v[94:95] op_sel:[0,1,0] op_sel_hi:[1,1,1]\n\t" \
  "v_pk_fma_f32 v[92:93], v[232:233], v[90:91], v[92:93] op_sel:[0,0,0] op_sel_hi:[1,0,1]\n\t" \
  "ds_read_b128 v[196:199], v21 offset:4672\n\t" \
  "v_pk_fma_f32 v[94:95], v[234:235], v[90:91], v[94:95] op_sel:[0,0,0] op_sel_hi:[1,0,1]\n\t" \
  "v_pk_fma_f32 v[92:93], v[236:237], v[90:91], v[92:93] op_sel:[0,1,0] op_sel_hi:[1,1,1]\n\t" \
  "v_pk_fma_f32 v[94:95], v[238:239], v[90:91], v[94:95] op_sel:[0,1,0] op_sel_hi:[1,1,1]\n\t" \
  "ds_read_b128 v[200:203], v21 offset:4640\n\t" \
  "v_cvt_pk_bf16_f32 v108, v92, v93\n\t" \
  "v_cvt_pk_bf16_f32 v109, v94, v95\n\t" \
  "ds_read_b128 v[204:207], v21 offset:4608\n\t" \
  "s_waitcnt lgkmcnt(8)\n\t" \
  "v_pk_mul_f32 v[96:97], v[144:145], v[84:85] op_sel_hi:[1,0]\n\t" \
  "ds_read_b128 v[208:211], v21 offset:7136\n\t" \
  "v_pk_mul_f32 v[98:99], v[146:147], v[84:85] op_sel_hi:[1,0]\n\t" \
  "v_pk_fma_f32 v[96:97], v[148:149], v[84:85], v[96:97] op_sel:[0,1,0] op_sel_hi:[1,1,1]\n\t" \
  "v_pk_fma_f32 v[98:99], v[150:151], v[84:85], v[98:99] op_sel:[0,1,0] op_sel_hi:[1,1,1]\n\t" \
  "ds_read_b128 v[212:215], v21 offset:7104\n\t" \
  "v_pk_fma_f32 v[96:97], v[152:153], v[86:87], v[96:97] op_sel:[0,0,0] op_sel_hi:[1,0,1]\n\t" \
  "v_pk_fma_f32 v[98:99], v[154:155], v[86:87], v[98:99] op_sel:[0,0,0] op_sel_hi:[1,0,1]\n\t" \
  "ds_read_b128 v[216:219], v21 offset:7072\n\t" \
  "v_pk_fma_f32 v[96:97], v[156:157], v[86:87], v[96:97] op_sel:[0,1,0] op_sel_hi:[1,1,1]\n\t" \
  "v_pk_fma_f32 v[98:99], v[158:159], v[86:87], v[98:99] op_sel:[0,1,0] op_sel_hi:[1,1,1]\n\t" \
  "ds_read_b128 v[220:223], v21 offset:7040\n\t" \
  "v_pk_fma_f32 v[96:97], v[160:161], v[88:89], v[96:97] op_sel:[0,0,0] op_sel_hi:[1,0,1]\n\t" \
  "v_pk_fma_f32 v[98:99], v[162:163], v[88:89], v[98:99] op_sel:[0,0,0] op_sel_hi:[1,0,1]\n\t" \
  "v_pk_fma_f32 v[96:97], v[164:165], v[88:89], v[96:97] op_sel:[0,1,0] op_sel_hi:[1,1,1]\n\t" \
  "ds_read_b128 v[224:227], v21 offset:7008\n\t" \
  "v_pk_fma_f32 v[98:99], v[166:167], v[88:89], v[98:99] op_sel:[0,1,0] op_sel_hi:[1,1,1]\n\t" \
  "v_pk_fma_f32 v[96:97], v[168:169], v[90:91], v[96:97] op_sel:[0,0,0] op_sel_hi:[1,0,1]\n\t" \
  "ds_read_b128 v[228:231], v21 offset:6976\n\t" \
  "v_pk_fma_f32 v[98:99], v[170:171], v[90:91], v[98:99] op_sel:[0,0,0] op_sel_hi:[1,0,1]\n\t" \
  "v_pk_fma_f32 v[96:97], v[172:173], v[90:91], v[96:97] op_sel:[0,1,0] op_sel_hi:[1,1,1]\n\t" \
  "v_pk_fma_f32 v[98:99], v[174:175], v[90:91], v[98:99] op_sel:[0,1,0] op_sel_hi:[1,1,1]\n\t" \
  "ds_read_b128 v[232:235], v21 offset:6944\n\t" \
  "v_cvt_pk_bf16_f32 v110, v96, v97\n\t" \
  "v_cvt_pk_bf16_f32 v111, v98, v99\n\t" \
  "ds_read_b128 v[236:239], v21 offset:6912\n\t" \
  "s_waitcnt lgkmcnt(8)\n\t" \
  "v_pk_mul_f32 v[100:101], v[176:177], v[84:85] op_sel_hi:[1,0]\n\t" \
  "v_pk_mul_f32 v[102:103], v[178:179], v[84:85] op_sel_hi:[1,0]\n\t" \
  "v_pk_fma_f32 v[100:101], v[180:181], v[84:85], v[100:101] op_sel:[0,1,0] op_sel_hi:[1,1,1]\n\t" \
  "v_pk_fma_f32 v[102:103], v[182:183], v[84:85], v[102:103] op_sel:[0,1,0] op_sel_hi:[1,1,1]\n\t" \
  "v_pk_fma_f32 v[100:101], v[184:185], v[86:87], v[100:101] op_sel:[0,0,0] op_sel_hi:[1,0,1]\n\t" \
  "v_pk_fma_f32 v[102:103], v[186:187], v[86:87], v[102:103] op_sel:[0,0,0] op_sel_hi:[1,0,1]\n\t" \
  "v_pk_fma_f32 v[100:101], v[188:189], v[86:87], v[100:101] op_sel:[0,1,0] op_sel_hi:[1,1,1]\n\t" \
  "v_pk_fma_f32 v[102:103], v[190:191], v[86:87], v[102:103] op_sel:[0,1,0] op_sel_hi:[1,1,1]\n\t" \
  "v_pk_fma_f32 v[100:101], v[192:193], v[88:89], v[100:101] op_sel:[0,0,0] op_sel_hi:[1,0,1]\n\t" \
  "v_pk_fma_f32 v[102:103], v[194:195], v[88:89], v[102:103] op_sel:[0,0,0] op_sel_hi:[1,0,1]\n\t" \
  "v_pk_fma_f32 v[100:101], v[196:197], v[88:89], v[100:101] op_sel:[0,1,0] op_sel_hi:[1,1,1]\n\t" \
  "v_pk_fma_f32 v[102:103], v[198:199], v[88:89], v[102:103] op_sel:[0,1,0] op_sel_hi:[1,1,1]\n\t" \
  "s_add_u32 s94, s66, 163840\n\t" \
  "s_addc_u32 s95, s67, 0\n\t" \
  "s_add_u32 m0, s83, 65536\n\t" \
  "s_nop 0\n\t" \
  "global_load_lds_dwordx4 v64, s[94:95]\n\t" \
  "v_pk_fma_f32 v[100:101], v[200:201], v[90:91], v[100:101] op_sel:[0,0,0] op_sel_hi:[1,0,1]\n\t" \
  "v_pk_fma_f32 v[102:103], v[202:203], v[90:91], v[102:103] op_sel:[0,0,0] op_sel_hi:[1,0,1]\n\t" \
  "v_pk_fma_f32 v[100:101], v[204:205], v[90:91], v[100:101] op_sel:[0,1,0] op_sel_hi:[1,1,1]\n\t" \
  "v_pk_fma_f32 v[102:103], v[206:207], v[90:91], v[102:103] op_sel:[0,1,0] op_sel_hi:[1,1,1]\n\t" \
  "v_cvt_pk_bf16_f32 v112, v100, v101\n\t" \
  "v_cvt_pk_bf16_f32 v113, v102, v103\n\t" \
  "s_waitcnt lgkmcnt(0)\n\t" \
  "ds_read_b128 v[144:147], v20 offset:736\n\t" \
  "v_pk_mul_f32 v[104:105], v[208:209], v[84:85] op_sel_hi:[1,0]\n\t" \
  "ds_read_b128 v[148:151], v20 offset:3040\n\t" \
  "v_pk_mul_f32 v[106:107], v[210:211], v[84:85] op_sel_hi:[1,0]\n\t" \
  "v_pk_fma_f32 v[104:105], v[212:213], v[84:85], v[104:105] op_sel:[0,1,0] op_sel_hi:[1,1,1]\n\t" \
  "ds_read_b128 v[152:155], v20 offset:704\n\t" \
  "v_pk_fma_f32 v[106:107], v[214:215], v[84:85], v[106:107] op_sel:[0,1,0] op_sel_hi:[1,1,1]\n\t" \
  "ds_read_b128 v[156:159], v20 offset:3008\n\t" \
  "v_pk_fma_f32 v[104:105], v[216:217], v[86:87], v[104:105] op_sel:[0,0,0] op_sel_hi:[1,0,1]\n\t" \
  "ds_read_b128 v[160:163], v20 offset:672\n\t" \
  "v_pk_fma_f32 v[106:107], v[218:219], v[86:87], v[106:107] op_sel:[0,0,0] op_sel_hi:[1,0,1]\n\t" \
  "ds_read_b128 v[164:167], v20 offset:2976\n\t" \
  "v_pk_fma_f32 v[104:105], v[220:221], v[86:87], v[104:105] op_sel:[0,1,0] op_sel_hi:[1,1,1]\n\t" \
  "ds_read_b128 v[168:171], v20 offset:640\n\t" \
  "v_pk_fma_f32 v[106:107], v[222:223], v[86:87], v[106:107] op_sel:[0,1,0] op_sel_hi:[1,1,1]\n\t" \
  "ds_read_b128 v[172:175], v20 offset:2944\n\t" \
  "v_pk_fma_f32 v[104:105], v[224:225], v[88:89], v[104:105] op_sel:[0,0,0] op_sel_hi:[1,0,1]\n\t" \
  "v_pk_fma_f32 v[106:107], v[226:227], v[88:89], v[106:107] op_sel:[0,0,0] op_sel_hi:[1,0,1]\n\t" \
  "ds_read_b128 v[176:179], v20 offset:608\n\t" \
  "v_pk_fma_f32 v[104:105], v[228:229], v[88:89], v[104:105] op_sel:[0,1,0] op_sel_hi:[1,1,1]\n\t" \
  "ds_read_b128 v[180:183], v20 offset:2912\n\t" \
  "v_pk_fma_f32 v[106:107], v[230:231], v[88:89], v[106:107] op_sel:[0,1,0] op_sel_hi:[1,1,1]\n\t" \
  "ds_read_b128 v[184:187], v20 offset:576\n\t" \
  "v_pk_fma_f32 v[104:105], v[232:233], v[90:91], v[104:105] op_sel:[0,0,0] op_sel_hi:[1,0,1]\n\t" \
  "ds_read_b128 v[188:191], v20 offset:2880\n\t" \
  "v_pk_fma_f32 v[106:107], v[234:235], v[90:91], v[106:107] op_sel:[0,0,0] op_sel_hi:[1,0,1]\n\t" \
  "ds_read_b128 v[192:195], v20 offset:544\n\t" \
  "v_pk_fma_f32 v[104:105], v[236:237], v[90:91], v[104:105] op_sel:[0,1,0] op_sel_hi:[1,1,1]\n\t" \
  "v_pk_fma_f32 v[106:107], v[238:239], v[90:91], v[106:107] op_sel:[0,1,0] op_sel_hi:[1,1,1]\n\t" \
  "ds_read_b128 v[196:199], v20 offset:2848\n\t" \
  "v_cvt_pk_bf16_f32 v114, v104, v105\n\t" \
  "ds_read_b128 v[200:203], v20 offset:512\n\t" \
  "v_cvt_pk_bf16_f32 v115, v106, v107\n\t" \
  "ds_read_b128 v[204:207], v20 offset:2816\n\t" \
  "v_permlane32_swap_b32 v108, v112\n\t" \
  "v_permlane32_swap_b32 v109, v113\n\t" \
  "v_permlane32_swap_b32 v110, v114\n\t" \
  "v_permlane32_swap_b32 v111, v115\n\t" \
  "global_store_dwordx4 v0, v[108:111], s[70:71] offset:0 sc1\n\t" \
  "global_store_dwordx4 v0, v[112:115], s[70:71] offset:1024 sc1\n\t" \
  "s_waitcnt lgkmcnt(8)\n\t" \
  "v_dot2_f32_bf16 v84, v76, v144, 0\n\t" \
  "v_dot2_f32_bf16 v85, v76, v152, 0\n\t" \
  "v_dot2_f32_bf16 v86, v76, v160, 0\n\t" \
  "global_load_lds_dwordx4 v65, s[94:95] offset:1024\n\t" \
  "v_dot2_f32_bf16 v87, v76, v168, 0\n\t" \
  "v_dot2c_f32_bf16 v84, v77, v145\n\t" \
  "v_dot2c_f32_bf16 v85, v77, v153\n\t" \
  "v_dot2c_f32_bf16 v86, v77, v161\n\t" \
  "v_dot2c_f32_bf16 v87, v77, v169\n\t" \
  "v_dot2c_f32_bf16 v84, v78, v146\n\t" \
  "v_dot2c_f32_bf16 v85, v78, v154\n\t" \
  "ds_read_b128 v[208:211], v21 offset:736\n\t" \
  "v_dot2c_f32_bf16 v86, v78, v162\n\t" \
  "v_dot2c_f32_bf16 v87, v78, v170\n\t" \
  "v_dot2c_f32_bf16 v84, v79, v147\n\t" \
  "ds_read_b128 v[212:215], v21 offset:704\n\t" \
  "v_dot2c_f32_bf16 v85, v79, v155\n\t" \
  "v_dot2c_f32_bf16 v86, v79, v163\n\t" \
  "v_dot2c_f32_bf16 v87, v79, v171\n\t" \
  "ds_read_b128 v[216:219], v21 offset:672\n\t" \
  "v_dot2c_f32_bf16 v84, v80, v148\n\t" \
  "v_dot2c_f32_bf16 v85, v80, v156\n\t" \
  "v_dot2c_f32_bf16 v86, v80, v164\n\t" \
  "ds_read_b128 v[220:223], v21 offset:640\n\t" \
  "v_dot2c_f32_bf16 v87, v80, v172\n\t" \
  "v_dot2c_f32_bf16 v84, v81, v149\n\t" \
  "v_dot2c_f32_bf16 v85, v81, v157\n\t" \
  "ds_read_b128 v[224:227], v21 offset:608\n\t" \
  "v_dot2c_f32_bf16 v86, v81, v165\n\t" \
  "v_dot2c_f32_bf16 v87, v81, v173\n\t" \
  "v_dot2c_f32_bf16 v84, v82, v150\n\t" \
  "ds_read_b128 v[228:231], v21 offset:576\n\t" \
  "v_dot2c_f32_bf16 v85, v82, v158\n\t" \
  "v_dot2c_f32_bf16 v86, v82, v166\n\t" \
  "v_dot2c_f32_bf16 v87, v82, v174\n\t" \
  "ds_read_b128 v[232:235], v21 offset:544\n\t" \
  "v_dot2c_f32_bf16 v84, v83, v151\n\t" \
  "v_dot2c_f32_bf16 v85, v83, v159\n\t" \
  "v_dot2c_f32_bf16 v86, v83, v167\n\t" \
  "ds_read_b128 v[236:239], v21 offset:512\n\t" \
  "v_dot2c_f32_bf16 v87, v83, v175\n\t" \
  "s_waitcnt lgkmcnt(8)\n\t" \
  "v_dot2_f32_bf16 v88, v76, v176, 0\n\t" \
  "v_dot2_f32_bf16 v89, v76, v184, 0\n\t" \
  "v_dot2_f32_bf16 v90, v76, v192, 0\n\t" \
  "v_dot2_f32_bf16 v91, v76, v200, 0\n\t" \
  "v_dot2c_f32_bf16 v88, v77, v177\n\t" \
  "v_dot2c_f32_bf16 v89, v77, v185\n\t" \
  "v_dot2c_f32_bf16 v90, v77, v193\n\t" \
  "v_dot2c_f32_bf16 v91, v77, v201\n\t" \
  "v_dot2c_f32_bf16 v88, v78, v178\n\t" \
  "v_dot2c_f32_bf16 v89, v78, v186\n\t" \
  "v_dot2c_f32_bf16 v90, v78, v194\n\t" \
  "v_dot2c_f32_bf16 v91, v78, v202\n\t" \
  "v_dot2c_f32_bf16 v88, v79, v179\n\t" \
  "s_add_u32 s92, s66, 196608\n\t" \
  "s_addc_u32 s93, s67, 0\n\t" \
  "s_add_u32 m0, s83, 73728\n\t" \
  "s_nop 0\n\t" \
  "global_load_lds_dwordx4 v64, s[92:93]\n\t" \
  "v_dot2c_f32_bf16 v89, v79, v187\n\t" \
  "v_dot2c_f32_bf16 v90, v79, v195\n\t" \
  "v_dot2c_f32_bf16 v91, v79, v203\n\t" \
  "v_dot2c_f32_bf16 v88, v80, v180\n\t" \
  "v_dot2c_f32_bf16 v89, v80, v188\n\t" \
  "v_dot2c_f32_bf16 v90, v80, v196\n\t" \
  "v_dot2c_f32_bf16 v91, v80, v204\n\t" \
  "v_dot2c_f32_bf16 v88, v81, v181\n\t" \
  "v_dot2c_f32_bf16 v89, v81, v189\n\t" \
  "v_dot2c_f32_bf16 v90, v81, v197\n\t" \
  "v_dot2c_f32_bf16 v91, v81, v205\n\t" \
  "v_dot2c_f32_bf16 v88, v82, v182\n\t" \
  "v_dot2c_f32_bf16 v89, v82, v190\n\t" \
  "v_dot2c_f32_bf16 v90, v82, v198\n\t" \
  "v_dot2c_f32_bf16 v91, v82, v206\n\t" \
  "v_dot2c_f32_bf16 v88, v83, v183\n\t" \
  "v_dot2c_f32_bf16 v89, v83, v191\n\t" \
  "v_dot2c_f32_bf16 v90, v83, v199\n\t" \
  "v_dot2c_f32_bf16 v91, v83, v207\n\t" \
  "v_max3_f32 v124, v84, v85, v86\n\t" \
  "v_max3_f32 v125, v87, v88, v89\n\t" \
  "s_nop 0\n\t" \
  "v_max3_f32 v124, v124, v90, v91\n\t" \
  "ds_read_b128 v[144:147], v21 offset:3040\n\t" \
  "v_max_f32 v124, v124, v125\n\t" \
  "v_mul_f32 v124, -v124, v32\n\t" \
  "v_pk_fma_f32 v[84:85], v[84:85], v[32:33], v[124:125] op_sel_hi:[1,0,0]\n\t" \
  "ds_read_b128 v[148:151], v21 offset:3008\n\t" \
  "v_pk_fma_f32 v[86:87], v[86:87], v[32:33], v[124:125] op_sel_hi:[1,0,0]\n\t" \
  "v_pk_fma_f32 v[88:89], v[88:89], v[32:33], v[124:125] op_sel_hi:[1,0,0]\n\t" \
  "v_pk_fma_f32 v[90:91], v[90:91], v[32:33], v[124:125] op_sel_hi:[1,0,0]\n\t" \
  "ds_read_b128 v[152:155], v21 offset:2976\n\t" \
  "v_exp_f32 v84, v84\n\t" \
  "v_exp_f32 v85, v85\n\t" \
  "v_exp_f32 v86, v86\n\t" \
  "ds_read_b128 v[156:159], v21 offset:2944\n\t" \
  "v_exp_f32 v87, v87\n\t" \
  "v_exp_f32 v88, v88\n\t" \
  "v_exp_f32 v89, v89\n\t" \
  "ds_read_b128 v[160:163], v21 offset:2912\n\t" \
  "v_exp_f32 v90, v90\n\t" \
  "v_exp_f32 v91, v91\n\t" \
  "v_pk_add_f32 v[124:125], v[84:85], v[86:87]\n\t" \
  "ds_read_b128 v[164:167], v21 offset:2880\n\t" \
  "v_pk_add_f32 v[126:127], v[88:89], v[90:91]\n\t" \
  "v_pk_add_f32 v[124:125], v[124:125], v[126:127]\n\t" \
  "v_add_f32 v124, v124, v125\n\t" \
  "ds_read_b128 v[168:171], v21 offset:2848\n\t" \
  "v_rcp_f32 v124, v124\n\t" \
  "s_nop 0\n\t" \
  "v_mul_f32 v124, 0xbfb8aa3b, v124\n\t" \
  "v_pk_mul_f32 v[84:85], v[84:85], v[124:125] op_sel_hi:[1,0]\n\t" \
  "ds_read_b128 v[172:175], v21 offset:2816\n\t" \
  "global_load_lds_dwordx4 v65, s[92:93] offset:1024\n\t" \
  "v_pk_mul_f32 v[86:87], v[86:87], v[124:125] op_sel_hi:[1,0]\n\t" \
  "v_pk_mul_f32 v[88:89], v[88:89], v[124:125] op_sel_hi:[1,0]\n\t" \
  "v_pk_mul_f32 v[90:91], v[90:91], v[124:125] op_sel_hi:[1,0]\n\t" \
  "s_waitcnt lgkmcnt(8)\n\t" \
  "v_pk_mul_f32 v[92:93], v[208:209], v[84:85] op_sel_hi:[1,0]\n\t" \
  "ds_read_b128 v[176:179], v21 offset:5344\n\t" \
  "v_pk_mul_f32 v[94:95], v[210:211], v[84:85] op_sel_hi:[1,0]\n\t" \
  "v_pk_fma_f32 v[92:93], v[212:213], v[84:85], v[92:93] op_sel:[0,1,0] op_sel_hi:[1,1,1]\n\t" \
  "v_pk_fma_f32 v[94:95], v[214:215], v[84:85], v[94:95] op_sel:[0,1,0] op_sel_hi:[1,1,1]\n\t" \
  "ds_read_b128 v[180:183], v21 offset:5312\n\t" \
  "v_pk_fma_f32 v[92:93], v[216:217], v[86:87], v[92:93] op_sel:[0,0,0] op_sel_hi:[1,0,1]\n\t" \
  "v_pk_fma_f32 v[94:95], v[218:219], v[86:87], v[94:95] op_sel:[0,0,0] op_sel_hi:[1,0,1]\n\t" \
  "ds_read_b128 v[184:187], v21 offset:5280\n\t" \
  "v_pk_fma_f32 v[92:93], v[220:221], v[86:87], v[92:93] op_sel:[0,1,0] op_sel_hi:[1,1,1]\n\t" \
  "v_pk_fma_f32 v[94:95], v[222:223], v[86:87], v[94:95] op_sel:[0,1,0] op_sel_hi:[1,1,1]\n\t" \
  "ds_read_b128 v[188:191], v21 offset:5248\n\t" \
  "v_pk_fma_f32 v[92:93], v[224:225], v[88:89], v[92:93] op_sel:[0,0,0] op_sel_hi:[1,0,1]\n\t" \
  "v_pk_fma_f32 v[94:95], v[226:227], v[88:89], v[94:95] op_sel:[0,0,0] op_sel_hi:[1,0,1]\n\t" \
  "v_pk_fma_f32 v[92:93], v[228:229], v[88:89], v[92:93] op_sel:[0,1,0] op_sel_hi:[1,1,1]\n\t" \
  "ds_read_b128 v[192:195], v21 offset:5216\n\t" \
  "v_pk_fma_f32 v[94:95], v[230:231], v[88:89], v[94:95] op_sel:[0,1,0] op_sel_hi:[1,1,1]\n\t" \
  "v_pk_fma_f32 v[92:93], v[232:233], v[90:91], v[92:93] op_sel:[0,0,0] op_sel_hi:[1,0,1]\n\t" \
  "ds_read_b128 v[196:199], v21 offset:5184\n\t" \
  "v_pk_fma_f32 v[94:95], v[234:235], v[90:91], v[94:95] op_sel:[0,0,0] op_sel_hi:[1,0,1]\n\t" \
  "v_pk_fma_f32 v[92:93], v[236:237], v[90:91], v[92:93] op_sel:[0,1,0] op_sel_hi:[1,1,1]\n\t" \
  "v_pk_fma_f32 v[94:95], v[238:239], v[90:91], v[94:95] op_sel:[0,1,0] op_sel_hi:[1,1,1]\n\t" \
  "ds_read_b128 v[200:203], v21 offset:5152\n\t" \
  "v_cvt_pk_bf16_f32 v116, v92, v93\n\t" \
  "v_cvt_pk_bf16_f32 v117, v94, v95\n\t" \
  "ds_read_b128 v[204:207], v21 offset:5120\n\t" \
  "s_waitcnt lgkmcnt(8)\n\t" \
  "v_pk_mul_f32 v[96:97], v[144:145], v[84:85] op_sel_hi:[1,0]\n\t" \
  "ds_read_b128 v[208:211], v21 offset:7648\n\t" \
  "v_pk_mul_f32 v[98:99], v[146:147], v[84:85] op_sel_hi:[1,0]\n\t" \
  "v_pk_fma_f32 v[96:97], v[148:149], v[84:85], v[96:97] op_sel:[0,1,0] op_sel_hi:[1,1,1]\n\t" \
  "v_pk_fma_f32 v[98:99], v[150:151], v[84:85], v[98:99] op_sel:[0,1,0] op_sel_hi:[1,1,1]\n\t" \
  "ds_read_b128 v[212:215], v21 offset:7616\n\t" \
  "v_pk_fma_f32 v[96:97], v[152:153], v[86:87], v[96:97] op_sel:[0,0,0] op_sel_hi:[1,0,1]\n\t" \
  "v_pk_fma_f32 v[98:99], v[154:155], v[86:87], v[98:99] op_sel:[0,0,0] op_sel_hi:[1,0,1]\n\t" \
  "ds_read_b128 v[216:219], v21 offset:7584\n\t" \
  "v_pk_fma_f32 v[96:97], v[156:157], v[86:87], v[96:97] op_sel:[0,1,0] op_sel_hi:[1,1,1]\n\t" \
  "v_pk_fma_f32 v[98:99], v[158:159], v[86:87], v[98:99] op_sel:[0,1,0] op_sel_hi:[1,1,1]\n\t" \
  "ds_read_b128 v[220:223], v21 offset:7552\n\t" \
  "v_pk_fma_f32 v[96:97], v[160:161], v[88:89], v[96:97] op_sel:[0,0,0] op_sel_hi:[1,0,1]\n\t" \
  "v_pk_fma_f32 v[98:99], v[162:163], v[88:89], v[98:99] op_sel:[0,0,0] op_sel_hi:[1,0,1]\n\t" \
  "v_pk_fma_f32 v[96:97], v[164:165], v[88:89], v[96:97] op_sel:[0,1,0] op_sel_hi:[1,1,1]\n\t" \
  "ds_read_b128 v[224:227], v21 offset:7520\n\t" \
  "v_pk_fma_f32 v[98:99], v[166:167], v[88:89], v[98:99] op_sel:[0,1,0] op_sel_hi:[1,1,1]\n\t" \
  "v_pk_fma_f32 v[96:97], v[168:169], v[90:91], v[96:97] op_sel:[0,0,0] op_sel_hi:[1,0,1]\n\t" \
  "ds_read_b128 v[228:231], v21 offset:7488\n\t" \
  "v_pk_fma_f32 v[98:99], v[170:171], v[90:91], v[98:99] op_sel:[0,0,0] op_sel_hi:[1,0,1]\n\t" \
  "s_add_u32 s94, s20, 65536\n\t" \
  "s_addc_u32 s95, s21, 0\n\t" \
  "s_add_u32 m0, s83, 81920\n\t" \
  "s_nop 0\n\t" \
  "global_load_lds_dwordx4 v64, s[94:95]\n\t" \
  "v_pk_fma_f32 v[96:97], v[172:173], v[90:91], v[96:97] op_sel:[0,1,0] op_sel_hi:[1,1,1]\n\t" \
  "v_pk_fma_f32 v[98:99], v[174:175], v[90:91], v[98:99] op_sel:[0,1,0] op_sel_hi:[1,1,1]\n\t" \
  "global_load_dwordx2 v[142:143], v13, s[44:45] sc1\n\t" \
  "ds_read_b128 v[232:235], v21 offset:7456\n\t" \
  "v_cvt_pk_bf16_f32 v118, v96, v97\n\t" \
  "v_cvt_pk_bf16_f32 v119, v98, v99\n\t" \
  "ds_read_b128 v[236:239], v21 offset:7424\n\t" \
  "s_waitcnt lgkmcnt(8)\n\t" \
  "v_pk_mul_f32 v[100:101], v[176:177], v[84:85] op_sel_hi:[1,0]\n\t" \
  "v_pk_mul_f32 v[102:103], v[178:179], v[84:85] op_sel_hi:[1,0]\n\t" \
  "v_pk_fma_f32 v[100:101], v[180:181], v[84:85], v[100:101] op_sel:[0,1,0] op_sel_hi:[1,1,1]\n\t" \
  "v_pk_fma_f32 v[102:103], v[182:183], v[84:85], v[102:103] op_sel:[0,1,0] op_sel_hi:[1,1,1]\n\t" \
  "v_pk_fma_f32 v[100:101], v[184:185], v[86:87], v[100:101] op_sel:[0,0,0] op_sel_hi:[1,0,1]\n\t" \
  "v_pk_fma_f32 v[102:103], v[186:187], v[86:87], v[102:103] op_sel:[0,0,0] op_sel_hi:[1,0,1]\n\t" \
  "v_pk_fma_f32 v[100:101], v[188:189], v[86:87], v[100:101] op_sel:[0,1,0] op_sel_hi:[1,1,1]\n\t" \
  "v_pk_fma_f32 v[102:103], v[190:191], v[86:87], v[102:103] op_sel:[0,1,0] op_sel_hi:[1,1,1]\n\t" \
  "v_pk_fma_f32 v[100:101], v[192:193], v[88:89], v[100:101] op_sel:[0,0,0] op_sel_hi:[1,0,1]\n\t" \
  "v_pk_fma_f32 v[102:103], v[194:195], v[88:89], v[102:103] op_sel:[0,0,0] op_sel_hi:[1,0,1]\n\t" \
  "v_pk_fma_f32 v[100:101], v[196:197], v[88:89], v[100:101] op_sel:[0,1,0] op_sel_hi:[1,1,1]\n\t" \
  "v_pk_fma_f32 v[102:103], v[198:199], v[88:89], v[102:103] op_sel:[0,1,0] op_sel_hi:[1,1,1]\n\t" \
  "v_pk_fma_f32 v[100:101], v[200:201], v[90:91], v[100:101] op_sel:[0,0,0] op_sel_hi:[1,0,1]\n\t" \
  "v_pk_fma_f32 v[102:103], v[202:203], v[90:91], v[102:103] op_sel:[0,0,0] op_sel_hi:[1,0,1]\n\t" \
  "v_pk_fma_f32 v[100:101], v[204:205], v[90:91], v[100:101] op_sel:[0,1,0] op_sel_hi:[1,1,1]\n\t" \
  "v_pk_fma_f32 v[102:103], v[206:207], v[90:91], v[102:103] op_sel:[0,1,0] op_sel_hi:[1,1,1]\n\t" \
  "v_cvt_pk_bf16_f32 v120, v100, v101\n\t" \
  "v_cvt_pk_bf16_f32 v121, v102, v103\n\t" \
  "s_waitcnt lgkmcnt(0)\n\t" \
  "v_pk_mul_f32 v[104:105], v[208:209], v[84:85] op_sel_hi:[1,0]\n\t" \
  "v_pk_mul_f32 v[106:107], v[210:211], v[84:85] op_sel_hi:[1,0]\n\t" \
  "v_pk_fma_f32 v[104:105], v[212:213], v[84:85], v[104:105] op_sel:[0,1,0] op_sel_hi:[1,1,1]\n\t" \
  "v_pk_fma_f32 v[106:107], v[214:215], v[84:85], v[106:107] op_sel:[0,1,0] op_sel_hi:[1,1,1]\n\t" \
  "v_pk_fma_f32 v[104:105], v[216:217], v[86:87], v[104:105] op_sel:[0,0,0] op_sel_hi:[1,0,1]\n\t" \
  "v_pk_fma_f32 v[106:107], v[218:219], v[86:87], v[106:107] op_sel:[0,0,0] op_sel_hi:[1,0,1]\n\t" \
  "v_pk_fma_f32 v[104:105], v[220:221], v[86:87], v[104:105] op_sel:[0,1,0] op_sel_hi:[1,1,1]\n\t" \
  "v_pk_fma_f32 v[106:107], v[222:223], v[86:87], v[106:107] op_sel:[0,1,0] op_sel_hi:[1,1,1]\n\t" \
  "v_pk_fma_f32 v[104:105], v[224:225], v[88:89], v[104:105] op_sel:[0,0,0] op_sel_hi:[1,0,1]\n\t" \
  "v_pk_fma_f32 v[106:107], v[226:227], v[88:89], v[106:107] op_sel:[0,0,0] op_sel_hi:[1,0,1]\n\t" \
  "v_pk_fma_f32 v[104:105], v[228:229], v[88:89], v[104:105] op_sel:[0,1,0] op_sel_hi:[1,1,1]\n\t" \
  "v_pk_fma_f32 v[106:107], v[230:231], v[88:89], v[106:107] op_sel:[0,1,0] op_sel_hi:[1,1,1]\n\t" \
  "v_pk_fma_f32 v[104:105], v[232:233], v[90:91], v[104:105] op_sel:[0,0,0] op_sel_hi:[1,0,1]\n\t" \
  "v_pk_fma_f32 v[106:107], v[234:235], v[90:91], v[106:107] op_sel:[0,0,0] op_sel_hi:[1,0,1]\n\t" \
  "v_pk_fma_f32 v[104:105], v[236:237], v[90:91], v[104:105] op_sel:[0,1,0] op_sel_hi:[1,1,1]\n\t" \
  "v_pk_fma_f32 v[106:107], v[238:239], v[90:91], v[106:107] op_sel:[0,1,0] op_sel_hi:[1,1,1]\n\t" \
  "v_cvt_pk_bf16_f32 v122, v104, v105\n\t" \
  "v_cvt_pk_bf16_f32 v123, v106, v107\n\t" \
  "v_permlane32_swap_b32 v116, v120\n\t" \
  "v_permlane32_swap_b32 v117, v121\n\t" \
  "v_permlane32_swap_b32 v118, v122\n\t" \
  "v_permlane32_swap_b32 v119, v123\n\t" \
  "global_store_dwordx4 v0, v[116:119], s[72:73] offset:0 sc1\n\t" \
  "global_store_dwordx4 v0, v[120:123], s[72:73] offset:1024 sc1\n\t" \
  "global_load_lds_dwordx4 v65, s[94:95] offset:1024\n\t" \
  "s_waitcnt vmcnt(3)\n\t" \
  "s_or_b32 s12, s12, 2\n\t" \
  "v_cmp_ge_u64 s[58:59], s[68:69], v[142:143]\n\t" \
  "v_cmp_le_u64 vcc, s[12:13], v[142:143]\n\t" \
  "s_nop 1\n\t" \
  "s_and_b32 s58, s58, vcc_lo\n\t" \
  "s_and_b32 s12, s12, -8\n\t" \
  "s_and_b32 s58, s58, 240\n\t" \
  "s_cmp_eq_u32 s58, 240\n\t" \
  "s_cbranch_scc0 P0AS%=\n\t" \
  "P0AB%=:\n\t" \
  "s_lshl_b32 s33, s19, 12\n\t" \
  "s_add_u32 s33, s33, 0\n\t" \
  "s_add_u32 s76, s74, s33\n\t" \
  "s_addc_u32 s77, s75, 0\n\t" \
  "global_load_dwordx4 a[0:3], v0, s[76:77] offset:0 sc1\n\t" \
  "global_load_dwordx4 a[4:7], v0, s[76:77] offset:1024 sc1\n\t" \
  "global_load_dwordx4 a[8:11], v0, s[76:77] offset:2048 sc1\n\t" \
  "global_load_dwordx4 a[12:15], v0, s[76:77] offset:3072 sc1\n\t" \
  "s_lshl_b32 s33, s19, 12\n\t" \
  "s_add_u32 s33, s33, 8192\n\t" \
  "s_add_u32 s76, s74, s33\n\t" \
  "s_addc_u32 s77, s75, 0\n\t" \
  "global_load_dwordx4 a[16:19], v0, s[76:77] offset:0 sc1\n\t" \
  "global_load_dwordx4 a[20:23], v0, s[76:77] offset:1024 sc1\n\t" \
  "global_load_dwordx4 a[24:27], v0, s[76:77] offset:2048 sc1\n\t" \
  "global_load_dwordx4 a[28:31], v0, s[76:77] offset:3072 sc1\n\t" \
  "s_xor_b32 s33, s19, 1\n\t" \
  "s_lshl_b32 s33, s33, 12\n\t" \
  "s_add_u32 s33, s33, 0\n\t" \
  "s_add_u32 s76, s74, s33\n\t" \
  "s_addc_u32 s77, s75, 0\n\t" \
  "global_load_dwordx4 a[32:35], v0, s[76:77] offset:0 sc1\n\t" \
  "global_load_dwordx4 a[36:39], v0, s[76:77] offset:1024 sc1\n\t" \
  "global_load_dwordx4 a[40:43], v0, s[76:77] offset:2048 sc1\n\t" \
  "global_load_dwordx4 a[44:47], v0, s[76:77] offset:3072 sc1\n\t" \
  "s_xor_b32 s33, s19, 1\n\t" \
  "s_lshl_b32 s33, s33, 12\n\t" \
  "s_add_u32 s33, s33, 8192\n\t" \
  "s_add_u32 s76, s74, s33\n\t" \
  "s_addc_u32 s77, s75, 0\n\t" \
  "global_load_dwordx4 a[48:51], v0, s[76:77] offset:0 sc1\n\t" \
  "global_load_dwordx4 a[52:55], v0, s[76:77] offset:1024 sc1\n\t" \
  "global_load_dwordx4 a[56:59], v0, s[76:77] offset:2048 sc1\n\t" \
  "global_load_dwordx4 a[60:63], v0, s[76:77] offset:3072 sc1\n\t" \
  "v_lshrrev_b32 v57, 9, v0\n\t" \
  "v_bfe_u32 v59, v0, 8, 1\n\t" \
  "s_add_u32 s33, s97, 1\n\t" \
  "s_and_b32 s33, s33, 3\n\t" \
  "s_lshl2_add_u32 s33, s33, s26\n\t" \
  "s_lshl_b32 s34, s33, 11\n\t" \
  "s_add_u32 s34, s34, 11534336\n\t" \
  "v_add_u32 v17, s34, v0\n\t" \
  "s_lshl_b32 s33, s33, 6\n\t" \
  "s_add_u32 s16, s33, 10485760\n\t" \
  "s_add_u32 s33, s97, 2\n\t" \
  "s_and_b32 s33, s33, 3\n\t" \
  "s_lshl2_add_u32 s33, s33, s26\n\t" \
  "s_lshl_b32 s34, s33, 11\n\t" \
  "s_add_u32 s34, s34, 13631488\n\t" \
  "v_add_u32 v18, s34, v0\n\t" \
  "s_lshl_b32 s33, s33, 6\n\t" \
  "s_add_u32 s17, s33, 10485768\n\t" \
  "s_add_u32 s33, s97, 3\n\t" \
  "s_and_b32 s33, s33, 3\n\t" \
  "s_lshl2_add_u32 s33, s33, s26\n\t" \
  "s_lshl_b32 s34, s33, 11\n\t" \
  "s_add_u32 s34, s34, 15728640\n\t" \
  "v_add_u32 v19, s34, v0\n\t" \
  "s_lshl_b32 s33, s33, 6\n\t" \
  "s_add_u32 s18, s33, 10485776\n\t" \
  "s_add_u32 s33, s97, 4\n\t" \
  "s_and_b32 s33, s33, 3\n\t" \
  "s_lshl2_add_u32 s33, s33, s26\n\t" \
  "s_lshl_b32 s34, s33, 11\n\t" \
  "s_add_u32 s34, s34, 11534336\n\t" \
  "v_add_u32 v20, s34, v0\n\t" \
  "s_lshl2_add_u32 s33, s87, s86\n\t" \
  "s_lshl_b32 s33, s33, 15\n\t" \
  "s_lshl_b32 s34, s98, 8\n\t" \
  "s_add_u32 s33, s33, s34\n\t" \
  "s_lshl_b32 s34, s97, 6\n\t" \
  "s_add_u32 s33, s33, s34\n\t" \
  "s_add_u32 s33, s33, 4096\n\t" \
  "v_and_b32 v58, 0xf0, v0\n\t" \
  "v_lshl_add_u32 v14, v58, 6, s33\n\t" \
  "v_lshl_add_u32 v14, v57, 4, v14\n\t" \
  "v_lshl_add_u32 v14, v59, 5, v14\n\t" \
  "s_sub_u32 s80, s80, 4096\n\t" \
  "s_subb_u32 s81, s81, 0\n\t" \
  "v_add_u32 v61, 16384, v14\n\t" \
  "s_lshl_b32 s33, s86, 12\n\t" \
  "s_add_u32 s33, s33, 196608\n\t" \
  "v_bfe_u32 v58, v0, 4, 3\n\t" \
  "v_lshl_add_u32 v9, v58, 3, s33\n\t" \
  "v_and_b32 v58, 0x380, v0\n\t" \
  "v_add_u32 v9, v9, v58\n\t" \
  "v_bfe_u32 v57, v0, 4, 2\n\t" \
  "v_min_u32 v57, 2, v57\n\t" \
  "v_lshl_add_u32 v57, v57, 3, s27\n\t" \
  "v_add_u32 v58, 2097152, v20\n\t" \
  "v_add_u32 v59, 4194304, v20\n\t" \
  "s_mov_b32 s30, 0x3f80\n\t" \
  "s_mov_b32 s31, 0x3f800000\n\t" \
  "s_waitcnt vmcnt(17)\n\t" \
  "v_or_b32 v138, 4, v138\n\t" \
  "s_mov_b64 exec, 1\n\t" \
  "global_store_dwordx2 v12, v[138:139], s[44:45] sc1\n\t" \
  "s_mov_b64 exec, -1\n\t" \
  "s_nop 0\n\t" \
  "v_and_b32 v138, -8, v138\n\t" \
  "v_or_b32 v138, 4, v138\n\t" \
  "s_waitcnt vmcnt(17)\n\t" \
  "s_waitcnt lgkmcnt(0)\n\t" \
  "s_barrier\n\t" \
  "ds_read_b64_tr_b16 v[144:145], v1 offset:0\n\t" \
  "ds_read_b64_tr_b16 v[146:147], v1 offset:2048\n\t" \
  "ds_read_b64_tr_b16 v[148:149], v2 offset:0\n\t" \
  "ds_read_b64_tr_b16 v[150:151], v2 offset:2048\n\t" \
  "ds_read_b64_tr_b16 v[152:153], v3 offset:0\n\t" \
  "ds_read_b64_tr_b16 v[154:155], v3 offset:2048\n\t" \
  "ds_read_b64_tr_b16 v[156:157], v1 offset:4096\n\t" \
  "ds_read_b64_tr_b16 v[158:159], v1 offset:6144\n\t" \
  "ds_read_b64_tr_b16 v[160:161], v2 offset:4096\n\t" \
  "ds_read_b64_tr_b16 v[162:163], v2 offset:6144\n\t" \
  "ds_read_b64_tr_b16 v[164:165], v3 offset:4096\n\t" \
  "ds_read_b64_tr_b16 v[166:167], v3 offset:6144\n\t" \
  "s_waitcnt lgkmcnt(8)\n\t" \
  "s_waitcnt vmcnt(16)\n\t" \
  "v_mfma_f32_32x32x16_bf16 v[72:87], v[144:147], a[0:3], 0\n\t" \
  "ds_read_b64_tr_b16 v[168:169], v1 offset:8192\n\t" \
  "ds_read_b64_tr_b16 v[170:171], v1 offset:10240\n\t" \
  "v_mfma_f32_32x32x16_bf16 v[88:103], v[148:151], a[0:3], 0\n\t" \
  "ds_read_b64_tr_b16 v[172:173], v2 offset:8192\n\t" \
  "ds_read_b64_tr_b16 v[174:175], v2 offset:10240\n\t" \
  "s_waitcnt lgkmcnt(8)\n\t" \
  "v_mfma_f32_32x32x16_bf16 v[104:119], v[152:155], a[0:3], 0\n\t" \
  "ds_read_b64_tr_b16 v[176:177], v3 offset:8192\n\t" \
  "ds_read_b64_tr_b16 v[178:179], v3 offset:10240\n\t" \
  "s_waitcnt vmcnt(15)\n\t" \
  "v_mfma_f32_32x32x16_bf16 v[72:87], v[156:159], a[4:7], v[72:87]\n\t" \
  "ds_read_b64_tr_b16 v[180:181], v1 offset:12288\n\t" \
  "ds_read_b64_tr_b16 v[182:183], v1 offset:14336\n\t" \
  "s_add_u32 s92, s20, 163840\n\t" \
  "s_addc_u32 s93, s21, 0\n\t" \
  "s_add_u32 m0, s83, 24576\n\t" \
  "s_nop 0\n\t" \
  "global_load_lds_dwordx4 v64, s[92:93]\n\t" \
  "s_waitcnt lgkmcnt(8)\n\t" \
  "v_mfma_f32_32x32x16_bf16 v[88:103], v[160:163], a[4:7], v[88:103]\n\t" \
  "ds_read_b64_tr_b16 v[184:185], v2 offset:12288\n\t" \
  "ds_read_b64_tr_b16 v[186:187], v2 offset:14336\n\t" \
  "v_mfma_f32_32x32x16_bf16 v[104:119], v[164:167], a[4:7], v[104:119]\n\t" \
  "ds_read_b64_tr_b16 v[188:189], v3 offset:12288\n\t" \
  "ds_read_b64_tr_b16 v[190:191], v3 offset:14336\n\t" \
  "global_load_lds_dwordx4 v65, s[92:93] offset:1024\n\t" \
  "s_waitcnt lgkmcnt(8)\n\t" \
  "s_waitcnt vmcnt(16)\n\t" \
  "v_mfma_f32_32x32x16_bf16 v[72:87], v[168:171], a[8:11], v[72:87]\n\t" \
  "ds_read_b64_tr_b16 v[144:145], v1 offset:16384\n\t" \
  "ds_read_b64_tr_b16 v[146:147], v1 offset:18432\n\t" \
  "v_mfma_f32_32x32x16_bf16 v[88:103], v[172:175], a[8:11], v[88:103]\n\t" \
  "ds_read_b64_tr_b16 v[148:149], v2 offset:16384\n\t" \
  "ds_read_b64_tr_b16 v[150:151], v2 offset:18432\n\t" \
  "s_add_u32 s94, s20, 196608\n\t" \
  "s_addc_u32 s95, s21, 0\n\t" \
  "s_add_u32 m0, s83, 32768\n\t" \
  "s_nop 0\n\t" \
  "global_load_lds_dwordx4 v64, s[94:95]\n\t" \
  "s_waitcnt lgkmcnt(8)\n\t" \
  "v_mfma_f32_32x32x16_bf16 v[104:119], v[176:179], a[8:11], v[104:119]\n\t" \
  "ds_read_b64_tr_b16 v[152:153], v3 offset:16384\n\t" \
  "ds_read_b64_tr_b16 v[154:155], v3 offset:18432\n\t" \
  "s_waitcnt vmcnt(16)\n\t" \
  "v_mfma_f32_32x32x16_bf16 v[72:87], v[180:183], a[12:15], v[72:87]\n\t" \
  "ds_read_b64_tr_b16 v[156:157], v1 offset:20480\n\t" \
  "ds_read_b64_tr_b16 v[158:159], v1 offset:22528\n\t" \
  "global_load_lds_dwordx4 v65, s[94:95] offset:1024\n\t" \
  "s_waitcnt lgkmcnt(8)\n\t" \
  "v_mfma_f32_32x32x16_bf16 v[88:103], v[184:187], a[12:15], v[88:103]\n\t" \
  "ds_read_b64_tr_b16 v[160:161], v2 offset:20480\n\t" \
  "ds_read_b64_tr_b16 v[162:163], v2 offset:22528\n\t" \
  "v_mfma_f32_32x32x16_bf16 v[104:119], v[188:191], a[12:15], v[104:119]\n\t" \
  "ds_read_b64_tr_b16 v[164:165], v3 offset:20480\n\t" \
  "ds_read_b64_tr_b16 v[166:167], v3 offset:22528\n\t" \
  "s_add_u32 s92, s62, 0\n\t" \
  "s_addc_u32 s93, s63, 0\n\t" \
  "s_add_u32 m0, s83, 40960\n\t" \
  "s_nop 0\n\t" \
  "global_load_lds_dwordx4 v64, s[92:93]\n\t" \
  "s_waitcnt lgkmcnt(8)\n\t" \
  "s_waitcnt vmcnt(17)\n\t" \
  "v_mfma_f32_32x32x16_bf16 v[72:87], v[144:147], a[16:19], v[72:87]\n\t" \
  "ds_read_b64_tr_b16 v[168:169], v1 offset:24576\n\t" \
  "ds_read_b64_tr_b16 v[170:171], v1 offset:26624\n\t" \
  "v_mfma_f32_32x32x16_bf16 v[88:103], v[148:151], a[16:19], v[88:103]\n\t" \
  "ds_read_b64_tr_b16 v[172:173], v2 offset:24576\n\t" \
  "ds_read_b64_tr_b16 v[174:175], v2 offset:26624\n\t" \
  "global_load_lds_dwordx4 v65, s[92:93] offset:1024\n\t" \
  "s_waitcnt lgkmcnt(8)\n\t" \
  "v_mfma_f32_32x32x16_bf16 v[104:119], v[152:155], a[16:19], v[104:119]\n\t" \
  "ds_read_b64_tr_b16 v[176:177], v3 offset:24576\n\t" \
  "ds_read_b64_tr_b16 v[178:179], v3 offset:26624\n\t" \
  "s_waitcnt vmcnt(17)\n\t" \
  "v_mfma_f32_32x32x16_bf16 v[72:87], v[156:159], a[20:23], v[72:87]\n\t" \
  "ds_read_b64_tr_b16 v[180:181], v1 offset:28672\n\t" \
  "ds_read_b64_tr_b16 v[182:183], v1 offset:30720\n\t" \
  "s_add_u32 s94, s62, 32768\n\t" \
  "s_addc_u32 s95, s63, 0\n\t" \
  "s_add_u32 m0, s83, 49152\n\t" \
  "s_nop 0\n\t" \
  "global_load_lds_dwordx4 v64, s[94:95]\n\t" \
  "s_waitcnt lgkmcnt(8)\n\t" \
  "v_mfma_f32_32x32x16_bf16 v[88:103], v[160:163], a[20:23], v[88:103]\n\t" \
  "ds_read_b64_tr_b16 v[184:185], v2 offset:28672\n\t" \
  "ds_read_b64_tr_b16 v[186:187], v2 offset:30720\n\t" \
  "v_mfma_f32_32x32x16_bf16 v[104:119], v[164:167], a[20:23], v[104:119]\n\t" \
  "ds_read_b64_tr_b16 v[188:189], v3 offset:28672\n\t" \
  "ds_read_b64_tr_b16 v[190:191], v3 offset:30720\n\t" \
  "global_load_lds_dwordx4 v65, s[94:95] offset:1024\n\t" \
  "s_waitcnt lgkmcnt(8)\n\t" \
  "s_waitcnt vmcnt(18)\n\t" \
  "v_mfma_f32_32x32x16_bf16 v[72:87], v[168:171], a[24:27], v[72:87]\n\t" \
  "ds_read_b64_tr_b16 v[144:145], v1 offset:32768\n\t" \
  "ds_read_b64_tr_b16 v[146:147], v1 offset:34816\n\t" \
  "global_load_dwordx2 v[142:143], v13, s[44:45] sc1\n\t" \
  "v_mfma_f32_32x32x16_bf16 v[88:103], v[172:175], a[24:27], v[88:103]\n\t" \
  "ds_read_b64_tr_b16 v[148:149], v2 offset:32768\n\t" \
  "ds_read_b64_tr_b16 v[150:151], v2 offset:34816\n\t" \
  "s_add_u32 s92, s62, 65536\n\t" \
  "s_addc_u32 s93, s63, 0\n\t" \
  "s_add_u32 m0, s83, 0\n\t" \
  "s_nop 0\n\t" \
  "global_load_lds_dwordx4 v64, s[92:93]\n\t" \
  "s_waitcnt lgkmcnt(8)\n\t" \
  "v_mfma_f32_32x32x16_bf16 v[104:119], v[176:179], a[24:27], v[104:119]\n\t" \
  "ds_read_b64_tr_b16 v[152:153], v3 offset:32768\n\t" \
  "ds_read_b64_tr_b16 v[154:155], v3 offset:34816\n\t" \
  "s_waitcnt vmcnt(19)\n\t" \
  "v_mfma_f32_32x32x16_bf16 v[72:87], v[180:183], a[28:31], v[72:87]\n\t" \
  "ds_read_b64_tr_b16 v[156:157], v1 offset:36864\n\t" \
  "ds_read_b64_tr_b16 v[158:159], v1 offset:38912\n\t" \
  "global_load_lds_dwordx4 v65, s[92:93] offset:1024\n\t" \
  "s_waitcnt lgkmcnt(8)\n\t" \
  "v_mfma_f32_32x32x16_bf16 v[88:103], v[184:187], a[28:31], v[88:103]\n\t" \
  "ds_read_b64_tr_b16 v[160:161], v2 offset:36864\n\t" \
  "ds_read_b64_tr_b16 v[162:163], v2 offset:38912\n\t" \
  "v_mfma_f32_32x32x16_bf16 v[104:119], v[188:191], a[28:31], v[104:119]\n\t" \
  "ds_read_b64_tr_b16 v[164:165], v3 offset:36864\n\t" \
  "ds_read_b64_tr_b16 v[166:167], v3 offset:38912\n\t" \
  "s_add_u32 s94, s62, 98304\n\t" \
  "s_addc_u32 s95, s63, 0\n\t" \
  "s_add_u32 m0, s83, 8192\n\t" \
  "s_nop 0\n\t" \
  "global_load_lds_dwordx4 v64, s[94:95]\n\t" \
  "s_waitcnt lgkmcnt(8)\n\t" \
  "s_waitcnt vmcnt(20)\n\t" \
  "v_mfma_f32_32x32x16_bf16 v[72:87], v[144:147], a[32:35], v[72:87]\n\t" \
  "ds_read_b64_tr_b16 v[168:169], v1 offset:40960\n\t" \
  "ds_read_b64_tr_b16 v[170:171], v1 offset:43008\n\t" \
  "v_mfma_f32_32x32x16_bf16 v[88:103], v[148:151], a[32:35], v[88:103]\n\t" \
  "ds_read_b64_tr_b16 v[172:173], v2 offset:40960\n\t" \
  "ds_read_b64_tr_b16 v[174:175], v2 offset:43008\n\t" \
  "global_load_lds_dwordx4 v65, s[94:95] offset:1024\n\t" \
  "s_waitcnt lgkmcnt(8)\n\t" \
  "v_mfma_f32_32x32x16_bf16 v[104:119], v[152:155], a[32:35], v[104:119]\n\t" \
  "ds_read_b64_tr_b16 v[176:177], v3 offset:40960\n\t" \
  "ds_read_b64_tr_b16 v[178:179], v3 offset:43008\n\t" \
  "s_waitcnt vmcnt(20)\n\t" \
  "v_mfma_f32_32x32x16_bf16 v[72:87], v[156:159], a[36:39], v[72:87]\n\t" \
  "ds_read_b64_tr_b16 v[180:181], v1 offset:45056\n\t" \
  "ds_read_b64_tr_b16 v[182:183], v1 offset:47104\n\t" \
  "s_waitcnt lgkmcnt(8)\n\t" \
  "v_mfma_f32_32x32x16_bf16 v[88:103], v[160:163], a[36:39], v[88:103]\n\t" \
  "ds_read_b64_tr_b16 v[184:185], v2 offset:45056\n\t" \
  "ds_read_b64_tr_b16 v[186:187], v2 offset:47104\n\t" \
  "v_mfma_f32_32x32x16_bf16 v[104:119], v[164:167], a[36:39], v[104:119]\n\t" \
  "ds_read_b64_tr_b16 v[188:189], v3 offset:45056\n\t" \
  "ds_read_b64_tr_b16 v[190:191], v3 offset:47104\n\t" \
  "s_waitcnt lgkmcnt(8)\n\t" \
  "s_waitcnt vmcnt(19)\n\t" \
  "v_mfma_f32_32x32x16_bf16 v[72:87], v[168:171], a[40:43], v[72:87]\n\t" \
  "ds_read_b64_tr_b16 v[144:145], v1 offset:49152\n\t" \
  "ds_read_b64_tr_b16 v[146:147], v1 offset:51200\n\t" \
  "s_waitcnt vmcnt(4)\n\t" \
  "s_or_b32 s12, s12, 4\n\t" \
  "v_cmp_ge_u64 s[58:59], s[68:69], v[142:143]\n\t" \
  "v_cmp_le_u64 vcc, s[12:13], v[142:143]\n\t" \
  "s_nop 1\n\t" \
  "s_and_b32 s58, s58, vcc_lo\n\t" \
  "s_and_b32 s12, s12, -8\n\t" \
  "s_and_b32 s58, s58, 240\n\t" \
  "s_cmp_eq_u32 s58, 240\n\t" \
  "s_cbranch_scc0 P8AS%=\n\t" \
  "P8AB%=:\n\t" \
  "v_mfma_f32_32x32x16_bf16 v[88:103], v[172:175], a[40:43], v[88:103]\n\t" \
  "ds_read_b64_tr_b16 v[148:149], v2 offset:49152\n\t" \
  "ds_read_b64_tr_b16 v[150:151], v2 offset:51200\n\t" \
  "s_lshl_b32 s33, s19, 12\n\t" \
  "s_add_u32 s33, s33, 16384\n\t" \
  "s_add_u32 s76, s74, s33\n\t" \
  "s_addc_u32 s77, s75, 0\n\t" \
  "global_load_dwordx4 a[64:67], v0, s[76:77] offset:0 sc1\n\t" \
  "global_load_dwordx4 a[68:71], v0, s[76:77] offset:1024 sc1\n\t" \
  "s_waitcnt lgkmcnt(8)\n\t" \
  "v_mfma_f32_32x32x16_bf16 v[104:119], v[176:179], a[40:43], v[104:119]\n\t" \
  "ds_read_b64_tr_b16 v[152:153], v3 offset:49152\n\t" \
  "ds_read_b64_tr_b16 v[154:155], v3 offset:51200\n\t" \
  "global_load_dwordx4 a[72:75], v0, s[76:77] offset:2048 sc1\n\t" \
  "global_load_dwordx4 a[76:79], v0, s[76:77] offset:3072 sc1\n\t" \
  "v_mfma_f32_32x32x16_bf16 v[72:87], v[180:183], a[44:47], v[72:87]\n\t" \
  "ds_read_b64_tr_b16 v[156:157], v1 offset:53248\n\t" \
  "ds_read_b64_tr_b16 v[158:159], v1 offset:55296\n\t" \
  "s_lshl_b32 s33, s19, 11\n\t" \
  "s_add_u32 s33, s33, 24576\n\t" \
  "s_add_u32 s76, s74, s33\n\t" \
  "s_addc_u32 s77, s75, 0\n\t" \
  "global_load_dwordx4 a[80:83], v0, s[76:77] offset:0 sc1\n\t" \
  "global_load_dwordx4 a[84:87], v0, s[76:77] offset:1024 sc1\n\t" \
  "s_waitcnt lgkmcnt(8)\n\t" \
  "v_mfma_f32_32x32x16_bf16 v[88:103], v[184:187], a[44:47], v[88:103]\n\t" \
  "ds_read_b64_tr_b16 v[160:161], v2 offset:53248\n\t" \
  "ds_read_b64_tr_b16 v[162:163], v2 offset:55296\n\t" \
  "s_xor_b32 s33, s19, 1\n\t" \
  "s_lshl_b32 s33, s33, 12\n\t" \
  "s_add_u32 s33, s33, 16384\n\t" \
  "s_add_u32 s76, s74, s33\n\t" \
  "s_addc_u32 s77, s75, 0\n\t" \
  "global_load_dwordx4 a[88:91], v0, s[76:77] offset:0 sc1\n\t" \
  "global_load_dwordx4 a[92:95], v0, s[76:77] offset:1024 sc1\n\t" \
  "v_mfma_f32_32x32x16_bf16 v[104:119], v[188:191], a[44:47], v[104:119]\n\t" \
  "ds_read_b64_tr_b16 v[164:165], v3 offset:53248\n\t" \
  "ds_read_b64_tr_b16 v[166:167], v3 offset:55296\n\t" \
  "global_load_dwordx4 a[96:99], v0, s[76:77] offset:2048 sc1\n\t" \
  "global_load_dwordx4 a[100:103], v0, s[76:77] offset:3072 sc1\n\t" \
  "s_waitcnt lgkmcnt(8)\n\t" \
  "v_mfma_f32_32x32x16_bf16 v[72:87], v[144:147], a[48:51], v[72:87]\n\t" \
  "ds_read_b64_tr_b16 v[168:169], v1 offset:57344\n\t" \
  "ds_read_b64_tr_b16 v[170:171], v1 offset:59392\n\t" \
  "s_xor_b32 s33, s19, 1\n\t" \
  "s_lshl_b32 s33, s33, 11\n\t" \
  "s_add_u32 s33, s33, 24576\n\t" \
  "s_add_u32 s76, s74, s33\n\t" \
  "s_addc_u32 s77, s75, 0\n\t" \
  "global_load_dwordx4 a[104:107], v0, s[76:77] offset:0 sc1\n\t" \
  "global_load_dwordx4 a[108:111], v0, s[76:77] offset:1024 sc1\n\t" \
  "v_mfma_f32_32x32x16_bf16 v[88:103], v[148:151], a[48:51], v[88:103]\n\t" \
  "ds_read_b64_tr_b16 v[172:173], v2 offset:57344\n\t" \
  "ds_read_b64_tr_b16 v[174:175], v2 offset:59392\n\t" \
  "s_waitcnt lgkmcnt(8)\n\t" \
  "v_mfma_f32_32x32x16_bf16 v[104:119], v[152:155], a[48:51], v[104:119]\n\t" \
  "ds_read_b64_tr_b16 v[176:177], v3 offset:57344\n\t" \
  "ds_read_b64_tr_b16 v[178:179], v3 offset:59392\n\t" \
  "v_mfma_f32_32x32x16_bf16 v[72:87], v[156:159], a[52:55], v[72:87]\n\t" \
  "ds_read_b64_tr_b16 v[180:181], v1 offset:61440\n\t" \
  "ds_read_b64_tr_b16 v[182:183], v1 offset:63488\n\t" \
  "s_waitcnt lgkmcnt(8)\n\t" \
  "v_mfma_f32_32x32x16_bf16 v[88:103], v[160:163], a[52:55], v[88:103]\n\t" \
  "ds_read_b64_tr_b16 v[184:185], v2 offset:61440\n\t" \
  "ds_read_b64_tr_b16 v[186:187], v2 offset:63488\n\t" \
  "v_mfma_f32_32x32x16_bf16 v[104:119], v[164:167], a[52:55], v[104:119]\n\t" \
  "ds_read_b64_tr_b16 v[188:189], v3 offset:61440\n\t" \
  "ds_read_b64_tr_b16 v[190:191], v3 offset:63488\n\t" \
  "s_waitcnt lgkmcnt(8)\n\t" \
  "v_mfma_f32_32x32x16_bf16 v[72:87], v[168:171], a[56:59], v[72:87]\n\t" \
  "ds_read_b64_tr_b16 v[144:145], v5 offset:32768\n\t" \
  "ds_read_b64_tr_b16 v[146:147], v5 offset:34816\n\t" \
  "v_mfma_f32_32x32x16_bf16 v[88:103], v[172:175], a[56:59], v[88:103]\n\t" \
  "ds_read_b64_tr_b16 v[148:149], v6 offset:32768\n\t" \
  "ds_read_b64_tr_b16 v[150:151], v6 offset:34816\n\t" \
  "s_waitcnt lgkmcnt(8)\n\t" \
  "v_mfma_f32_32x32x16_bf16 v[104:119], v[176:179], a[56:59], v[104:119]\n\t" \
  "ds_read_b64_tr_b16 v[152:153], v7 offset:32768\n\t" \
  "ds_read_b64_tr_b16 v[154:155], v7 offset:34816\n\t" \
  "v_mfma_f32_32x32x16_bf16 v[72:87], v[180:183], a[60:63], v[72:87]\n\t" \
  "ds_read_b64_tr_b16 v[156:157], v5 offset:36864\n\t" \
  "ds_read_b64_tr_b16 v[158:159], v5 offset:38912\n\t" \
  "s_waitcnt lgkmcnt(8)\n\t" \
  "v_mfma_f32_32x32x16_bf16 v[88:103], v[184:187], a[60:63], v[88:103]\n\t" \
  "ds_read_b64_tr_b16 v[160:161], v6 offset:36864\n\t" \
  "ds_read_b64_tr_b16 v[162:163], v6 offset:38912\n\t" \
  "v_mfma_f32_32x32x16_bf16 v[104:119], v[188:191], a[60:63], v[104:119]\n\t" \
  "ds_read_b64_tr_b16 v[164:165], v7 offset:36864\n\t" \
  "ds_read_b64_tr_b16 v[166:167], v7 offset:38912\n\t" \
  "s_waitcnt lgkmcnt(8)\n\t" \
  "s_waitcnt vmcnt(11)\n\t" \
  "v_mfma_f32_32x32x16_bf16 v[72:87], v[144:147], a[64:67], v[72:87]\n\t" \
  "ds_read_b64_tr_b16 v[168:169], v5 offset:40960\n\t" \
  "ds_read_b64_tr_b16 v[170:171], v5 offset:43008\n\t" \
  "global_load_dwordx2 v[136:137], v13, s[44:45] sc1\n\t" \
  "v_mfma_f32_32x32x16_bf16 v[88:103], v[148:151], a[64:67], v[88:103]\n\t" \
  "ds_read_b64_tr_b16 v[172:173], v6 offset:40960\n\t" \
  "ds_read_b64_tr_b16 v[174:175], v6 offset:43008\n\t" \
  "s_waitcnt lgkmcnt(8)\n\t" \
  "v_mfma_f32_32x32x16_bf16 v[104:119], v[152:155], a[64:67], v[104:119]\n\t" \
  "ds_read_b64_tr_b16 v[176:177], v7 offset:40960\n\t" \
  "ds_read_b64_tr_b16 v[178:179], v7 offset:43008\n\t" \
  "s_waitcnt vmcnt(11)\n\t" \
  "v_mfma_f32_32x32x16_bf16 v[72:87], v[156:159], a[68:71], v[72:87]\n\t" \
  "ds_read_b64_tr_b16 v[180:181], v5 offset:45056\n\t" \
  "ds_read_b64_tr_b16 v[182:183], v5 offset:47104\n\t" \
  "s_waitcnt lgkmcnt(8)\n\t" \
  "v_mfma_f32_32x32x16_bf16 v[88:103], v[160:163], a[68:71], v[88:103]\n\t" \
  "ds_read_b64_tr_b16 v[184:185], v6 offset:45056\n\t" \
  "ds_read_b64_tr_b16 v[186:187], v6 offset:47104\n\t" \
  "v_mfma_f32_32x32x16_bf16 v[104:119], v[164:167], a[68:71], v[104:119]\n\t" \
  "ds_read_b64_tr_b16 v[188:189], v7 offset:45056\n\t" \
  "ds_read_b64_tr_b16 v[190:191], v7 offset:47104\n\t" \
  "s_waitcnt lgkmcnt(8)\n\t" \
  "s_waitcnt vmcnt(10)\n\t" \
  "v_mfma_f32_32x32x16_bf16 v[72:87], v[168:171], a[72:75], v[72:87]\n\t" \
  "ds_read_b64_tr_b16 v[144:145], v5 offset:49152\n\t" \
  "ds_read_b64_tr_b16 v[146:147], v5 offset:51200\n\t" \
  "v_mfma_f32_32x32x16_bf16 v[88:103], v[172:175], a[72:75], v[88:103]\n\t" \
  "ds_read_b64_tr_b16 v[148:149], v6 offset:49152\n\t" \
  "ds_read_b64_tr_b16 v[150:151], v6 offset:51200\n\t" \
  "s_waitcnt lgkmcnt(8)\n\t" \
  "v_mfma_f32_32x32x16_bf16 v[104:119], v[176:179], a[72:75], v[104:119]\n\t" \
  "ds_read_b64_tr_b16 v[152:153], v7 offset:49152\n\t" \
  "ds_read_b64_tr_b16 v[154:155], v7 offset:51200\n\t" \
  "s_waitcnt vmcnt(9)\n\t" \
  "v_mfma_f32_32x32x16_bf16 v[72:87], v[180:183], a[76:79], v[72:87]\n\t" \
  "ds_read_b64_tr_b16 v[156:157], v5 offset:53248\n\t" \
  "ds_read_b64_tr_b16 v[158:159], v5 offset:55296\n\t" \
  "s_waitcnt lgkmcnt(8)\n\t" \
  "v_mfma_f32_32x32x16_bf16 v[88:103], v[184:187], a[76:79], v[88:103]\n\t" \
  "ds_read_b64_tr_b16 v[160:161], v6 offset:53248\n\t" \
  "ds_read_b64_tr_b16 v[162:163], v6 offset:55296\n\t" \
  "v_mfma_f32_32x32x16_bf16 v[104:119], v[188:191], a[76:79], v[104:119]\n\t" \
  "ds_read_b64_tr_b16 v[164:165], v7 offset:53248\n\t" \
  "ds_read_b64_tr_b16 v[166:167], v7 offset:55296\n\t" \
  "s_waitcnt lgkmcnt(8)\n\t" \
  "s_waitcnt vmcnt(8)\n\t" \
  "v_mfma_f32_32x32x16_bf16 v[72:87], v[144:147], a[80:83], v[72:87]\n\t" \
  "ds_read_b64_tr_b16 v[168:169], v5 offset:57344\n\t" \
  "ds_read_b64_tr_b16 v[170:171], v5 offset:59392\n\t" \
  "s_waitcnt vmcnt(0)\n\t" \
  "s_or_b32 s12, s12, 4\n\t" \
  "v_cmp_ge_u64 s[58:59], s[68:69], v[136:137]\n\t" \
  "v_cmp_le_u64 vcc, s[12:13], v[136:137]\n\t" \
  "s_nop 1\n\t" \
  "s_and_b32 s58, s58, vcc_lo\n\t" \
  "s_and_b32 s12, s12, -8\n\t" \
  "s_and_b32 s58, s58, 15\n\t" \
  "s_cmp_eq_u32 s58, 15\n\t" \
  "s_cbranch_scc0 P14AS%=\n\t" \
  "P14AB%=:\n\t" \
  "v_mfma_f32_32x32x16_bf16 v[88:103], v[148:151], a[80:83], v[88:103]\n\t" \
  "ds_read_b64_tr_b16 v[172:173], v6 offset:57344\n\t" \
  "ds_read_b64_tr_b16 v[174:175], v6 offset:59392\n\t" \
  "s_add_u32 s76, s74, 28672\n\t" \
  "s_addc_u32 s77, s75, 0\n\t" \
  "global_load_dwordx4 a[112:115], v0, s[76:77] offset:0 sc1\n\t" \
  "global_load_dwordx4 a[116:119], v0, s[76:77] offset:1024 sc1\n\t" \
  "s_waitcnt lgkmcnt(8)\n\t" \
  "v_mfma_f32_32x32x16_bf16 v[104:119], v[152:155], a[80:83], v[104:119]\n\t" \
  "ds_read_b64_tr_b16 v[176:177], v7 offset:57344\n\t" \
  "ds_read_b64_tr_b16 v[178:179], v7 offset:59392\n\t" \
  "global_load_dwordx4 a[120:123], v0, s[76:77] offset:2048 sc1\n\t" \
  "global_load_dwordx4 a[124:127], v0, s[76:77] offset:3072 sc1\n\t" \
  "v_mfma_f32_32x32x16_bf16 v[72:87], v[156:159], a[84:87], v[72:87]\n\t" \
  "ds_read_b64_tr_b16 v[180:181], v5 offset:61440\n\t" \
  "ds_read_b64_tr_b16 v[182:183], v5 offset:63488\n\t" \
  "s_add_u32 s76, s74, 32768\n\t" \
  "s_addc_u32 s77, s75, 0\n\t" \
  "global_load_dwordx4 a[128:131], v0, s[76:77] offset:0 sc1\n\t" \
  "global_load_dwordx4 a[132:135], v0, s[76:77] offset:1024 sc1\n\t" \
  "s_waitcnt lgkmcnt(8)\n\t" \
  "v_mfma_f32_32x32x16_bf16 v[88:103], v[160:163], a[84:87], v[88:103]\n\t" \
  "ds_read_b64_tr_b16 v[184:185], v6 offset:61440\n\t" \
  "ds_read_b64_tr_b16 v[186:187], v6 offset:63488\n\t" \
  "global_load_dwordx4 a[136:139], v0, s[76:77] offset:2048 sc1\n\t" \
  "global_load_dwordx4 a[140:143], v0, s[76:77] offset:3072 sc1\n\t" \
  "v_mfma_f32_32x32x16_bf16 v[104:119], v[164:167], a[84:87], v[104:119]\n\t" \
  "ds_read_b64_tr_b16 v[188:189], v7 offset:61440\n\t" \
  "ds_read_b64_tr_b16 v[190:191], v7 offset:63488\n\t" \
  "s_barrier\n\t" \
  "s_waitcnt lgkmcnt(8)\n\t" \
  "v_mfma_f32_32x32x16_bf16 v[72:87], v[168:171], a[88:91], v[72:87]\n\t" \
  "ds_read_b64_tr_b16 v[144:145], v5 offset:0\n\t" \
  "ds_read_b64_tr_b16 v[146:147], v5 offset:2048\n\t" \
  "v_mfma_f32_32x32x16_bf16 v[88:103], v[172:175], a[88:91], v[88:103]\n\t" \
  "ds_read_b64_tr_b16 v[148:149], v6 offset:0\n\t" \
  "ds_read_b64_tr_b16 v[150:151], v6 offset:2048\n\t" \
  "s_waitcnt lgkmcnt(8)\n\t" \
  "v_mfma_f32_32x32x16_bf16 v[104:119], v[176:179], a[88:91], v[104:119]\n\t" \
  "ds_read_b64_tr_b16 v[152:153], v7 offset:0\n\t" \
  "ds_read_b64_tr_b16 v[154:155], v7 offset:2048\n\t" \
  "v_mfma_f32_32x32x16_bf16 v[72:87], v[180:183], a[92:95], v[72:87]\n\t" \
  "ds_read_b64_tr_b16 v[156:157], v5 offset:4096\n\t" \
  "ds_read_b64_tr_b16 v[158:159], v5 offset:6144\n\t" \
  "s_waitcnt lgkmcnt(8)\n\t" \
  "v_mfma_f32_32x32x16_bf16 v[88:103], v[184:187], a[92:95], v[88:103]\n\t" \
  "ds_read_b64_tr_b16 v[160:161], v6 offset:4096\n\t" \
  "ds_read_b64_tr_b16 v[162:163], v6 offset:6144\n\t" \
  "global_load_dwordx4 v[24:27], v14, s[40:41]\n\t" \
  "global_load_dwordx4 v[28:31], v61, s[40:41]\n\t" \
  "v_mfma_f32_32x32x16_bf16 v[104:119], v[188:191], a[92:95], v[104:119]\n\t" \
  "ds_read_b64_tr_b16 v[164:165], v7 offset:4096\n\t" \
  "ds_read_b64_tr_b16 v[166:167], v7 offset:6144\n\t" \
  "s_waitcnt lgkmcnt(8)\n\t" \
  "v_mfma_f32_32x32x16_bf16 v[72:87], v[144:147], a[96:99], v[72:87]\n\t" \
  "ds_read_b64_tr_b16 v[168:169], v5 offset:8192\n\t" \
  "ds_read_b64_tr_b16 v[170:171], v5 offset:10240\n\t" \
  "v_mfma_f32_32x32x16_bf16 v[88:103], v[148:151], a[96:99], v[88:103]\n\t" \
  "ds_read_b64_tr_b16 v[172:173], v6 offset:8192\n\t" \
  "ds_read_b64_tr_b16 v[174:175], v6 offset:10240\n\t" \
  "s_waitcnt lgkmcnt(8)\n\t" \
  "v_mfma_f32_32x32x16_bf16 v[104:119], v[152:155], a[96:99], v[104:119]\n\t" \
  "ds_read_b64_tr_b16 v[176:177], v7 offset:8192\n\t" \
  "ds_read_b64_tr_b16 v[178:179], v7 offset:10240\n\t" \
  "v_mfma_f32_32x32x16_bf16 v[72:87], v[156:159], a[100:103], v[72:87]\n\t" \
  "ds_read_b64_tr_b16 v[180:181], v5 offset:12288\n\t" \
  "ds_read_b64_tr_b16 v[182:183], v5 offset:14336\n\t" \
  "s_waitcnt lgkmcnt(8)\n\t" \
  "v_mfma_f32_32x32x16_bf16 v[88:103], v[160:163], a[100:103], v[88:103]\n\t" \
  "ds_read_b64_tr_b16 v[184:185], v6 offset:12288\n\t" \
  "ds_read_b64_tr_b16 v[186:187], v6 offset:14336\n\t" \
  "v_mfma_f32_32x32x16_bf16 v[104:119], v[164:167], a[100:103], v[104:119]\n\t" \
  "ds_read_b64_tr_b16 v[188:189], v7 offset:12288\n\t" \
  "ds_read_b64_tr_b16 v[190:191], v7 offset:14336\n\t" \
  "s_waitcnt lgkmcnt(8)\n\t" \
  "v_mfma_f32_32x32x16_bf16 v[72:87], v[168:171], a[104:107], v[72:87]\n\t" \
  "ds_read_b64_tr_b16 v[144:145], v5 offset:16384\n\t" \
  "ds_read_b64_tr_b16 v[146:147], v5 offset:18432\n\t" \
  "v_mfma_f32_32x32x16_bf16 v[88:103], v[172:175], a[104:107], v[88:103]\n\t" \
  "ds_read_b64_tr_b16 v[148:149], v5 offset:20480\n\t" \
  "ds_read_b64_tr_b16 v[150:151], v5 offset:22528\n\t" \
  "s_barrier\n\t" \
  "s_waitcnt lgkmcnt(8)\n\t" \
  "v_mfma_f32_32x32x16_bf16 v[104:119], v[176:179], a[104:107], v[104:119]\n\t" \
  "ds_read_b64_tr_b16 v[152:153], v5 offset:24576\n\t" \
  "ds_read_b64_tr_b16 v[154:155], v5 offset:26624\n\t" \
  "v_mfma_f32_32x32x16_bf16 v[72:87], v[180:183], a[108:111], v[72:87]\n\t" \
  "ds_read_b64_tr_b16 v[156:157], v5 offset:28672\n\t" \
  "ds_read_b64_tr_b16 v[158:159], v5 offset:30720\n\t" \
  "s_waitcnt lgkmcnt(8)\n\t" \
  "v_mfma_f32_32x32x16_bf16 v[88:103], v[184:187], a[108:111], v[88:103]\n\t" \
  "ds_read_b64_tr_b16 v[160:161], v10 offset:0\n\t" \
  "ds_read_b64_tr_b16 v[162:163], v10 offset:2048\n\t" \
  "v_mfma_f32_32x32x16_bf16 v[104:119], v[188:191], a[108:111], v[104:119]\n\t" \
  "ds_read_b64_tr_b16 v[164:165], v10 offset:4096\n\t" \
  "ds_read_b64_tr_b16 v[166:167], v10 offset:6144\n\t" \
  "s_waitcnt lgkmcnt(8)\n\t" \
  "s_waitcnt vmcnt(9)\n\t" \
  "v_mfma_f32_32x32x16_bf16 v[72:87], v[144:147], a[112:115], v[72:87]\n\t" \
  "ds_read_b64_tr_b16 v[168:169], v10 offset:8192\n\t" \
  "ds_read_b64_tr_b16 v[170:171], v10 offset:10240\n\t" \
  "s_waitcnt vmcnt(8)\n\t" \
  "v_mfma_f32_32x32x16_bf16 v[72:87], v[148:151], a[116:119], v[72:87]\n\t" \
  "ds_read_b64_tr_b16 v[172:173], v10 offset:12288\n\t" \
  "ds_read_b64_tr_b16 v[174:175], v10 offset:14336\n\t" \
  "s_waitcnt lgkmcnt(8)\n\t" \
  "s_waitcnt vmcnt(7)\n\t" \
  "v_mfma_f32_32x32x16_bf16 v[72:87], v[152:155], a[120:123], v[72:87]\n\t" \
  "ds_read_b64_tr_b16 v[176:177], v6 offset:16384\n\t" \
  "ds_read_b64_tr_b16 v[178:179], v6 offset:18432\n\t" \
  "s_waitcnt vmcnt(6)\n\t" \
  "v_mfma_f32_32x32x16_bf16 v[72:87], v[156:159], a[124:127], v[72:87]\n\t" \
  "ds_read_b64_tr_b16 v[180:181], v6 offset:20480\n\t" \
  "ds_read_b64_tr_b16 v[182:183], v6 offset:22528\n\t" \
  "s_waitcnt lgkmcnt(8)\n\t" \
  "s_waitcnt vmcnt(5)\n\t" \
  "v_mfma_f32_32x32x16_bf16 v[72:87], v[160:163], a[128:131], v[72:87]\n\t" \
  "ds_read_b64_tr_b16 v[184:185], v6 offset:24576\n\t" \
  "ds_read_b64_tr_b16 v[186:187], v6 offset:26624\n\t" \
  "s_waitcnt vmcnt(4)\n\t" \
  "v_mfma_f32_32x32x16_bf16 v[72:87], v[164:167], a[132:135], v[72:87]\n\t" \
  "ds_read_b64_tr_b16 v[188:189], v6 offset:28672\n\t" \
  "ds_read_b64_tr_b16 v[190:191], v6 offset:30720\n\t" \
  "s_waitcnt lgkmcnt(8)\n\t" \
  "s_waitcnt vmcnt(3)\n\t" \
  "v_mfma_f32_32x32x16_bf16 v[72:87], v[168:171], a[136:139], v[72:87]\n\t" \
  "ds_read_b64_tr_b16 v[144:145], v11 offset:0\n\t" \
  "ds_read_b64_tr_b16 v[146:147], v11 offset:2048\n\t" \
  "s_waitcnt vmcnt(2)\n\t" \
  "v_mfma_f32_32x32x16_bf16 v[72:87], v[172:175], a[140:143], v[72:87]\n\t" \
  "ds_read_b64_tr_b16 v[148:149], v11 offset:4096\n\t" \
  "ds_read_b64_tr_b16 v[150:151], v11 offset:6144\n\t" \
  "s_waitcnt lgkmcnt(8)\n\t" \
  "v_mfma_f32_32x32x16_bf16 v[88:103], v[176:179], a[112:115], v[88:103]\n\t" \
  "ds_read_b64_tr_b16 v[152:153], v11 offset:8192\n\t" \
  "ds_read_b64_tr_b16 v[154:155], v11 offset:10240\n\t" \
  "v_mfma_f32_32x32x16_bf16 v[88:103], v[180:183], a[116:119], v[88:103]\n\t" \
  "ds_read_b64_tr_b16 v[156:157], v11 offset:12288\n\t" \
  "ds_read_b64_tr_b16 v[158:159], v11 offset:14336\n\t" \
  "s_waitcnt lgkmcnt(8)\n\t" \
  "v_mfma_f32_32x32x16_bf16 v[88:103], v[184:187], a[120:123], v[88:103]\n\t" \
  "ds_read_b64_tr_b16 v[160:161], v7 offset:16384\n\t" \
  "ds_read_b64_tr_b16 v[162:163], v7 offset:18432\n\t" \
  "s_waitcnt vmcnt(0)\n\t" \
  "v_permlane16_swap_b32 v24, v28\n\t" \
  "v_permlane16_swap_b32 v25, v29\n\t" \
  "v_mfma_f32_32x32x16_bf16 v[88:103], v[188:191], a[124:127], v[88:103]\n\t" \
  "ds_read_b64_tr_b16 v[164:165], v7 offset:20480\n\t" \
  "ds_read_b64_tr_b16 v[166:167], v7 offset:22528\n\t" \
  "v_permlane16_swap_b32 v26, v30\n\t" \
  "v_permlane16_swap_b32 v27, v31\n\t" \
  "v_cvt_pk_bf16_f32 v204, v72, v73\n\t" \
  "v_cvt_pk_bf16_f32 v205, v74, v75\n\t" \
  "s_waitcnt lgkmcnt(8)\n\t" \
  "v_mfma_f32_32x32x16_bf16 v[88:103], v[144:147], a[128:131], v[88:103]\n\t" \
  "ds_read_b64_tr_b16 v[168:169], v7 offset:24576\n\t" \
  "ds_read_b64_tr_b16 v[170:171], v7 offset:26624\n\t" \
  "v_permlane32_swap_b32 v24, v26\n\t" \
  "v_permlane32_swap_b32 v25, v27\n\t" \
  "v_cvt_pk_bf16_f32 v206, v76, v77\n\t" \
  "v_cvt_pk_bf16_f32 v207, v78, v79\n\t" \
  "v_mfma_f32_32x32x16_bf16 v[88:103], v[148:151], a[132:135], v[88:103]\n\t" \
  "ds_read_b64_tr_b16 v[172:173], v7 offset:28672\n\t" \
  "ds_read_b64_tr_b16 v[174:175], v7 offset:30720\n\t" \
  "v_permlane32_swap_b32 v28, v30\n\t" \
  "v_permlane32_swap_b32 v29, v31\n\t" \
  "global_store_dwordx4 v17, v[204:207], s[44:45] offset:0 sc1\n\t" \
  "s_waitcnt lgkmcnt(8)\n\t" \
  "v_mfma_f32_32x32x16_bf16 v[88:103], v[152:155], a[136:139], v[88:103]\n\t" \
  "ds_read_b64_tr_b16 v[176:177], v15 offset:0\n\t" \
  "ds_read_b64_tr_b16 v[178:179], v15 offset:2048\n\t" \
  "v_lshlrev_b32 v32, 16, v24\n\t" \
  "v_and_b32 v33, 0xffff0000, v24\n\t" \
  "v_cvt_pk_bf16_f32 v208, v80, v81\n\t" \
  "v_cvt_pk_bf16_f32 v209, v82, v83\n\t" \
  "v_mfma_f32_32x32x16_bf16 v[88:103], v[156:159], a[140:143], v[88:103]\n\t" \
  "ds_read_b64_tr_b16 v[180:181], v15 offset:4096\n\t" \
  "ds_read_b64_tr_b16 v[182:183], v15 offset:6144\n\t" \
  "v_lshlrev_b32 v34, 16, v25\n\t" \
  "v_and_b32 v35, 0xffff0000, v25\n\t" \
  "v_cvt_pk_bf16_f32 v210, v84, v85\n\t" \
  "v_cvt_pk_bf16_f32 v211, v86, v87\n\t" \
  "s_waitcnt lgkmcnt(8)\n\t" \
  "v_mfma_f32_32x32x16_bf16 v[104:119], v[160:163], a[112:115], v[104:119]\n\t" \
  "ds_read_b64_tr_b16 v[184:185], v15 offset:8192\n\t" \
  "ds_read_b64_tr_b16 v[186:187], v15 offset:10240\n\t" \
  "v_lshlrev_b32 v36, 16, v26\n\t" \
  "v_and_b32 v37, 0xffff0000, v26\n\t" \
  "s_cmp_eq_u32 s84, 0\n\t" \
  "s_cbranch_scc0 GSA%=\n\t" \
  "global_load_dwordx2 v[48:49], v9, s[44:45] offset:0 sc1\n\t" \
  "global_load_dwordx2 v[50:51], v9, s[44:45] offset:1024 sc1\n\t" \
  "global_load_dwordx2 v[52:53], v9, s[44:45] offset:2048 sc1\n\t" \
  "global_load_dwordx2 v[54:55], v9, s[44:45] offset:3072 sc1\n\t" \
  "GSA%=:\n\t" \
  "global_store_dwordx4 v17, v[208:211], s[44:45] offset:1024 sc1\n\t" \
  "v_mfma_f32_32x32x16_bf16 v[104:119], v[164:167], a[116:119], v[104:119]\n\t" \
  "ds_read_b64_tr_b16 v[188:189], v15 offset:12288\n\t" \
  "ds_read_b64_tr_b16 v[190:191], v15 offset:14336\n\t" \
  "v_lshlrev_b32 v38, 16, v27\n\t" \
  "v_and_b32 v39, 0xffff0000, v27\n\t" \
  "s_waitcnt lgkmcnt(8)\n\t" \
  "v_mfma_f32_32x32x16_bf16 v[104:119], v[168:171], a[120:123], v[104:119]\n\t" \
  "ds_read_b64_tr_b16 v[144:145], v4 offset:0\n\t" \
  "ds_read_b64_tr_b16 v[146:147], v4 offset:2048\n\t" \
  "v_lshlrev_b32 v40, 16, v28\n\t" \
  "v_and_b32 v41, 0xffff0000, v28\n\t" \
  "v_mfma_f32_32x32x16_bf16 v[104:119], v[172:175], a[124:127], v[104:119]\n\t" \
  "ds_read_b64_tr_b16 v[148:149], v4 offset:4096\n\t" \
  "ds_read_b64_tr_b16 v[150:151], v4 offset:6144\n\t" \
  "v_lshlrev_b32 v42, 16, v29\n\t" \
  "v_and_b32 v43, 0xffff0000, v29\n\t" \
  "v_cvt_pk_bf16_f32 v212, v88, v89\n\t" \
  "v_cvt_pk_bf16_f32 v213, v90, v91\n\t" \
  "s_waitcnt lgkmcnt(8)\n\t" \
  "v_mfma_f32_32x32x16_bf16 v[104:119], v[176:179], a[128:131], v[104:119]\n\t" \
  "ds_read_b64_tr_b16 v[152:153], v4 offset:8192\n\t" \
  "ds_read_b64_tr_b16 v[154:155], v4 offset:10240\n\t" \
  "v_lshlrev_b32 v44, 16, v30\n\t" \
  "v_and_b32 v45, 0xffff0000, v30\n\t" \
  "v_cvt_pk_bf16_f32 v214, v92, v93\n\t" \
  "v_cvt_pk_bf16_f32 v215, v94, v95\n\t" \
  "v_mfma_f32_32x32x16_bf16 v[104:119], v[180:183], a[132:135], v[104:119]\n\t" \
  "ds_read_b64_tr_b16 v[156:157], v4 offset:12288\n\t" \
  "ds_read_b64_tr_b16 v[158:159], v4 offset:14336\n\t" \
  "v_lshlrev_b32 v46, 16, v31\n\t" \
  "v_and_b32 v47, 0xffff0000, v31\n\t" \
  "global_store_dwordx4 v18, v[212:215], s[44:45] offset:0 sc1\n\t" \
  "s_waitcnt lgkmcnt(8)\n\t" \
  "v_mfma_f32_32x32x16_bf16 v[104:119], v[184:187], a[136:139], v[104:119]\n\t" \
  "ds_read_b64_tr_b16 v[160:161], v4 offset:16384\n\t" \
  "ds_read_b64_tr_b16 v[162:163], v4 offset:18432\n\t" \
  "v_cvt_pk_bf16_f32 v216, v96, v97\n\t" \
  "v_cvt_pk_bf16_f32 v217, v98, v99\n\t" \
  "v_mfma_f32_32x32x16_bf16 v[104:119], v[188:191], a[140:143], v[104:119]\n\t" \
  "ds_read_b64_tr_b16 v[164:165], v4 offset:20480\n\t" \
  "ds_read_b64_tr_b16 v[166:167], v4 offset:22528\n\t" \
  "v_cvt_pk_bf16_f32 v218, v100, v101\n\t" \
  "v_cvt_pk_bf16_f32 v219, v102, v103\n\t" \
  "s_waitcnt lgkmcnt(8)\n\t" \
  "v_mfma_f32_32x32x16_bf16 v[120:135], v[144:147], a[0:3], 0\n\t" \
  "ds_read_b64_tr_b16 v[168:169], v4 offset:24576\n\t" \
  "ds_read_b64_tr_b16 v[170:171], v4 offset:26624\n\t" \
  "global_store_dwordx4 v18, v[216:219], s[44:45] offset:1024 sc1\n\t" \
  "v_mfma_f32_32x32x16_bf16 v[120:135], v[148:151], a[4:7], v[120:135]\n\t" \
  "ds_read_b64_tr_b16 v[172:173], v4 offset:28672\n\t" \
  "ds_read_b64_tr_b16 v[174:175], v4 offset:30720\n\t" \
  "s_waitcnt lgkmcnt(8)\n\t" \
  "v_mfma_f32_32x32x16_bf16 v[120:135], v[152:155], a[8:11], v[120:135]\n\t" \
  "ds_read_b64_tr_b16 v[176:177], v4 offset:32768\n\t" \
  "ds_read_b64_tr_b16 v[178:179], v4 offset:34816\n\t" \
  "v_mfma_f32_32x32x16_bf16 v[120:135], v[156:159], a[12:15], v[120:135]\n\t" \
  "ds_read_b64_tr_b16 v[180:181], v4 offset:36864\n\t" \
  "ds_read_b64_tr_b16 v[182:183], v4 offset:38912\n\t" \
  "v_cvt_pk_bf16_f32 v220, v104, v105\n\t" \
  "v_cvt_pk_bf16_f32 v221, v106, v107\n\t" \
  "s_waitcnt lgkmcnt(8)\n\t" \
  "v_mfma_f32_32x32x16_bf16 v[120:135], v[160:163], a[16:19], v[120:135]\n\t" \
  "ds_read_b64_tr_b16 v[184:185], v4 offset:40960\n\t" \
  "ds_read_b64_tr_b16 v[186:187], v4 offset:43008\n\t" \
  "v_cvt_pk_bf16_f32 v222, v108, v109\n\t" \
  "v_cvt_pk_bf16_f32 v223, v110, v111\n\t" \
  "v_mfma_f32_32x32x16_bf16 v[120:135], v[164:167], a[20:23], v[120:135]\n\t" \
  "ds_read_b64_tr_b16 v[188:189], v4 offset:45056\n\t" \
  "ds_read_b64_tr_b16 v[190:191], v4 offset:47104\n\t" \
  "global_store_dwordx4 v19, v[220:223], s[44:45] offset:0 sc1\n\t" \
  "s_waitcnt lgkmcnt(8)\n\t" \
  "v_mfma_f32_32x32x16_bf16 v[120:135], v[168:171], a[24:27], v[120:135]\n\t" \
  "ds_read_b64_tr_b16 v[144:145], v4 offset:49152\n\t" \
  "ds_read_b64_tr_b16 v[146:147], v4 offset:51200\n\t" \
  "v_cvt_pk_bf16_f32 v224, v112, v113\n\t" \
  "v_cvt_pk_bf16_f32 v225, v114, v115\n\t" \
  "v_mfma_f32_32x32x16_bf16 v[120:135], v[172:175], a[28:31], v[120:135]\n\t" \
  "ds_read_b64_tr_b16 v[148:149], v4 offset:53248\n\t" \
  "ds_read_b64_tr_b16 v[150:151], v4 offset:55296\n\t" \
  "v_cvt_pk_bf16_f32 v226, v116, v117\n\t" \
  "v_cvt_pk_bf16_f32 v227, v118, v119\n\t" \
  "s_waitcnt lgkmcnt(8)\n\t" \
  "v_mfma_f32_32x32x16_bf16 v[120:135], v[176:179], a[32:35], v[120:135]\n\t" \
  "ds_read_b64_tr_b16 v[152:153], v4 offset:57344\n\t" \
  "ds_read_b64_tr_b16 v[154:155], v4 offset:59392\n\t" \
  "global_store_dwordx4 v19, v[224:227], s[44:45] offset:1024 sc1\n\t" \
  "v_mfma_f32_32x32x16_bf16 v[120:135], v[180:183], a[36:39], v[120:135]\n\t" \
  "ds_read_b64_tr_b16 v[156:157], v4 offset:61440\n\t" \
  "ds_read_b64_tr_b16 v[158:159], v4 offset:63488\n\t" \
  "s_waitcnt lgkmcnt(8)\n\t" \
  "v_mfma_f32_32x32x16_bf16 v[120:135], v[184:187], a[40:43], v[120:135]\n\t" \
  "ds_read_b64_tr_b16 v[160:161], v8 offset:32768\n\t" \
  "ds_read_b64_tr_b16 v[162:163], v8 offset:34816\n\t" \
  "v_mfma_f32_32x32x16_bf16 v[120:135], v[188:191], a[44:47], v[120:135]\n\t" \
  "ds_read_b64_tr_b16 v[164:165], v8 offset:36864\n\t" \
  "ds_read_b64_tr_b16 v[166:167], v8 offset:38912\n\t" \
  "s_waitcnt lgkmcnt(8)\n\t" \
  "v_mfma_f32_32x32x16_bf16 v[120:135], v[144:147], a[48:51], v[120:135]\n\t" \
  "ds_read_b64_tr_b16 v[168:169], v8 offset:40960\n\t" \
  "ds_read_b64_tr_b16 v[170:171], v8 offset:43008\n\t" \
  "v_mfma_f32_32x32x16_bf16 v[120:135], v[148:151], a[52:55], v[120:135]\n\t" \
  "ds_read_b64_tr_b16 v[172:173], v8 offset:45056\n\t" \
  "ds_read_b64_tr_b16 v[174:175], v8 offset:47104\n\t" \
  "s_waitcnt lgkmcnt(8)\n\t" \
  "v_mfma_f32_32x32x16_bf16 v[120:135], v[152:155], a[56:59], v[120:135]\n\t" \
  "ds_read_b64_tr_b16 v[176:177], v8 offset:49152\n\t" \
  "ds_read_b64_tr_b16 v[178:179], v8 offset:51200\n\t" \
  "s_waitcnt vmcnt(4)\n\t" \
  "v_mov_b32 v56, s16\n\t" \
  "s_mov_b64 exec, 1\n\t" \
  "global_store_dwordx2 v56, v[138:139], s[44:45] sc1\n\t" \
  "s_mov_b64 exec, -1\n\t" \
  "v_mfma_f32_32x32x16_bf16 v[120:135], v[156:159], a[60:63], v[120:135]\n\t" \
  "ds_read_b64_tr_b16 v[180:181], v8 offset:53248\n\t" \
  "ds_read_b64_tr_b16 v[182:183], v8 offset:55296\n\t" \
  "s_waitcnt lgkmcnt(8)\n\t" \
  "v_mfma_f32_32x32x16_bf16 v[120:135], v[160:163], a[64:67], v[120:135]\n\t" \
  "ds_read_b64_tr_b16 v[184:185], v8 offset:57344\n\t" \
  "ds_read_b64_tr_b16 v[186:187], v8 offset:59392\n\t" \
  "v_mfma_f32_32x32x16_bf16 v[120:135], v[164:167], a[68:71], v[120:135]\n\t" \
  "ds_read_b64_tr_b16 v[188:189], v8 offset:61440\n\t" \
  "ds_read_b64_tr_b16 v[190:191], v8 offset:63488\n\t" \
  "s_waitcnt lgkmcnt(8)\n\t" \
  "v_mfma_f32_32x32x16_bf16 v[120:135], v[168:171], a[72:75], v[120:135]\n\t" \
  "ds_read_b64_tr_b16 v[144:145], v8 offset:0\n\t" \
  "ds_read_b64_tr_b16 v[146:147], v8 offset:2048\n\t" \
  "v_mfma_f32_32x32x16_bf16 v[120:135], v[172:175], a[76:79], v[120:135]\n\t" \
  "ds_read_b64_tr_b16 v[148:149], v8 offset:4096\n\t" \
  "ds_read_b64_tr_b16 v[150:151], v8 offset:6144\n\t" \
  "s_waitcnt lgkmcnt(8)\n\t" \
  "v_mfma_f32_32x32x16_bf16 v[120:135], v[176:179], a[80:83], v[120:135]\n\t" \
  "ds_read_b64_tr_b16 v[152:153], v8 offset:8192\n\t" \
  "ds_read_b64_tr_b16 v[154:155], v8 offset:10240\n\t" \
  "v_mfma_f32_32x32x16_bf16 v[120:135], v[180:183], a[84:87], v[120:135]\n\t" \
  "ds_read_b64_tr_b16 v[156:157], v8 offset:12288\n\t" \
  "ds_read_b64_tr_b16 v[158:159], v8 offset:14336\n\t" \
  "s_waitcnt lgkmcnt(8)\n\t" \
  "v_mfma_f32_32x32x16_bf16 v[120:135], v[184:187], a[88:91], v[120:135]\n\t" \
  "ds_read_b64_tr_b16 v[160:161], v8 offset:16384\n\t" \
  "ds_read_b64_tr_b16 v[162:163], v8 offset:18432\n\t" \
  "s_waitcnt vmcnt(3)\n\t" \
  "v_mov_b32 v56, s17\n\t" \
  "s_mov_b64 exec, 1\n\t" \
  "global_store_dwordx2 v56, v[138:139], s[44:45] sc1\n\t" \
  "s_mov_b64 exec, -1\n\t" \
  "v_mfma_f32_32x32x16_bf16 v[120:135], v[188:191], a[92:95], v[120:135]\n\t" \
  "ds_read_b64_tr_b16 v[164:165], v8 offset:20480\n\t" \
  "ds_read_b64_tr_b16 v[166:167], v8 offset:22528\n\t" \
  "s_waitcnt lgkmcnt(8)\n\t" \
  "v_mfma_f32_32x32x16_bf16 v[120:135], v[144:147], a[96:99], v[120:135]\n\t" \
  "ds_read_b64_tr_b16 v[168:169], v8 offset:24576\n\t" \
  "ds_read_b64_tr_b16 v[170:171], v8 offset:26624\n\t" \
  "v_mfma_f32_32x32x16_bf16 v[120:135], v[148:151], a[100:103], v[120:135]\n\t" \
  "ds_read_b64_tr_b16 v[172:173], v8 offset:28672\n\t" \
  "ds_read_b64_tr_b16 v[174:175], v8 offset:30720\n\t" \
  "s_waitcnt lgkmcnt(8)\n\t" \
  "v_mfma_f32_32x32x16_bf16 v[120:135], v[152:155], a[104:107], v[120:135]\n\t" \
  "ds_read_b64_tr_b16 v[176:177], v16 offset:0\n\t" \
  "ds_read_b64_tr_b16 v[178:179], v16 offset:2048\n\t" \
  "v_mfma_f32_32x32x16_bf16 v[120:135], v[156:159], a[108:111], v[120:135]\n\t" \
  "ds_read_b64_tr_b16 v[180:181], v16 offset:4096\n\t" \
  "ds_read_b64_tr_b16 v[182:183], v16 offset:6144\n\t" \
  "s_waitcnt lgkmcnt(8)\n\t" \
  "v_mfma_f32_32x32x16_bf16 v[120:135], v[160:163], a[112:115], v[120:135]\n\t" \
  "ds_read_b64_tr_b16 v[184:185], v16 offset:8192\n\t" \
  "ds_read_b64_tr_b16 v[186:187], v16 offset:10240\n\t" \
  "v_mfma_f32_32x32x16_bf16 v[120:135], v[164:167], a[116:119], v[120:135]\n\t" \
  "ds_read_b64_tr_b16 v[188:189], v16 offset:12288\n\t" \
  "ds_read_b64_tr_b16 v[190:191], v16 offset:14336\n\t" \
  "s_waitcnt lgkmcnt(8)\n\t" \
  "v_mfma_f32_32x32x16_bf16 v[120:135], v[168:171], a[120:123], v[120:135]\n\t" \
  "s_waitcnt vmcnt(2)\n\t" \
  "v_mov_b32 v56, s18\n\t" \
  "s_mov_b64 exec, 1\n\t" \
  "global_store_dwordx2 v56, v[138:139], s[44:45] sc1\n\t" \
  "s_mov_b64 exec, -1\n\t" \
  "v_mfma_f32_32x32x16_bf16 v[120:135], v[172:175], a[124:127], v[120:135]\n\t" \
  "s_waitcnt lgkmcnt(4)\n\t" \
  "v_mfma_f32_32x32x16_bf16 v[120:135], v[176:179], a[128:131], v[120:135]\n\t" \
  "v_mfma_f32_32x32x16_bf16 v[120:135], v[180:183], a[132:135], v[120:135]\n\t" \
  "s_waitcnt lgkmcnt(0)\n\t" \
  "v_mfma_f32_32x32x16_bf16 v[120:135], v[184:187], a[136:139], v[120:135]\n\t" \
  "v_mfma_f32_32x32x16_bf16 v[120:135], v[188:191], a[140:143], v[120:135]\n\t" \
  "global_load_dwordx2 v[142:143], v57, s[44:45] sc1\n\t" \
  "s_waitcnt vmcnt(0)\n\t" \
  "v_cmp_eq_u64 vcc, s[68:69], v[142:143]\n\t" \
  "s_nop 1\n\t" \
  "s_and_b32 s58, vcc_lo, 7\n\t" \
  "s_cmp_eq_u32 s58, 7\n\t" \
  "s_cbranch_scc0 RAS%=\n\t" \
  "RAB%=:\n\t" \
  "global_load_dwordx4 v[228:231], v20, s[44:45] offset:0 sc1\n\t" \
  "global_load_dwordx4 v[232:235], v20, s[44:45] offset:1024 sc1\n\t" \
  "global_load_dwordx4 v[236:239], v58, s[44:45] offset:0 sc1\n\t" \
  "global_load_dwordx4 v[240:243], v58, s[44:45] offset:1024 sc1\n\t" \
  "global_load_dwordx4 v[244:247], v59, s[44:45] offset:0 sc1\n\t" \
  "global_load_dwordx4 v[248:251], v59, s[44:45] offset:1024 sc1\n\t" \
  "s_cmp_eq_u32 s84, 0\n\t" \
  "s_cbranch_scc0 GDA%=\n\t" \
  "s_mov_b32 s56, 0x10000\n\t" \
  "GLA%=:\n\t" \
  "v_cmp_eq_u64 vcc, s[68:69], v[48:49]\n\t" \
  "v_cmp_eq_u64 s[58:59], s[68:69], v[50:51]\n\t" \
  "v_cmp_eq_u64 s[60:61], s[68:69], v[52:53]\n\t" \
  "v_cmp_eq_u64 s[62:63], s[68:69], v[54:55]\n\t" \
  "s_nop 1\n\t" \
  "s_and_b64 vcc, vcc, s[58:59]\n\t" \
  "s_and_b64 s[60:61], s[60:61], s[62:63]\n\t" \
  "s_and_b64 vcc, vcc, s[60:61]\n\t" \
  "s_cmp_eq_u64 vcc, -1\n\t" \
  "s_cbranch_scc1 GKA%=\n\t" \
  "s_sleep 1\n\t" \
  "global_load_dwordx2 v[48:49], v9, s[44:45] offset:0 sc1\n\t" \
  "global_load_dwordx2 v[50:51], v9, s[44:45] offset:1024 sc1\n\t" \
  "global_load_dwordx2 v[52:53], v9, s[44:45] offset:2048 sc1\n\t" \
  "global_load_dwordx2 v[54:55], v9, s[44:45] offset:3072 sc1\n\t" \
  "s_waitcnt vmcnt(0)\n\t" \
  "s_sub_u32 s56, s56, 1\n\t" \
  "s_cbranch_scc0 GLA%=\n\t" \
  "GKA%=:\n\t" \
  "s_barrier\n\t" \
  "s_cmp_eq_u32 s86, 0\n\t" \
  "s_cbranch_scc0 GDA%=\n\t" \
  "v_mov_b32 v56, 0\n\t" \
  "v_mov_b32 v62, s14\n\t" \
  "v_mov_b32 v63, s15\n\t" \
  "s_mov_b64 exec, 1\n\t" \
  "global_store_dwordx2 v56, v[62:63], s[44:45] sc1\n\t" \
  "s_mov_b64 exec, -1\n\t" \
  "GDA%=:\n\t" \
  "s_waitcnt vmcnt(5)\n\t" \
  "v_dot2c_f32_bf16 v120, s30, v228\n\t" \
  "v_dot2c_f32_bf16 v121, s31, v228\n\t" \
  "v_dot2c_f32_bf16 v122, s30, v229\n\t" \
  "v_dot2c_f32_bf16 v123, s31, v229\n\t" \
  "v_dot2c_f32_bf16 v124, s30, v230\n\t" \
  "v_dot2c_f32_bf16 v125, s31, v230\n\t" \
  "v_dot2c_f32_bf16 v126, s30, v231\n\t" \
  "v_dot2c_f32_bf16 v127, s31, v231\n\t" \
  "s_waitcnt vmcnt(4)\n\t" \
  "v_dot2c_f32_bf16 v128, s30, v232\n\t" \
  "v_dot2c_f32_bf16 v129, s31, v232\n\t" \
  "v_dot2c_f32_bf16 v130, s30, v233\n\t" \
  "v_dot2c_f32_bf16 v131, s31, v233\n\t" \
  "v_dot2c_f32_bf16 v132, s30, v234\n\t" \
  "v_dot2c_f32_bf16 v133, s31, v234\n\t" \
  "v_dot2c_f32_bf16 v134, s30, v235\n\t" \
  "v_dot2c_f32_bf16 v135, s31, v235\n\t" \
  "s_waitcnt vmcnt(3)\n\t" \
  "v_dot2c_f32_bf16 v120, s30, v236\n\t" \
  "v_dot2c_f32_bf16 v121, s31, v236\n\t" \
  "v_dot2c_f32_bf16 v122, s30, v237\n\t" \
  "v_dot2c_f32_bf16 v123, s31, v237\n\t" \
  "v_dot2c_f32_bf16 v124, s30, v238\n\t" \
  "v_dot2c_f32_bf16 v125, s31, v238\n\t" \
  "v_dot2c_f32_bf16 v126, s30, v239\n\t" \
  "v_dot2c_f32_bf16 v127, s31, v239\n\t" \
  "s_waitcnt vmcnt(2)\n\t" \
  "v_dot2c_f32_bf16 v128, s30, v240\n\t" \
  "v_dot2c_f32_bf16 v129, s31, v240\n\t" \
  "v_dot2c_f32_bf16 v130, s30, v241\n\t" \
  "v_dot2c_f32_bf16 v131, s31, v241\n\t" \
  "v_dot2c_f32_bf16 v132, s30, v242\n\t" \
  "v_dot2c_f32_bf16 v133, s31, v242\n\t" \
  "v_dot2c_f32_bf16 v134, s30, v243\n\t" \
  "v_dot2c_f32_bf16 v135, s31, v243\n\t" \
  "s_waitcnt vmcnt(1)\n\t" \
  "v_dot2c_f32_bf16 v120, s30, v244\n\t" \
  "v_dot2c_f32_bf16 v121, s31, v244\n\t" \
  "v_dot2c_f32_bf16 v122, s30, v245\n\t" \
  "v_dot2c_f32_bf16 v123, s31, v245\n\t" \
  "v_dot2c_f32_bf16 v124, s30, v246\n\t" \
  "v_dot2c_f32_bf16 v125, s31, v246\n\t" \
  "v_dot2c_f32_bf16 v126, s30, v247\n\t" \
  "v_dot2c_f32_bf16 v127, s31, v247\n\t" \
  "s_waitcnt vmcnt(0)\n\t" \
  "v_dot2c_f32_bf16 v128, s30, v248\n\t" \
  "v_dot2c_f32_bf16 v129, s31, v248\n\t" \
  "v_dot2c_f32_bf16 v130, s30, v249\n\t" \
  "v_dot2c_f32_bf16 v131, s31, v249\n\t" \
  "v_dot2c_f32_bf16 v132, s30, v250\n\t" \
  "v_dot2c_f32_bf16 v133, s31, v250\n\t" \
  "v_dot2c_f32_bf16 v134, s30, v251\n\t" \
  "v_dot2c_f32_bf16 v135, s31, v251\n\t" \
  "s_mov_b32 s58, 0xbf317218\n\t" \
  "v_pk_fma_f32 v[120:121], v[120:121], s[58:59], v[32:33] op_sel_hi:[1,0,1]\n\t" \
  "v_pk_fma_f32 v[122:123], v[122:123], s[58:59], v[34:35] op_sel_hi:[1,0,1]\n\t" \
  "v_pk_fma_f32 v[124:125], v[124:125], s[58:59], v[36:37] op_sel_hi:[1,0,1]\n\t" \
  "v_pk_fma_f32 v[126:127], v[126:127], s[58:59], v[38:39] op_sel_hi:[1,0,1]\n\t" \
  "v_pk_fma_f32 v[128:129], v[128:129], s[58:59], v[40:41] op_sel_hi:[1,0,1]\n\t" \
  "v_pk_fma_f32 v[130:131], v[130:131], s[58:59], v[42:43] op_sel_hi:[1,0,1]\n\t" \
  "v_pk_fma_f32 v[132:133], v[132:133], s[58:59], v[44:45] op_sel_hi:[1,0,1]\n\t" \
  "v_pk_fma_f32 v[134:135], v[134:135], s[58:59], v[46:47] op_sel_hi:[1,0,1]\n\t" \
  "v_cvt_pk_bf16_f32 v204, v120, v121\n\t" \
  "v_cvt_pk_bf16_f32 v205, v122, v123\n\t" \
  "v_cvt_pk_bf16_f32 v206, v124, v125\n\t" \
  "v_cvt_pk_bf16_f32 v207, v126, v127\n\t" \
  "v_cvt_pk_bf16_f32 v208, v128, v129\n\t" \
  "v_cvt_pk_bf16_f32 v209, v130, v131\n\t" \
  "v_cvt_pk_bf16_f32 v210, v132, v133\n\t" \
  "v_cvt_pk_bf16_f32 v211, v134, v135\n\t" \
  "v_permlane32_swap_b32 v204, v206\n\t" \
  "v_permlane32_swap_b32 v205, v207\n\t" \
  "v_permlane32_swap_b32 v208, v210\n\t" \
  "v_permlane32_swap_b32 v209, v211\n\t" \
  "s_nop 0\n\t" \
  "v_permlane16_swap_b32 v204, v208\n\t" \
  "v_permlane16_swap_b32 v205, v209\n\t" \
  "v_permlane16_swap_b32 v206, v210\n\t" \
  "v_permlane16_swap_b32 v207, v211\n\t" \
  "global_store_dwordx4 v14, v[204:207], s[80:81] sc1\n\t" \
  "global_store_dwordx4 v61, v[208:211], s[80:81] sc1\n\t" \
  "FIN%=:\n\t" \
  "s_branch END%=\n\t" \
  "KRF%=:\n\t" \
  "s_sleep 1\n\t" \
  "global_load_dwordx4 v[24:27], v60, s[44:45] sc1\n\t" \
  "global_load_dwordx4 v[28:31], v60, s[44:45] offset:16 sc1\n\t" \
  "global_load_dwordx4 v[32:35], v61, s[44:45] sc1\n\t" \
  "global_load_dwordx4 v[36:39], v61, s[44:45] offset:16 sc1\n\t" \
  "global_load_dwordx4 v[76:79], v62, s[44:45] sc1\n\t" \
  "s_waitcnt vmcnt(0)\n\t" \
  "s_sub_u32 s56, s56, 1\n\t" \
  "s_cbranch_scc0 KCF%=\n\t" \
  "s_branch KOF%=\n\t" \
  "P0FS%=:\n\t" \
  "global_load_dwordx2 v[142:143], v13, s[44:45] sc1\n\t" \
  "s_waitcnt vmcnt(0)\n\t" \
  "v_or_b32 v138, 4, v138\n\t" \
  "s_mov_b64 exec, 1\n\t" \
  "global_store_dwordx2 v12, v[138:139], s[44:45] sc1\n\t" \
  "s_mov_b64 exec, -1\n\t" \
  "s_nop 0\n\t" \
  "v_and_b32 v138, -8, v138\n\t" \
  "s_or_b32 s12, s12, 2\n\t" \
  "v_cmp_ge_u64 s[58:59], s[68:69], v[142:143]\n\t" \
  "v_cmp_le_u64 vcc, s[12:13], v[142:143]\n\t" \
  "s_nop 1\n\t" \
  "s_and_b32 s58, s58, vcc_lo\n\t" \
  "s_and_b32 s12, s12, -8\n\t" \
  "s_and_b32 s58, s58, 240\n\t" \
  "s_cmp_eq_u32 s58, 240\n\t" \
  "s_cbranch_scc1 P0FB%=\n\t" \
  "s_mov_b32 s56, 0x10000\n\t" \
  "P0FL%=:\n\t" \
  "global_load_dwordx2 v[142:143], v13, s[44:45] sc1\n\t" \
  "s_waitcnt vmcnt(0)\n\t" \
  "s_or_b32 s12, s12, 2\n\t" \
  "v_cmp_ge_u64 s[58:59], s[68:69], v[142:143]\n\t" \
  "v_cmp_le_u64 vcc, s[12:13], v[142:143]\n\t" \
  "s_nop 1\n\t" \
  "s_and_b32 s58, s58, vcc_lo\n\t" \
  "s_and_b32 s12, s12, -8\n\t" \
  "s_and_b32 s58, s58, 240\n\t" \
  "s_cmp_eq_u32 s58, 240\n\t" \
  "s_cbranch_scc1 P0FB%=\n\t" \
  "s_sub_u32 s56, s56, 1\n\t" \
  "s_cbranch_scc0 P0FL%=\n\t" \
  "s_branch P0FB%=\n\t" \
  "P14FS%=:\n\t" \
  "s_mov_b32 s56, 0x10000\n\t" \
  "P14FL%=:\n\t" \
  "global_load_dwordx2 v[136:137], v13, s[44:45] sc1\n\t" \
  "s_waitcnt vmcnt(0)\n\t" \
  "s_or_b32 s12, s12, 4\n\t" \
  "v_cmp_ge_u64 s[58:59], s[68:69], v[136:137]\n\t" \
  "v_cmp_le_u64 vcc, s[12:13], v[136:137]\n\t" \
  "s_nop 1\n\t" \
  "s_and_b32 s58, s58, vcc_lo\n\t" \
  "s_and_b32 s12, s12, -8\n\t" \
  "s_and_b32 s58, s58, 15\n\t" \
  "s_cmp_eq_u32 s58, 15\n\t" \
  "s_cbranch_scc1 P14FB%=\n\t" \
  "s_sub_u32 s56, s56, 1\n\t" \
  "s_cbranch_scc0 P14FL%=\n\t" \
  "s_branch P14FB%=\n\t" \
  "P8FS%=:\n\t" \
  "s_mov_b32 s56, 0x10000\n\t" \
  "P8FL%=:\n\t" \
  "global_load_dwordx2 v[142:143], v13, s[44:45] sc1\n\t" \
  "s_waitcnt vmcnt(0)\n\t" \
  "s_or_b32 s12, s12, 4\n\t" \
  "v_cmp_ge_u64 s[58:59], s[68:69], v[142:143]\n\t" \
  "v_cmp_le_u64 vcc, s[12:13], v[142:143]\n\t" \
  "s_nop 1\n\t" \
  "s_and_b32 s58, s58, vcc_lo\n\t" \
  "s_and_b32 s12, s12, -8\n\t" \
  "s_and_b32 s58, s58, 240\n\t" \
  "s_cmp_eq_u32 s58, 240\n\t" \
  "s_cbranch_scc1 P8FB%=\n\t" \
  "s_sub_u32 s56, s56, 1\n\t" \
  "s_cbranch_scc0 P8FL%=\n\t" \
  "s_branch P8FB%=\n\t" \
  "RFS%=:\n\t" \
  "s_mov_b32 s56, 0x10000\n\t" \
  "RFL%=:\n\t" \
  "global_load_dwordx2 v[142:143], v57, s[44:45] sc1\n\t" \
  "s_waitcnt vmcnt(0)\n\t" \
  "v_cmp_eq_u64 vcc, s[68:69], v[142:143]\n\t" \
  "s_nop 1\n\t" \
  "s_and_b32 s58, vcc_lo, 7\n\t" \
  "s_cmp_eq_u32 s58, 7\n\t" \
  "s_cbranch_scc1 RFB%=\n\t" \
  "s_sub_u32 s56, s56, 1\n\t" \
  "s_cbranch_scc0 RFL%=\n\t" \
  "s_branch RFB%=\n\t" \
  "KRA%=:\n\t" \
  "s_sleep 1\n\t" \
  "global_load_dwordx4 v[24:27], v60, s[44:45] sc1\n\t" \
  "global_load_dwordx4 v[28:31], v60, s[44:45] offset:16 sc1\n\t" \
  "global_load_dwordx4 v[32:35], v61, s[44:45] sc1\n\t" \
  "global_load_dwordx4 v[36:39], v61, s[44:45] offset:16 sc1\n\t" \
  "global_load_dwordx4 v[40:43], v59, s[44:45] sc1\n\t" \
  "global_load_dwordx4 v[44:47], v59, s[44:45] offset:16 sc1\n\t" \
  "global_load_dwordx4 v[76:79], v62, s[44:45] sc1\n\t" \
  "s_waitcnt vmcnt(0)\n\t" \
  "s_sub_u32 s56, s56, 1\n\t" \
  "s_cbranch_scc0 KCA%=\n\t" \
  "s_branch KOA%=\n\t" \
  "P0AS%=:\n\t" \
  "global_load_dwordx2 v[142:143], v13, s[44:45] sc1\n\t" \
  "s_waitcnt vmcnt(0)\n\t" \
  "v_or_b32 v138, 4, v138\n\t" \
  "s_mov_b64 exec, 1\n\t" \
  "global_store_dwordx2 v12, v[138:139], s[44:45] sc1\n\t" \
  "s_mov_b64 exec, -1\n\t" \
  "s_nop 0\n\t" \
  "v_and_b32 v138, -8, v138\n\t" \
  "s_or_b32 s12, s12, 2\n\t" \
  "v_cmp_ge_u64 s[58:59], s[68:69], v[142:143]\n\t" \
  "v_cmp_le_u64 vcc, s[12:13], v[142:143]\n\t" \
  "s_nop 1\n\t" \
  "s_and_b32 s58, s58, vcc_lo\n\t" \
  "s_and_b32 s12, s12, -8\n\t" \
  "s_and_b32 s58, s58, 240\n\t" \
  "s_cmp_eq_u32 s58, 240\n\t" \
  "s_cbranch_scc1 P0AB%=\n\t" \
  "s_mov_b32 s56, 0x10000\n\t" \
  "P0AL%=:\n\t" \
  "global_load_dwordx2 v[142:143], v13, s[44:45] sc1\n\t" \
  "s_waitcnt vmcnt(0)\n\t" \
  "s_or_b32 s12, s12, 2\n\t" \
  "v_cmp_ge_u64 s[58:59], s[68:69], v[142:143]\n\t" \
  "v_cmp_le_u64 vcc, s[12:13], v[142:143]\n\t" \
  "s_nop 1\n\t" \
  "s_and_b32 s58, s58, vcc_lo\n\t" \
  "s_and_b32 s12, s12, -8\n\t" \
  "s_and_b32 s58, s58, 240\n\t" \
  "s_cmp_eq_u32 s58, 240\n\t" \
  "s_cbranch_scc1 P0AB%=\n\t" \
  "s_sub_u32 s56, s56, 1\n\t" \
  "s_cbranch_scc0 P0AL%=\n\t" \
  "s_branch P0AB%=\n\t" \
  "P14AS%=:\n\t" \
  "s_mov_b32 s56, 0x10000\n\t" \
  "P14AL%=:\n\t" \
  "global_load_dwordx2 v[136:137], v13, s[44:45] sc1\n\t" \
  "s_waitcnt vmcnt(0)\n\t" \
  "s_or_b32 s12, s12, 4\n\t" \
  "v_cmp_ge_u64 s[58:59], s[68:69], v[136:137]\n\t" \
  "v_cmp_le_u64 vcc, s[12:13], v[136:137]\n\t" \
  "s_nop 1\n\t" \
  "s_and_b32 s58, s58, vcc_lo\n\t" \
  "s_and_b32 s12, s12, -8\n\t" \
  "s_and_b32 s58, s58, 15\n\t" \
  "s_cmp_eq_u32 s58, 15\n\t" \
  "s_cbranch_scc1 P14AB%=\n\t" \
  "s_sub_u32 s56, s56, 1\n\t" \
  "s_cbranch_scc0 P14AL%=\n\t" \
  "s_branch P14AB%=\n\t" \
  "P8AS%=:\n\t" \
  "s_mov_b32 s56, 0x10000\n\t" \
  "P8AL%=:\n\t" \
  "global_load_dwordx2 v[142:143], v13, s[44:45] sc1\n\t" \
  "s_waitcnt vmcnt(0)\n\t" \
  "s_or_b32 s12, s12, 4\n\t" \
  "v_cmp_ge_u64 s[58:59], s[68:69], v[142:143]\n\t" \
  "v_cmp_le_u64 vcc, s[12:13], v[142:143]\n\t" \
  "s_nop 1\n\t" \
  "s_and_b32 s58, s58, vcc_lo\n\t" \
  "s_and_b32 s12, s12, -8\n\t" \
  "s_and_b32 s58, s58, 240\n\t" \
  "s_cmp_eq_u32 s58, 240\n\t" \
  "s_cbranch_scc1 P8AB%=\n\t" \
  "s_sub_u32 s56, s56, 1\n\t" \
  "s_cbranch_scc0 P8AL%=\n\t" \
  "s_branch P8AB%=\n\t" \
  "RAS%=:\n\t" \
  "s_mov_b32 s56, 0x10000\n\t" \
  "RAL%=:\n\t" \
  "global_load_dwordx2 v[142:143], v57, s[44:45] sc1\n\t" \
  "s_waitcnt vmcnt(0)\n\t" \
  "v_cmp_eq_u64 vcc, s[68:69], v[142:143]\n\t" \
  "s_nop 1\n\t" \
  "s_and_b32 s58, vcc_lo, 7\n\t" \
  "s_cmp_eq_u32 s58, 7\n\t" \
  "s_cbranch_scc1 RAB%=\n\t" \
  "s_sub_u32 s56, s56, 1\n\t" \
  "s_cbranch_scc0 RAL%=\n\t" \
  "s_branch RAB%=\n\t" \
  "END%=:\n\t"

#define BIGCLOB "v1", "v2", "v3", "v4", "v5", "v6", "v7", "v8", "v9", "v10", "v11", "v12", "v13", "v14", "v15", "v16", "v17", "v18", "v19", "v20", "v21", "v22", "v23", "v24", "v25", "v26", "v27", "v28", "v29", "v30", "v31", "v32", "v33", "v34", "v35", "v36", "v37", "v38", "v39", "v40", "v41", "v42", "v43", "v44", "v45", "v46", "v47", "v48", "v49", "v50", "v51", "v52", "v53", "v54", "v55", "v56", "v57", "v58", "v59", "v60", "v61", "v62", "v63", "v64", "v65", "v66", "v67", "v68", "v69", "v70", "v71", "v72", "v73", "v74", "v75", "v76", "v77", "v78", "v79", "v80", "v81", "v82", "v83", "v84", "v85", "v86", "v87", "v88", "v89", "v90", "v91", "v92", "v93", "v94", "v95", "v96", "v97", "v98", "v99", "v100", "v101", "v102", "v103", "v104", "v105", "v106", "v107", "v108", "v109", "v110", "v111", "v112", "v113", "v114", "v115", "v116", "v117", "v118", "v119", "v120", "v121", "v122", "v123", "v124", "v125", "v126", "v127", "v128", "v129", "v130", "v131", "v132", "v133", "v134", "v135", "v136", "v137", "v138", "v139", "v140", "v141", "v142", "v143", "v144", "v145", "v146", "v147", "v148", "v149", "v150", "v151", "v152", "v153", "v154", "v155", "v156", "v157", "v158", "v159", "v160", "v161", "v162", "v163", "v164", "v165", "v166", "v167", "v168", "v169", "v170", "v171", "v172", "v173", "v174", "v175", "v176", "v177", "v178", "v179", "v180", "v181", "v182", "v183", "v184", "v185", "v186", "v187", "v188", "v189", "v190", "v191", "v192", "v193", "v194", "v195", "v196", "v197", "v198", "v199", "v200", "v201", "v202", "v203", "v204", "v205", "v206", "v207", "v208", "v209", "v210", "v211", "v212", "v213", "v214", "v215", "v216", "v217", "v218", "v219", "v220", "v221", "v222", "v223", "v224", "v225", "v226", "v227", "v228", "v229", "v230", "v231", "v232", "v233", "v234", "v235", "v236", "v237", "v238", "v239", "v240", "v241", "v242", "v243", "v244", "v245", "v246", "v247", "v248", "v249", "v250", "v251", "v252", "v253", "v254", "v255", "a0", "a1", "a2", "a3", "a4", "a5", "a6", "a7", "a8", "a9", "a10", "a11", "a12", "a13", "a14", "a15", "a16", "a17", "a18", "a19", "a20", "a21", "a22", "a23", "a24", "a25", "a26", "a27", "a28", "a29", "a30", "a31", "a32", "a33", "a34", "a35", "a36", "a37", "a38", "a39", "a40", "a41", "a42", "a43", "a44", "a45", "a46", "a47", "a48", "a49", "a50", "a51", "a52", "a53", "a54", "a55", "a56", "a57", "a58", "a59", "a60", "a61", "a62", "a63", "a64", "a65", "a66", "a67", "a68", "a69", "a70", "a71", "a72", "a73", "a74", "a75", "a76", "a77", "a78", "a79", "a80", "a81", "a82", "a83", "a84", "a85", "a86", "a87", "a88", "a89", "a90", "a91", "a92", "a93", "a94", "a95", "a96", "a97", "a98", "a99", "a100", "a101", "a102", "a103", "a104", "a105", "a106", "a107", "a108", "a109", "a110", "a111", "a112", "a113", "a114", "a115", "a116", "a117", "a118", "a119", "a120", "a121", "a122", "a123", "a124", "a125", "a126", "a127", "a128", "a129", "a130", "a131", "a132", "a133", "a134", "a135", "a136", "a137", "a138", "a139", "a140", "a141", "a142", "a143", "a144", "a145", "a146", "a147", "a148", "a149", "a150", "a151", "a152", "a153", "a154", "a155", "a156", "a157", "a158", "a159", "a160", "a161", "a162", "a163", "a164", "a165", "a166", "a167", "a168", "a169", "a170", "a171", "a172", "a173", "a174", "a175", "a176", "a177", "a178", "a179", "a180", "a181", "a182", "a183", "a184", "a185", "a186", "a187", "a188", "a189", "a190", "a191", "a192", "a193", "a194", "a195", "a196", "a197", "a198", "a199", "a200", "a201", "a202", "a203", "a204", "a205", "a206", "a207", "a208", "a209", "a210", "a211", "a212", "a213", "a214", "a215", "a216", "a217", "a218", "a219", "a220", "a221", "a222", "a223", "a224", "a225", "a226", "a227", "a228", "a229", "a230", "a231", "a232", "a233", "a234", "a235", "a236", "a237", "a238", "a239", "a240", "a241", "a242", "a243", "a244", "a245", "a246", "a247", "a248", "a249", "a250", "a251", "a252", "a253", "a254", "a255", "s4", "s5", "s6", "s7", "s8", "s9", "s10", "s11", "s12", "s13", "s14", "s15", "s16", "s17", "s18", "s19", "s20", "s21", "s22", "s23", "s24", "s25", "s26", "s27", "s28", "s29", "s30", "s31", "s33", "s34", "s35", "s36", "s37", "s38", "s39", "s40", "s41", "s42", "s43", "s44", "s45", "s46", "s47", "s48", "s49", "s50", "s51", "s52", "s53", "s54", "s55", "s56", "s57", "s58", "s59", "s60", "s61", "s62", "s63", "s64", "s65", "s66", "s67", "s68", "s69", "s70", "s71", "s72", "s73", "s74", "s75", "s76", "s77", "s78", "s79", "s80", "s81", "s82", "s83", "s85", "s86", "s87", "s88", "s89", "s90", "s91", "s92", "s93", "s94", "s95", "s96", "s97", "s98", "s99"

__global__ __launch_bounds__(256, 1) void fused_kernel(const char* x, const char* wqkv, const char* wo,
                                                        const char* w1, const char* w2, char* out, char* ws) {
  asm volatile(BIGASM
               :
               : "s"(__builtin_amdgcn_kernarg_segment_ptr()), "{v0}"(threadIdx.x), "{s84}"(blockIdx.x)
               : "memory", "scc", "vcc", BIGCLOB);
}

extern "C" void kernel_launch(void* const* d_in, const int* in_sizes, int n_in, void* d_out, int out_size, void* d_ws, size_t ws_size,
                              hipStream_t stream) {
  (void)in_sizes; (void)n_in; (void)out_size; (void)ws_size;
  (void)hipFuncSetAttribute((const void*)fused_kernel, hipFuncAttributeMaxDynamicSharedMemorySize, LDS_BYTES);
  fused_kernel<<<256, 256, LDS_BYTES, stream>>>((const char*)d_in[0], (const char*)d_in[1], (const char*)d_in[2], (const char*)d_in[4],
                                                (const char*)d_in[5], (char*)d_out, (char*)d_ws + WSOFF);
}
```
